```python
import torch

def _softmax(x, axis=-1):
    return torch.softmax(x, dim=axis)
def _gelu(x):
    return torch.nn.functional.gelu(x, approximate="tanh")
def _silu(x):
    return torch.nn.functional.silu(x)
def _sigmoid(x):
    return torch.sigmoid(x)
def _layernorm(x, axis=-1, eps=1e-5):
    m = x.mean(dim=axis, keepdim=True)
    v = x.var(dim=axis, keepdim=True, unbiased=False)
    return (x - m) / torch.sqrt(v + eps)
def _scatter_add(tgt, idx, src, axis=0):
    return tgt.clone().index_add_(axis, idx, src)
def _segment_sum(src, seg, n):
    out = torch.zeros((n,) + src.shape[1:], dtype=src.dtype)
    return out.index_add_(0, seg, src)
def _pad(x, pw):
    flat = tuple(v for pair in reversed(pw) for v in pair)
    return torch.nn.functional.pad(x, flat)

kernel_name = 'recurrent_scan__softmax_full_mha__glu_gated_glu__prompt'

def setup_inputs():

    _w = (lambda a: (lambda t: t.to(torch.bfloat16) if t.is_floating_point() else t)(torch.as_tensor(a)))
    def _A_setup():
        B, S, D = 1, 2048, 576
        return {
            "x": _w((lambda *s: torch.randn(*s, dtype=torch.bfloat16) * 0.05)(B, S, D)),
            "wqkv": _w((lambda *s: torch.randn(*s, dtype=torch.bfloat16) * 0.05)(D, 3 * D)),
            "wo": _w((lambda *s: torch.randn(*s, dtype=torch.bfloat16) * 0.05)(D, D)),
        }
    _A_in = _A_setup()

    def _B_setup():
        B, S, D, F = 1, 2048, 576, 4608
        return {
            "x": _w((lambda *s: torch.randn(*s, dtype=torch.bfloat16) * 0.05)(B, S, D)),
            "w_gate": _w((lambda *s: torch.randn(*s, dtype=torch.bfloat16) * 0.05)(D, F)),
            "w_up": _w((lambda *s: torch.randn(*s, dtype=torch.bfloat16) * 0.05)(D, F)),
            "w_down": _w((lambda *s: torch.randn(*s, dtype=torch.bfloat16) * 0.05)(F, D)),
        }
    _B_in = _B_setup()

    return {
        "A_x": _A_in["x"],
        "A_wqkv": _A_in["wqkv"],
        "A_wo": _A_in["wo"],
        "B_x": _B_in["x"],
        "B_w_gate": _B_in["w_gate"],
        "B_w_up": _B_in["w_up"],
        "B_w_down": _B_in["w_down"],
    }

def reference(A_x, A_wqkv, A_wo, B_x, B_w_gate, B_w_up, B_w_down):
    def _A_ref(x, wqkv, wo):
        B, S, D = x.shape
        H = 32; Dh = D // H
        qkv = (lambda x, *d: x.permute(*d))((x @ wqkv).reshape(B, S, 3, H, Dh), 2, 0, 3, 1, 4)
        q, k, v = qkv[0], qkv[1], qkv[2]
        scale = Dh ** -0.5
        scores = torch.einsum("bhqd,bhkd->bhqk", q, k) * scale
        p = _softmax(scores, axis=-1)
        o = torch.einsum("bhqk,bhkd->bhqd", p, v)
        return (lambda x, *d: x.permute(*d))(o, 0, 2, 1, 3).reshape(B, S, D) @ wo

    def _B_ref(x, w_gate, w_up, w_down):
        g = torch.einsum("bsd,df->bsf", x, w_gate)
        u = torch.einsum("bsd,df->bsf", x, w_up)
        h = _sigmoid(g) * u
        return torch.einsum("bsf,fd->bsd", h, w_down)

    x = A_x
    xn = _layernorm(x, axis=-1)
    s = _A_ref(xn, A_wqkv, A_wo)
    g = _silu(_B_ref(xn, B_w_gate, B_w_up, B_w_down))
    return x + s * g
```

```cpp
#include <hip/hip_runtime.h>
#include <hip/hip_bf16.h>
#include <stdint.h>

typedef __attribute__((ext_vector_type(8))) short bf16x8;
typedef __attribute__((ext_vector_type(4))) short bf16x4;
typedef __attribute__((ext_vector_type(2))) __bf16 bf16p;
typedef __attribute__((ext_vector_type(2))) float f32x2;
typedef __attribute__((ext_vector_type(4))) float f32x4;
typedef __attribute__((ext_vector_type(16))) float f32x16;
typedef __attribute__((ext_vector_type(2))) unsigned u32x2;
typedef __attribute__((ext_vector_type(4))) unsigned u32x4;
typedef __attribute__((address_space(3))) const char* lds_cptr;
typedef __attribute__((address_space(3))) char* lds_ptr;
typedef short v4i16_t __attribute__((ext_vector_type(4)));

#define DI __device__ __forceinline__
#define MFMA16(a, b, c) __builtin_amdgcn_mfma_f32_32x32x16_bf16((a), (b), (c), 0, 0, 0)
#define MFMA8(a, b, c) __builtin_amdgcn_mfma_f32_32x32x8bf16_1k((a), (b), (c), 0, 0, 0)
#define SBAR() __builtin_amdgcn_sched_barrier(0)

constexpr int S = 2048, D = 576, NH = 32, DH = 18, F = 4608;
constexpr int NTB = S / 32;
constexpr int KS = D / 16;
constexpr int FS = F / 16;

constexpr size_t WS_XNF = 0;
constexpr size_t WS_WQP = WS_XNF + (size_t)NTB * KS * 1024;
constexpr size_t WS_QF1 = WS_WQP + (size_t)NH * KS * 2048;
constexpr size_t WS_QF2 = WS_QF1 + (size_t)NH * NTB * 1024;
constexpr int KVREC = 2560;
constexpr size_t WS_KV = WS_QF2 + (size_t)NH * S * 4;
constexpr size_t WS_KMX = WS_KV + (size_t)NH * NTB * KVREC;
constexpr size_t WS_OF = WS_KMX + (size_t)NH * NTB * 4;
constexpr size_t WS_HF = WS_OF + (size_t)NTB * KS * 1024;
constexpr size_t WS_PART = WS_HF + (size_t)NTB * FS * 1024;
constexpr size_t WS_END = WS_PART + (size_t)4 * NTB * 18 * 4096;

constexpr float LOG2E = 1.4426950408889634f;
constexpr float QSCALE = 0.23570226039551584f * LOG2E;

DI unsigned cvtpk(float lo, float hi) { return __builtin_bit_cast(unsigned, __builtin_convertvector((f32x2){lo, hi}, bf16p)); }
DI float bf_lo(unsigned u) { return __uint_as_float(u << 16); }
DI float bf_hi(unsigned u) { return __uint_as_float(u & 0xffff0000u); }
DI float rbf(float x) { return bf_lo(cvtpk(x, x)); }
DI bf16x4 trread(lds_cptr p) { return __builtin_bit_cast(bf16x4, __builtin_amdgcn_ds_read_tr16_b64_v4i16((__attribute__((address_space(3))) v4i16_t*)p)); }
DI bf16x8 trfrag(lds_cptr p0, lds_cptr p1) { const bf16x4 a = trread(p0), b = trread(p1); return (bf16x8){a[0], a[1], a[2], a[3], b[0], b[1], b[2], b[3]}; }
DI float swap32(float x) { auto r = __builtin_amdgcn_permlane32_swap(__float_as_uint(x), __float_as_uint(x), false, false); return __uint_as_float((threadIdx.x & 32) ? r[0] : r[1]); }

DI void glds16(const void* sbase, unsigned voff, unsigned lds_base) {
    unsigned sv;
    asm volatile("s_mov_b32 %0, m0\n\ts_mov_b32 m0, %3\n\ts_nop 0\n\tglobal_load_lds_dwordx4 %1, %2\n\ts_mov_b32 m0, %0"
                 : "=&s"(sv) : "v"(voff), "s"(sbase), "s"(lds_base) : "memory");
}
DI void gstore16(void* sbase, unsigned voff, u32x4 v) { asm volatile("global_store_dwordx4 %0, %1, %2\n\ts_nop 1" :: "v"(voff), "v"(v), "s"(sbase) : "memory"); }
DI void gstore8(void* sbase, unsigned voff, u32x2 v) { asm volatile("global_store_dwordx2 %0, %1, %2" :: "v"(voff), "v"(v), "s"(sbase) : "memory"); }
DI void gstore4(void* sbase, unsigned voff, unsigned v) { asm volatile("global_store_dword %0, %1, %2" :: "v"(voff), "v"(v), "s"(sbase) : "memory"); }
#define WAITV(n) asm volatile("s_waitcnt vmcnt(" #n ")" ::: "memory")
#define WAITV_BAR(n) asm volatile("s_waitcnt vmcnt(" #n ") lgkmcnt(0)\n\ts_barrier" ::: "memory")

__global__ __launch_bounds__(256) void k_prep(const unsigned short* __restrict__ x, const unsigned short* __restrict__ wqkv, char* __restrict__ ws) {
    const int tid = threadIdx.x;
    if (blockIdx.x < 256) {
        const int row = blockIdx.x * 8 + (tid >> 5), t = tid & 31;
        const u32x4* xr = (const u32x4*)(x + (size_t)row * D);
        u32x4 v[3];
        v[0] = xr[t]; v[1] = xr[t + 32]; v[2] = (t < 8) ? xr[t + 64] : (u32x4){0u, 0u, 0u, 0u};
        float sum = 0.f;
#pragma unroll
        for (int i = 0; i < 3; ++i)
#pragma unroll
            for (int j = 0; j < 4; ++j) sum += bf_lo(v[i][j]) + bf_hi(v[i][j]);
#pragma unroll
        for (int o = 16; o > 0; o >>= 1) sum += __shfl_xor(sum, o);
        const float mean = sum * (1.0f / D);
        float sq = 0.f;
#pragma unroll
        for (int i = 0; i < 3; ++i)
            if (i < 2 || t < 8)
#pragma unroll
                for (int j = 0; j < 4; ++j) { const float a = bf_lo(v[i][j]) - mean, b = bf_hi(v[i][j]) - mean; sq += a * a + b * b; }
#pragma unroll
        for (int o = 16; o > 0; o >>= 1) sq += __shfl_xor(sq, o);
        const float mb = rbf(mean);
        const float den = rbf(__fsqrt_rn(rbf(rbf(sq * (1.0f / D)) + 1e-5f)));
        char* dst = ws + WS_XNF + (size_t)(row >> 5) * KS * 1024 + (row & 31) * 16;
#pragma unroll
        for (int i = 0; i < 3; ++i) {
            if (i < 2 || t < 8) {
                const int p = t + 32 * i;
                u32x4 o;
#pragma unroll
                for (int j = 0; j < 4; ++j) o[j] = cvtpk(__fdiv_rn(rbf(bf_lo(v[i][j]) - mb), den), __fdiv_rn(rbf(bf_hi(v[i][j]) - mb), den));
                *(u32x4*)(dst + (p >> 1) * 1024 + (p & 1) * 512) = o;
            }
        }
    } else {
        const int base = (blockIdx.x - 256) * 1024 + tid;
#pragma unroll
        for (int i = 0; i < 4; ++i) {
            const int n = base + 256 * i;
            const int pc = n & 3, r = (n >> 2) & 15, sel = (n >> 6) & 1, hs = n >> 7, s = hs % KS, h = hs / KS;
            const int c = pc ^ (2 * sel), t = c >> 1, g = c & 1;
            const unsigned* w = (const unsigned*)(wqkv + (size_t)(16 * s + r) * (3 * D) + DH * h);
            u32x4 o = {0u, 0u, 0u, 0u};
            if (t == 0 || g == 0) {
                const int col = (t == 0 ? g * D : 2 * D) + 8 * sel;
                o = (u32x4){w[col / 2], w[col / 2 + 1], w[col / 2 + 2], w[col / 2 + 3]};
            } else if (sel == 0) {
                o = (u32x4){w[8], w[D / 2 + 8], w[D + 8], 0u};
            }
            *(u32x4*)(ws + WS_WQP + (size_t)n * 16) = o;
        }
    }
}

constexpr int G1_BUF = KS * 2048;

DI int g1_aoff(int lane) {
    const int hi = lane >> 5, g16 = (lane >> 4) & 1, q = (lane & 15) >> 2, p = lane & 3;
    return (p >> 1) * 1024 + (8 * hi + q) * 64 + ((g16 ^ (2 * (p >> 1))) * 16) + 8 * (p & 1);
}
DI void g1_dma_ffn(const char* wsel, int f0, unsigned voff, unsigned ldsb, int w) {
    const char* src = wsel + (size_t)(16 * (w >> 1)) * (F * 2) + f0 * 2;
    const unsigned dst = ldsb + (w >> 1) * 2048 + (w & 1) * 1024;
#pragma unroll
    for (int i = 0; i < 9; ++i) glds16(src + (size_t)i * 64 * (F * 2), voff, dst + i * 8192);
}
template <int NT, bool FIRST>
DI void g1_mfma(lds_cptr a0, lds_cptr a1, bf16x8 (&xb)[KS], f32x16& c0, f32x16& c1) {
    constexpr int N = KS * NT, AHEAD = 3;
    bf16x8 fr[4];
    auto rd = [&](int i) { const lds_cptr a = ((NT == 2 && (i & 1)) ? a1 : a0) + (i / NT) * 2048; return trfrag(a, a + 256); };
#pragma unroll
    for (int i = 0; i < AHEAD; ++i) fr[i] = rd(i);
    c0 = (f32x16){}; c1 = (f32x16){};
#pragma unroll
    for (int i = 0; i < N; ++i) {
        if (i + AHEAD < N) fr[(i + AHEAD) & 3] = rd(i + AHEAD);
        if (FIRST && (i % NT) == 0) asm volatile("s_waitcnt vmcnt(%1)" : "+v"(xb[i / NT]) : "n"(KS - 1 - i / NT + 9));
        if (NT == 2 && (i & 1)) c1 = MFMA16(fr[i & 3], xb[i / NT], c1);
        else c0 = MFMA16(fr[i & 3], xb[i / NT], c0);
        SBAR();
    }
}
DI void g1_epi_ffn(const f32x16& c, char* hdst, unsigned voff) {
    float h[8];
#pragma unroll
    for (int j = 0; j < 8; ++j) {
        const int gi = (j >> 2) * 8 + (j & 3);
        const float e = __builtin_amdgcn_exp2f(c[gi] * -LOG2E);
        h[j] = c[gi + 4] * __builtin_amdgcn_rcpf(1.0f + e);
    }
    gstore16(hdst, voff, (u32x4){cvtpk(h[0], h[1]), cvtpk(h[2], h[3]), cvtpk(h[4], h[5]), cvtpk(h[6], h[7])});
}
DI void g1_epi_qkv(const f32x16& a, const f32x16& b, char* ws, int h, int tb, int lane) {
    asm volatile("" : "+v"(lane));
    const int r = lane & 31, hi = lane >> 5;
    char* qf1 = ws + WS_QF1 + ((size_t)h * NTB + tb) * 1024;
    char* rec = ws + WS_KV + ((size_t)h * NTB + tb) * KVREC;
    gstore16(qf1, lane * 16, (u32x4){cvtpk(a[0] * QSCALE, a[1] * QSCALE), cvtpk(a[2] * QSCALE, a[3] * QSCALE), cvtpk(a[4] * QSCALE, a[5] * QSCALE), cvtpk(a[6] * QSCALE, a[7] * QSCALE)});
    const u32x4 kp = {cvtpk(a[8], a[9]), cvtpk(a[10], a[11]), cvtpk(a[12], a[13]), cvtpk(a[14], a[15])};
    gstore16(rec, lane * 16, kp);
    gstore8(rec, 1280 + r * 40 + hi * 8, (u32x2){cvtpk(b[0], b[1]), cvtpk(b[2], b[3])});
    gstore8(rec, 1280 + r * 40 + 16 + hi * 8, (u32x2){cvtpk(b[4], b[5]), cvtpk(b[6], b[7])});
    const unsigned p89 = cvtpk(b[8], b[9]), p1011 = cvtpk(b[10], b[11]);
    gstore8(rec, hi ? 1280 + r * 40 + 32 : 1024 + r * 8, hi ? (u32x2){p89, 0x00003f80u} : (u32x2){p1011, 0u});
    float n2 = hi ? 0.f : bf_lo(p1011) * bf_lo(p1011) + bf_hi(p1011) * bf_hi(p1011);
#pragma unroll
    for (int j = 0; j < 4; ++j) n2 += bf_lo(kp[j]) * bf_lo(kp[j]) + bf_hi(kp[j]) * bf_hi(kp[j]);
    n2 += swap32(n2);
#pragma unroll
    for (int o = 16; o > 0; o >>= 1) n2 = fmaxf(n2, __shfl_xor(n2, o));
    char* p = hi ? ws + WS_KMX + ((size_t)h * NTB + tb) * 4 : ws + WS_QF2 + ((size_t)h * S + tb * 32 + r) * 4;
    const unsigned val = hi ? __float_as_uint(n2) : cvtpk(b[8] * QSCALE, b[9] * QSCALE);
    asm volatile("global_store_dword %0, %1, off" :: "v"(p), "v"(val) : "memory");
}

__global__ __launch_bounds__(512, 2) void k_gemm1(const char* wg, const char* wu, char* ws) {
    extern __shared__ __attribute__((aligned(16))) char lds[];
    const int tid = threadIdx.x, lane = tid & 63;
    const int w = __builtin_amdgcn_readfirstlane(tid >> 6);
    const int xcd = blockIdx.x & 7, idx = blockIdx.x >> 3;
    const int cg = 4 * xcd + (idx & 3), rg = idx >> 2, tb = 8 * rg + w;
    const unsigned lds0 = (unsigned)(uintptr_t)lds;
    const bool late = w >= 4;

    {
        const char* src = ws + WS_WQP + (size_t)cg * G1_BUF + w * 9 * 1024;
#pragma unroll
        for (int i = 0; i < 9; ++i) glds16(src + i * 1024, lane * 16, lds0 + (w * 9 + i) * 1024);
    }
    bf16x8 xb[KS];
    {
        const char* xs = ws + WS_XNF + (size_t)tb * KS * 1024;
        const unsigned lo = lane * 16;
#pragma unroll
        for (int s = 0; s < KS; ++s)
            asm volatile("global_load_dwordx4 %0, %1, %2 offset:%3" : "=v"(xb[s]) : "v"(lo), "s"(xs + (s >> 2) * 4096), "n"((s & 3) * 1024));
    }
    const char* wsel = (w & 1) ? wu : wg;
    const unsigned voff = (lane >> 2) * (F * 2) + (((lane & 3) ^ (2 * (w & 1))) * 16);
    const int fbase = 144 * cg;
    const int aoff0 = g1_aoff(lane);
    const lds_cptr A0 = (lds_cptr)lds + aoff0, A1 = (lds_cptr)lds + (aoff0 ^ 32);
    char* hdst = ws + WS_HF + ((size_t)tb * FS + 9 * cg) * 1024;
    const unsigned hv = lane * 16;
    f32x16 c0, c1;

    g1_dma_ffn(wsel, fbase, voff, lds0 + G1_BUF, w);
    asm volatile("s_waitcnt vmcnt(45)\n\ts_barrier" ::: "memory");
    g1_mfma<2, true>(A0, A1, xb, c0, c1);
    if (!late) { g1_epi_qkv(c0, c1, ws, cg, tb, lane); WAITV_BAR(6); } else { WAITV_BAR(0); }

#pragma unroll
    for (int n = 1; n <= 5; ++n) {
        const int buf = n & 1;
        if (n < 5) {
            const int fn = (n == 4) ? (cg == 31 ? fbase + 112 : fbase + 128) : fbase + 32 * n;
            g1_dma_ffn(wsel, fn, voff, lds0 + (buf ^ 1) * G1_BUF, w);
        }
        if (late) {
            if (n == 1) g1_epi_qkv(c0, c1, ws, cg, tb, lane);
            else { g1_epi_ffn(c0, hdst + (2 * n - 4) * 1024, hv); g1_epi_ffn(c1, hdst + (2 * n - 3) * 1024, hv); }
        }
        if (n < 5) {
            g1_mfma<2, false>(A0 + buf * G1_BUF, A1 + buf * G1_BUF, xb, c0, c1);
            if (!late) { g1_epi_ffn(c0, hdst + (2 * n - 2) * 1024, hv); g1_epi_ffn(c1, hdst + (2 * n - 1) * 1024, hv); WAITV_BAR(2); }
            else if (n == 1) { WAITV_BAR(6); } else { WAITV_BAR(2); }
        } else {
            int l2 = lane;
            asm volatile("" : "+v"(l2));
            const lds_cptr AL = (lds_cptr)lds + buf * G1_BUF + (g1_aoff(l2) ^ (cg == 31 ? 32 : 0));
            g1_mfma<1, false>(AL, AL, xb, c0, c1);
            g1_epi_ffn(c0, hdst + 8 * 1024, hv);
        }
    }
}

constexpr int AT_STAGE = 8 * KVREC;
constexpr float AT_BOUND_MAX = 50.0f;

struct KFrag { bf16x8 k1; bf16x4 k2; };
struct VFrag { bf16x8 v0, v1; };
DI KFrag at_readk(lds_cptr rec, int lane) {
    KFrag k;
    k.k1 = *(const __attribute__((address_space(3))) bf16x8*)(rec + lane * 16);
    k.k2 = *(const __attribute__((address_space(3))) bf16x4*)(rec + 1024 + (lane & 31) * 8);
    return k;
}
DI VFrag at_readv(lds_cptr recv) {
    VFrag v;
    v.v0 = trfrag(recv, recv + 8 * 40);
    v.v1 = trfrag(recv + 16 * 40, recv + 24 * 40);
    return v;
}
DI bf16x8 packp(const f32x16& p, int s) {
    return __builtin_bit_cast(bf16x8, (u32x4){cvtpk(p[8 * s], p[8 * s + 1]), cvtpk(p[8 * s + 2], p[8 * s + 3]), cvtpk(p[8 * s + 4], p[8 * s + 5]), cvtpk(p[8 * s + 6], p[8 * s + 7])});
}

DI void at_dma(const char* kvh, int stage, unsigned ldsb, int w, int lane) {
    const char* src = kvh + (size_t)stage * AT_STAGE + w * 5 * 1024;
#pragma unroll
    for (int i = 0; i < 5; ++i) glds16(src + i * 1024, lane * 16, ldsb + (w * 5 + i) * 1024);
}

#define AT_STEP(SX, PX0, PX1, SY, CY, QY1, QY2, KF, OY, PY0, PY1, VF, LOADS)                                   \
    do {                                                                                                       \
        u32x4 n0_, n1_;                                                                                        \
        SY = MFMA16(KF.k1, QY1, CY);                                                                           \
        LOADS;                                                                                                 \
        _Pragma("unroll") for (int i_ = 0; i_ < 4; ++i_) SX[i_] = __builtin_amdgcn_exp2f(SX[i_]);              \
        n0_[0] = cvtpk(SX[0], SX[1]); n0_[1] = cvtpk(SX[2], SX[3]);                                            \
        asm volatile("" : "+v"(n0_[0]), "+v"(n0_[1]));                                                         \
        SBAR();                                                                                                \
        SY = MFMA8(KF.k2, QY2, SY);                                                                            \
        _Pragma("unroll") for (int i_ = 4; i_ < 8; ++i_) SX[i_] = __builtin_amdgcn_exp2f(SX[i_]);              \
        n0_[2] = cvtpk(SX[4], SX[5]); n0_[3] = cvtpk(SX[6], SX[7]);                                            \
        asm volatile("" : "+v"(n0_[2]), "+v"(n0_[3]));                                                         \
        SBAR();                                                                                                \
        OY = MFMA16(VF.v0, PY0, OY);                                                                           \
        _Pragma("unroll") for (int i_ = 8; i_ < 12; ++i_) SX[i_] = __builtin_amdgcn_exp2f(SX[i_]);             \
        n1_[0] = cvtpk(SX[8], SX[9]); n1_[1] = cvtpk(SX[10], SX[11]);                                          \
        asm volatile("" : "+v"(n1_[0]), "+v"(n1_[1]));                                                         \
        SBAR();                                                                                                \
        OY = MFMA16(VF.v1, PY1, OY);                                                                           \
        _Pragma("unroll") for (int i_ = 12; i_ < 16; ++i_) SX[i_] = __builtin_amdgcn_exp2f(SX[i_]);            \
        n1_[2] = cvtpk(SX[12], SX[13]); n1_[3] = cvtpk(SX[14], SX[15]);                                        \
        asm volatile("" : "+v"(n1_[2]), "+v"(n1_[3]));                                                         \
        SBAR();                                                                                                \
        PX0 = __builtin_bit_cast(bf16x8, n0_); PX1 = __builtin_bit_cast(bf16x8, n1_);                          \
    } while (0)

DI void at_online(const KFrag& k, const VFrag& v, bf16x8 q1, bf16x4 q2, float& m, f32x16& o) {
    f32x16 z = {};
    f32x16 s = MFMA16(k.k1, q1, z);
    s = MFMA8(k.k2, q2, s);
    float t = s[0];
#pragma unroll
    for (int i = 1; i < 16; ++i) t = fmaxf(t, s[i]);
    t = fmaxf(t, swap32(t));
    const float mn = fmaxf(m, t);
    const float alpha = __builtin_amdgcn_exp2f(m - mn);
    m = mn;
#pragma unroll
    for (int i = 0; i < 16; ++i) { s[i] = __builtin_amdgcn_exp2f(s[i] - mn); o[i] *= alpha; }
    o = MFMA16(v.v0, packp(s, 0), o);
    o = MFMA16(v.v1, packp(s, 1), o);
}

DI void at_store(const f32x16& o, char* ws, int h, int tb, int lane) {
    const float inv = 1.0f / o[10];
    char* of = ws + WS_OF + (size_t)tb * KS * 1024;
    *(u32x4*)(of + h * 1024 + lane * 16) = (u32x4){cvtpk(o[0] * inv, o[1] * inv), cvtpk(o[2] * inv, o[3] * inv), cvtpk(o[4] * inv, o[5] * inv), cvtpk(o[6] * inv, o[7] * inv)};
    if (lane < 32) *(unsigned*)(of + (32 + (h >> 3)) * 1024 + (lane + 32 * ((h >> 2) & 1)) * 16 + (h & 3) * 4) = cvtpk(o[8] * inv, o[9] * inv);
}

DI float at_qnorm2(bf16x8 q1, bf16x4 q2) {
    const u32x4 a = __builtin_bit_cast(u32x4, q1); const u32x2 b = __builtin_bit_cast(u32x2, q2);
    float n = bf_lo(b[0]) * bf_lo(b[0]) + bf_hi(b[0]) * bf_hi(b[0]);
#pragma unroll
    for (int j = 0; j < 4; ++j) n += bf_lo(a[j]) * bf_lo(a[j]) + bf_hi(a[j]) * bf_hi(a[j]);
    return n + swap32(n);
}

DI void attn_block(char* ws, char* lds, int bid) {
    const int tid = threadIdx.x, lane = tid & 63, hi = lane >> 5;
    const int w = __builtin_amdgcn_readfirstlane(tid >> 6);
    const int xcd = bid & 7, idx = bid >> 3;
    const int h = 4 * xcd + (idx & 3), qg = idx >> 2;
    const int tbA = 8 * qg + 2 * w, tbB = tbA + 1;
    const unsigned lds0 = (unsigned)(uintptr_t)lds;
    const char* kvh = ws + WS_KV + (size_t)h * NTB * KVREC;

    at_dma(kvh, 0, lds0, w, lane);
    at_dma(kvh, 1, lds0 + AT_STAGE, w, lane);

    const bf16x8 qA1 = *(const bf16x8*)(ws + WS_QF1 + ((size_t)h * NTB + tbA) * 1024 + lane * 16);
    const bf16x8 qB1 = *(const bf16x8*)(ws + WS_QF1 + ((size_t)h * NTB + tbB) * 1024 + lane * 16);
    const unsigned qa2 = *(const unsigned*)(ws + WS_QF2 + ((size_t)h * S + tbA * 32 + (lane & 31)) * 4);
    const unsigned qb2 = *(const unsigned*)(ws + WS_QF2 + ((size_t)h * S + tbB * 32 + (lane & 31)) * 4);
    float km = *(const float*)(ws + WS_KMX + ((size_t)h * NTB + lane) * 4);
    const bf16x4 qA2 = __builtin_bit_cast(bf16x4, (u32x2){hi ? 0u : qa2, 0u});
    const bf16x4 qB2 = __builtin_bit_cast(bf16x4, (u32x2){hi ? 0u : qb2, 0u});
#pragma unroll
    for (int o = 32; o > 0; o >>= 1) km = fmaxf(km, __shfl_xor(km, o));
    const float mA = __builtin_sqrtf(at_qnorm2(qA1, qA2) * km) * 1.001f, mB = __builtin_sqrtf(at_qnorm2(qB1, qB2) * km) * 1.001f;
    const bool fast = __all(mA <= AT_BOUND_MAX && mB <= AT_BOUND_MAX);

    const int g16 = (lane >> 4) & 1, q = (lane & 15) >> 2, p = lane & 3;
    const int voffs = 1280 + (4 * hi + q) * 40 + (g16 ? 32 : p * 8);
    const lds_cptr L = (lds_cptr)lds;
    f32x16 oA = {}, oB = {};

    if (fast) {
        f32x16 cA, cB;
#pragma unroll
        for (int i = 0; i < 16; ++i) { cA[i] = -mA; cB[i] = -mB; }
        asm volatile("" : "+v"(cA), "+v"(cB));
        f32x16 sA, sB;
        bf16x8 pA0, pA1, pB0 = {}, pB1 = {};
        KFrag k0, k1; VFrag v0, v1;
        WAITV_BAR(5);
        k0 = at_readk(L, lane);
        v1 = at_readv(L + voffs);
        sA = MFMA16(k0.k1, qA1, cA); sA = MFMA8(k0.k2, qA2, sA);
        int slot = 0;
        for (int st = 0; st < 8; ++st) {
            const lds_cptr cur = L + slot * AT_STAGE;
            const int nslot = slot == 2 ? 0 : slot + 1;
#pragma unroll
            for (int t = 0; t < 8; t += 2) {
                AT_STEP(sA, pA0, pA1, sB, cB, qB1, qB2, k0, oB, pB0, pB1, v1,
                        (k1 = at_readk(cur + (t + 1) * KVREC, lane), v0 = at_readv(cur + t * KVREC + voffs)));
                AT_STEP(sB, pB0, pB1, sA, cA, qA1, qA2, k1, oA, pA0, pA1, v0, (void)0);
                lds_cptr nk = cur + (t + 2) * KVREC;
                if (t == 6) {
                    if (st < 7) {
                        if (st < 6) { WAITV_BAR(0); at_dma(kvh, st + 2, lds0 + (nslot == 2 ? 0 : nslot + 1) * AT_STAGE, w, lane); }
                        else WAITV_BAR(0);
                        nk = L + nslot * AT_STAGE;
                    } else nk = cur;
                }
                AT_STEP(sA, pA0, pA1, sB, cB, qB1, qB2, k1, oB, pB0, pB1, v0,
                        (k0 = at_readk(nk, lane), v1 = at_readv(cur + (t + 1) * KVREC + voffs)));
                AT_STEP(sB, pB0, pB1, sA, cA, qA1, qA2, k0, oA, pA0, pA1, v1, (void)0);
            }
            slot = nslot;
        }
        oB = MFMA16(v1.v0, pB0, oB);
        oB = MFMA16(v1.v1, pB1, oB);
    } else {
        float mxA = -INFINITY, mxB = -INFINITY;
        WAITV_BAR(5);
        int slot = 0;
        for (int st = 0; st < 8; ++st) {
            const lds_cptr cur = L + slot * AT_STAGE;
            const int nslot = slot == 2 ? 0 : slot + 1;
            for (int t = 0; t < 8; ++t) {
                const KFrag k = at_readk(cur + t * KVREC, lane);
                const VFrag v = at_readv(cur + t * KVREC + voffs);
                at_online(k, v, qA1, qA2, mxA, oA);
                at_online(k, v, qB1, qB2, mxB, oB);
            }
            if (st < 7) {
                WAITV_BAR(0);
                if (st < 6) at_dma(kvh, st + 2, lds0 + (nslot == 2 ? 0 : nslot + 1) * AT_STAGE, w, lane);
            }
            slot = nslot;
        }
    }
    at_store(oA, ws, h, tbA, lane);
    at_store(oB, ws, h, tbB, lane);
}

__global__ __launch_bounds__(256, 2) void k_attn(char* ws) {
    extern __shared__ __attribute__((aligned(16))) char lds[];
    attn_block(ws, lds, blockIdx.x);
}

constexpr int DN_STAGE = 2 * 2 * 9 * 1024;

DI void dn_dma(const char* wd, int fs0, int dh, unsigned ldsb, int w, unsigned voff) {
    const char* src = wd + (size_t)(fs0 + (w & 1)) * 16 * (D * 2) + dh * 576;
    const unsigned dst = ldsb + (w >> 1) * 18432 + (w & 1) * 9216;
#pragma unroll
    for (int i = 0; i < 9; ++i) glds16(src + i * 64, voff, dst + i * 1024);
}

DI void down_block(const char* wd, char* ws, char* lds, int bid) {
    const int tid = threadIdx.x, lane = tid & 63;
    const int w = __builtin_amdgcn_readfirstlane(tid >> 6);
    const int xcd = bid & 7, idx = bid >> 3;
    const int kq = xcd >> 1, tg = (xcd & 1) * 16 + (idx >> 1), dh = idx & 1;
    const int tb = 2 * tg + (w & 1), kh = w >> 1;
    const int fs0 = 72 * kq + 36 * kh;
    const unsigned lds0 = (unsigned)(uintptr_t)lds;
    const unsigned voff = (lane >> 2) * (D * 2) + (lane & 3) * 16;
    const int hi = lane >> 5, g16 = (lane >> 4) & 1, q = (lane & 15) >> 2, p = lane & 3;
    const lds_cptr A = (lds_cptr)lds + kh * 18432 + (4 * hi + q) * 64 + g16 * 32 + p * 8;
    const bf16x8* hs = (const bf16x8*)(ws + WS_HF + ((size_t)tb * FS + fs0) * 1024) + lane;

    f32x16 acc[9];
#pragma unroll
    for (int i = 0; i < 9; ++i) acc[i] = (f32x16){};
    dn_dma(wd, fs0, dh, lds0, w, voff);
    bf16x8 b0 = hs[0], b1 = hs[64];
    for (int st = 0; st < 18; ++st) {
        asm volatile("s_waitcnt vmcnt(0)\n\ts_barrier" ::: "memory");
        bf16x8 n0 = b0, n1 = b1;
        if (st < 17) {
            dn_dma(wd, fs0 + 2 * st + 2, dh, lds0 + ((st + 1) & 1) * DN_STAGE, w, voff);
            n0 = hs[(2 * st + 2) * 64]; n1 = hs[(2 * st + 3) * 64];
        }
        const lds_cptr a = A + (st & 1) * DN_STAGE;
#pragma unroll
        for (int i = 0; i < 9; ++i) acc[i] = MFMA16(trfrag(a + i * 1024, a + i * 1024 + 512), b0, acc[i]);
#pragma unroll
        for (int i = 0; i < 9; ++i) acc[i] = MFMA16(trfrag(a + 9216 + i * 1024, a + 9216 + i * 1024 + 512), b1, acc[i]);
        b0 = n0; b1 = n1;
        asm volatile("s_waitcnt lgkmcnt(0)" ::: "memory");
    }
    __syncthreads();
    f32x4* red = (f32x4*)(lds + (w & 1) * 36864) + lane;
    if (kh == 1) {
#pragma unroll
        for (int i = 0; i < 9; ++i)
#pragma unroll
            for (int r = 0; r < 4; ++r) red[(i * 4 + r) * 64] = (f32x4){acc[i][4 * r], acc[i][4 * r + 1], acc[i][4 * r + 2], acc[i][4 * r + 3]};
    }
    __syncthreads();
    if (kh == 0) {
        f32x4* dst = (f32x4*)(ws + WS_PART + (((size_t)kq * NTB + tb) * 18 + 9 * dh) * 4096) + lane;
#pragma unroll
        for (int i = 0; i < 9; ++i)
#pragma unroll
            for (int r = 0; r < 4; ++r) {
                const f32x4 o = red[(i * 4 + r) * 64];
                dst[(i * 4 + r) * 64] = (f32x4){acc[i][4 * r] + o[0], acc[i][4 * r + 1] + o[1], acc[i][4 * r + 2] + o[2], acc[i][4 * r + 3] + o[3]};
            }
    }
}

__global__ __launch_bounds__(256, 2) void k_down(const char* wd, char* ws) {
    extern __shared__ __attribute__((aligned(16))) char lds[];
    down_block(wd, ws, lds, blockIdx.x);
}

__global__ __launch_bounds__(256) void k_final(const char* wo, const unsigned short* x, char* ws, unsigned short* out) {
    __shared__ __attribute__((aligned(16))) char lds[KS * 1024];
    const int tid = threadIdx.x, lane = tid & 63;
    const int w = __builtin_amdgcn_readfirstlane(tid >> 6);
    const int dt = blockIdx.x % 18, tg = blockIdx.x / 18, tb = 4 * tg + w;
    const unsigned lds0 = (unsigned)(uintptr_t)lds;
    {
        const int rho = lane >> 2;
        const unsigned voff = rho * (D * 2) + (lane & 3) * 16;
#pragma unroll
        for (int i = 0; i < 8; ++i) { const int ks = w * 8 + i; glds16(wo + (size_t)(DH * ks) * (D * 2) + dt * 64, voff, lds0 + ks * 1024); }
        const int head = 8 * w + 4 * ((rho >> 2) & 1) + 2 * (rho >> 3) + ((rho & 3) >> 1);
        const unsigned voff2 = (DH * head + 16 + (rho & 1)) * (D * 2) + (lane & 3) * 16;
        glds16(wo + dt * 64, voff2, lds0 + (32 + w) * 1024);
    }
    const bf16x8* os = (const bf16x8*)(ws + WS_OF + (size_t)tb * KS * 1024) + lane;
    bf16x8 b[KS];
#pragma unroll
    for (int s = 0; s < KS; ++s) b[s] = os[s * 64];
    const int hi = lane >> 5, g16 = (lane >> 4) & 1, q = (lane & 15) >> 2, p = lane & 3;
    f32x4 part[4];
#pragma unroll
    for (int r = 0; r < 4; ++r) {
        const f32x4* pp = (const f32x4*)(ws + WS_PART + ((size_t)tb * 18 + dt) * 4096) + r * 64 + lane;
        constexpr size_t QS = (size_t)NTB * 18 * 4096 / 16;
        part[r] = (pp[0] + pp[QS]) + (pp[2 * QS] + pp[3 * QS]);
    }
    const size_t rowoff = (size_t)(tb * 32 + (lane & 31)) * D + dt * 32 + 4 * hi;
    u32x2 xv[4];
#pragma unroll
    for (int r = 0; r < 4; ++r) xv[r] = *(const u32x2*)(x + rowoff + 8 * r);
    asm volatile("s_waitcnt vmcnt(0)\n\ts_barrier" ::: "memory");
    const lds_cptr A = (lds_cptr)lds + (4 * hi + q) * 64 + g16 * 32 + p * 8;
    f32x16 acc = {};
#pragma unroll
    for (int s = 0; s < KS; ++s) acc = MFMA16(trfrag(A + s * 1024, A + s * 1024 + 512), b[s], acc);
#pragma unroll
    for (int r = 0; r < 4; ++r) {
        float o[4];
#pragma unroll
        for (int c = 0; c < 4; ++c) {
            const float f = rbf(part[r][c]);
            const float g = rbf(f * __builtin_amdgcn_rcpf(1.0f + __builtin_amdgcn_exp2f(f * -LOG2E)));
            const float xx = (c & 1) ? bf_hi(xv[r][c >> 1]) : bf_lo(xv[r][c >> 1]);
            o[c] = xx + rbf(rbf(acc[4 * r + c]) * g);
        }
        *(u32x2*)(out + rowoff + 8 * r) = (u32x2){cvtpk(o[0], o[1]), cvtpk(o[2], o[3])};
    }
}

extern "C" void kernel_launch(void* const* d_in, const int* in_sizes, int n_in, void* d_out, int out_size, void* d_ws, size_t ws_size, hipStream_t stream) {
    const unsigned short* x = (const unsigned short*)d_in[0];
    const unsigned short* wqkv = (const unsigned short*)d_in[1];
    const char* wo = (const char*)d_in[2];
    const char* wg = (const char*)d_in[4];
    const char* wu = (const char*)d_in[5];
    const char* wd = (const char*)d_in[6];
    char* ws = (char*)d_ws;
    (void)hipFuncSetAttribute((const void*)k_gemm1, hipFuncAttributeMaxDynamicSharedMemorySize, 2 * G1_BUF);
    (void)hipFuncSetAttribute((const void*)k_attn, hipFuncAttributeMaxDynamicSharedMemorySize, 3 * AT_STAGE);
    (void)hipFuncSetAttribute((const void*)k_down, hipFuncAttributeMaxDynamicSharedMemorySize, 2 * DN_STAGE);
    k_prep<<<400, 256, 0, stream>>>(x, wqkv, ws);
    k_gemm1<<<256, 512, 2 * G1_BUF, stream>>>(wg, wu, ws);
    k_attn<<<256, 256, 3 * AT_STAGE, stream>>>(ws);
    k_down<<<256, 256, 2 * DN_STAGE, stream>>>(wd, ws);
    k_final<<<288, 256, 0, stream>>>(wo, x, ws, (unsigned short*)d_out);
}
```

```cpp
#include <hip/hip_runtime.h>
#include <hip/hip_bf16.h>
#include <stdint.h>
#include <type_traits>

typedef __attribute__((ext_vector_type(8))) short bf16x8;
typedef __attribute__((ext_vector_type(4))) short bf16x4;
typedef __attribute__((ext_vector_type(2))) __bf16 bf16p;
typedef __attribute__((ext_vector_type(2))) float f32x2;
typedef __attribute__((ext_vector_type(4))) float f32x4;
typedef __attribute__((ext_vector_type(16))) float f32x16;
typedef __attribute__((ext_vector_type(2))) unsigned u32x2;
typedef __attribute__((ext_vector_type(4))) unsigned u32x4;
typedef __attribute__((address_space(3))) const char* lds_cptr;
typedef __attribute__((address_space(3))) char* lds_ptr;
typedef short v4i16_t __attribute__((ext_vector_type(4)));

#define DI __device__ __forceinline__
#define MFMA16(a, b, c) __builtin_amdgcn_mfma_f32_32x32x16_bf16((a), (b), (c), 0, 0, 0)
#define MFMA32K(a, b, c) __builtin_amdgcn_mfma_f32_16x16x32_bf16((a), (b), (c), 0, 0, 0)
#define MFMA8(a, b, c) __builtin_amdgcn_mfma_f32_32x32x8bf16_1k((a), (b), (c), 0, 0, 0)
#define SBAR() __builtin_amdgcn_sched_barrier(0)

template <int I, int N, class Fn> DI void static_for(Fn&& fn) {
    if constexpr (I < N) { fn(std::integral_constant<int, I>{}); static_for<I + 1, N>(fn); }
}

constexpr int S = 2048, D = 576, NH = 32, DH = 18, F = 4608;
constexpr int NTB = S / 32;
constexpr int NT16 = S / 16;
constexpr int KS = D / 32;
constexpr int FS = F / 32;

constexpr int KVREC = 2560;
constexpr size_t WS_XNF = 0;
constexpr size_t WS_WQP = WS_XNF + (size_t)NT16 * KS * 1024;
constexpr size_t WS_QF1 = WS_WQP + (size_t)NH * KS * 4096;
constexpr size_t WS_QF2 = WS_QF1 + (size_t)NH * NTB * 1024;
constexpr size_t WS_KV = WS_QF2 + (size_t)NH * S * 4;
constexpr size_t WS_KMX = WS_KV + (size_t)NH * NTB * KVREC;
constexpr size_t WS_TRASH = WS_KMX + (size_t)NH * NTB * 4;
constexpr size_t WS_OF = WS_TRASH + 1024;
constexpr size_t WS_HF = WS_OF + (size_t)NT16 * KS * 1024;
constexpr size_t WS_PART = WS_HF + (size_t)NT16 * FS * 1024;
constexpr size_t WS_END = WS_PART + (size_t)4 * NT16 * 36 * 1024;

#define STAMP_BEGIN
#define STAMP_END(k)
#define MARKS
#define MARK(i)
#define MARK_DUMP(k, n)

constexpr float LOG2E = 1.4426950408889634f;
constexpr float QSCALE = 0.23570226039551584f * LOG2E;

DI unsigned cvtpk(float lo, float hi) { return __builtin_bit_cast(unsigned, __builtin_convertvector((f32x2){lo, hi}, bf16p)); }
DI float bf_lo(unsigned u) { return __uint_as_float(u << 16); }
DI float bf_hi(unsigned u) { return __uint_as_float(u & 0xffff0000u); }
DI float rbf(float x) { return bf_lo(cvtpk(x, x)); }
DI bf16x4 trread(lds_cptr p) { return __builtin_bit_cast(bf16x4, __builtin_amdgcn_ds_read_tr16_b64_v4i16((__attribute__((address_space(3))) v4i16_t*)p)); }
DI bf16x8 trfrag(lds_cptr p0, lds_cptr p1) { const bf16x4 a = trread(p0), b = trread(p1); return (bf16x8){a[0], a[1], a[2], a[3], b[0], b[1], b[2], b[3]}; }
DI float swap32(float x) { auto r = __builtin_amdgcn_permlane32_swap(__float_as_uint(x), __float_as_uint(x), false, false); return __uint_as_float((threadIdx.x & 32) ? r[0] : r[1]); }

DI void glds16(const void* sbase, unsigned voff, unsigned lds_base) {
    asm volatile("s_mov_b32 m0, %2\n\ts_nop 0\n\tglobal_load_lds_dwordx4 %0, %1" :: "v"(voff), "s"(sbase), "s"(lds_base) : "memory");
}
#define STPOL "sc1"
DI void gstore16(void* sbase, unsigned voff, u32x4 v) { asm volatile("global_store_dwordx4 %0, %1, %2 " STPOL "\n\ts_nop 1" :: "v"(voff), "v"(v), "s"(sbase) : "memory"); }
DI void gstore8(void* sbase, unsigned voff, u32x2 v) { asm volatile("global_store_dwordx2 %0, %1, %2" :: "v"(voff), "v"(v), "s"(sbase) : "memory"); }
DI void gstore4(void* sbase, unsigned voff, unsigned v) { asm volatile("global_store_dword %0, %1, %2" :: "v"(voff), "v"(v), "s"(sbase) : "memory"); }
#define WAITV(n) asm volatile("s_waitcnt vmcnt(" #n ")" ::: "memory")
#define WAITV_BAR(n) asm volatile("s_waitcnt vmcnt(" #n ") lgkmcnt(0)\n\ts_barrier" ::: "memory")

__global__ __launch_bounds__(256) void k_prep(const unsigned short* __restrict__ x, const unsigned short* __restrict__ wqkv, char* __restrict__ ws) {
    STAMP_BEGIN;
    const int tid = threadIdx.x;
    if (blockIdx.x < 256) {
        const int row = blockIdx.x * 8 + (tid >> 5), t = tid & 31;
        const u32x4* xr = (const u32x4*)(x + (size_t)row * D);
        u32x4 v[3];
        v[0] = xr[t]; v[1] = xr[t + 32]; v[2] = (t < 8) ? xr[t + 64] : (u32x4){0u, 0u, 0u, 0u};
        float sum = 0.f;
#pragma unroll
        for (int i = 0; i < 3; ++i)
#pragma unroll
            for (int j = 0; j < 4; ++j) sum += bf_lo(v[i][j]) + bf_hi(v[i][j]);
#pragma unroll
        for (int o = 16; o > 0; o >>= 1) sum += __shfl_xor(sum, o);
        const float mean = sum * (1.0f / D);
        float sq = 0.f;
#pragma unroll
        for (int i = 0; i < 3; ++i)
            if (i < 2 || t < 8)
#pragma unroll
                for (int j = 0; j < 4; ++j) { const float a = bf_lo(v[i][j]) - mean, b = bf_hi(v[i][j]) - mean; sq += a * a + b * b; }
#pragma unroll
        for (int o = 16; o > 0; o >>= 1) sq += __shfl_xor(sq, o);
        const float mb = rbf(mean);
        const float den = rbf(__fsqrt_rn(rbf(rbf(sq * (1.0f / D)) + 1e-5f)));
        char* dst = ws + WS_XNF + (size_t)(row >> 4) * KS * 1024 + (row & 15) * 16;
#pragma unroll
        for (int i = 0; i < 3; ++i) {
            if (i < 2 || t < 8) {
                const int p = t + 32 * i;
                unsigned o[4];
#pragma unroll
                for (int j = 0; j < 4; ++j) o[j] = cvtpk(__fdiv_rn(rbf(bf_lo(v[i][j]) - mb), den), __fdiv_rn(rbf(bf_hi(v[i][j]) - mb), den));
                char* d = dst + (p >> 2) * 1024 + (p & 1) * 512 + ((p >> 1) & 1) * 8;
                *(u32x2*)d = (u32x2){o[0], o[1]};
                *(u32x2*)(d + 256) = (u32x2){o[2], o[3]};
            }
        }
    } else {
        const int base = (blockIdx.x - 256) * 1024 + tid;
#pragma unroll
        for (int i = 0; i < 4; ++i) {
            const int n = base + 256 * i;
            const int pc = n & 3, r = (n >> 2) & 31, sel = (n >> 7) & 1, hs = n >> 8, s = hs % KS, h = hs / KS;
            const int c = pc ^ (2 * ((r >> 2) & 1)), t = c >> 1, g = c & 1;
            const unsigned* w = (const unsigned*)(wqkv + (size_t)(32 * s + r) * (3 * D) + DH * h);
            u32x4 o = {0u, 0u, 0u, 0u};
            if (sel == 0 || t == 0) {
                const int col = (sel == 0 ? t * D : 2 * D) + 8 * g;
                o = (u32x4){w[col / 2], w[col / 2 + 1], w[col / 2 + 2], w[col / 2 + 3]};
            } else if (g == 0) {
                o = (u32x4){w[8], w[D / 2 + 8], w[D + 8], 0u};
            }
            *(u32x4*)(ws + WS_WQP + (size_t)n * 16) = o;
        }
    }
    STAMP_END(0);
}

constexpr int G1_SLOT = 9 * 4096;
constexpr int G1_AHEAD = 3;

DI int img_aoff(int lane) {
    const int kg = lane >> 4, q = (lane & 15) >> 2, p = lane & 3;
    return (4 * kg + q) * 64 + (((p >> 1) ^ (2 * (kg & 1))) * 16) + 8 * (p & 1);
}
DI void g1_ldx(bf16x8& d, const char* base, unsigned lo, int ks) {
    asm volatile("global_load_dwordx4 %0, %1, %2 offset:%3" : "=v"(d) : "v"(lo), "s"(base + (ks >> 2) * 4096), "n"((ks & 3) * 1024));
}
DI float glu(float g, float u) { return u * __builtin_amdgcn_rcpf(1.0f + __builtin_amdgcn_exp2f(g * -LOG2E)); }

constexpr int g1_group(int u) { return u < 4 ? (u & 1) : u / 2; }
constexpr int g1_half(int u) { return u < 4 ? (u >> 1) : (u & 1); }
constexpr int g1_dma_at(int u, int g) {
    if (u == 0) return (g >= 12 && g <= 20 && !(g & 1)) ? 1 * 8 + (g - 12) / 2 : (g >= 22 && g <= 30 && !(g & 1)) ? 2 * 8 + (g - 22) / 2 : -1;
    if (u == 1) return -1;
    if (u == 2) return (g <= 16 && !(g & 3)) ? 3 * 8 + g / 4 : (g >= 20 && g <= 28 && !(g & 1)) ? 4 * 8 + (g - 20) / 2 : -1;
    if (u <= 9) { const int sp = 5; return (g % sp == 0 && g / sp < 5) ? (u + 2) * 8 + g / sp : -1; }
    return -1;
}
constexpr int g1_x_at(int u, int g) {
    if (g & 3) return -1;
    if (u == 0) return g / 4 < 3 ? 6 + g / 4 : -1;
    if (u == 1) return 9 + g / 4;
    return -1;
}
constexpr int g1_x_younger(int ks, int u, int g) {
    int n = 0; bool seen = ks < 6;
    if (ks < 6) n += 2 * (5 - ks);
    for (int uu = 0; uu <= u; ++uu)
        for (int gg = 0; gg < 36 && (uu < u || gg <= g); ++gg) {
            const int x = g1_x_at(uu, gg);
            if (x >= 0) { if (seen) n += 2; if (x == ks) seen = true; }
            if (g1_dma_at(uu, gg) >= 0 && seen) n += 1;
        }
    return n;
}

struct G1Ctx {
    const char *wrow, *wq, *xs;
    char* hdst;
    unsigned lds0, dsto, voff, lo;
    int fbase, fsingle, w;
};
DI void g1_piece(const G1Ctx& c, int unit, int m) {
    const int grp = g1_group(unit), hk = g1_half(unit);
    const unsigned slot = c.lds0 + (unit & 3) * G1_SLOT;
    const int mm = (m == 4 && c.w >= 4) ? 3 : m;
    if (grp == 0) {
        const int pi = c.w + 8 * mm;
        glds16(c.wq + hk * G1_SLOT + pi * 1024, c.lo, slot + pi * 1024);
    } else {
        const int f0 = grp == 5 ? c.fsingle : c.fbase + 32 * (grp - 1);
        glds16(c.wrow + (size_t)(288 * hk + 64 * mm) * (F * 2) + f0 * 2, c.voff, slot + c.dsto + mm * 8192);
    }
}

struct G1Epi { float e[2], h[2]; u32x4 pk; };
DI void g1_epi_slice(int s, const f32x4 (&c)[2][2][2], G1Epi& t, char* hdst, unsigned lo) {
    const int v = s >> 2, nh = v >> 3, j = v & 7, ph = s & 3;
    if (ph == 0) { t.e[v & 1] = __builtin_amdgcn_exp2f(c[0][j >> 2][nh][j & 3] * -LOG2E); asm volatile("" : "+v"(t.e[v & 1])); }
    if (ph == 1) { t.e[v & 1] = __builtin_amdgcn_rcpf(1.0f + t.e[v & 1]); asm volatile("" : "+v"(t.e[v & 1])); }
    if (ph == 2) { t.h[v & 1] = c[1][j >> 2][nh][j & 3] * t.e[v & 1]; asm volatile("" : "+v"(t.h[v & 1])); }
    if (ph == 3 && (v & 1)) {
        t.pk[j >> 1] = cvtpk(t.h[0], t.h[1]);
        asm volatile("" : "+v"(t.pk[j >> 1]));
        if (j == 7) gstore16(hdst + (size_t)nh * FS * 1024, lo, t.pk);
    }
}

template <int U>
DI void g1_unit(const G1Ctx& cx, lds_cptr A0, lds_cptr A1, bf16x8 (&xb)[2][KS], f32x4 (&c)[2][2][2], const f32x4 (&cprev)[2][2][2]) {
    constexpr int GRP = g1_group(U), HK = g1_half(U);
    constexpr bool PAIR = GRP != 5;
    constexpr int PER = PAIR ? 4 : 2, N = 9 * PER;
    constexpr bool EPI = U >= 4;
    bf16x8 fr[4];
    lds_cptr a0 = A0 + (U & 3) * G1_SLOT, a1 = A1 + (U & 3) * G1_SLOT;
    asm volatile("" : "+v"(a0), "+v"(a1));
    auto rd = [&](int i) {
        const int ks = i / PER, sel = PAIR ? (i >> 1) & 1 : i & 1, t = PAIR ? i & 1 : 0;
        const lds_cptr a = (t ? a1 : a0) + ks * 4096 + sel * 2048;
        return trfrag(a, a + 1024);
    };
#pragma unroll
    for (int i = 0; i < G1_AHEAD; ++i) fr[i] = rd(i);
    if (HK == 0) {
#pragma unroll
        for (int i = 0; i < 8; ++i) c[i >> 2][(i >> 1) & 1][i & 1] = (f32x4){};
    }
    G1Epi ep;
    char* hprev = cx.hdst + (U / 2 - 2) * 1024;
    static_for<0, N>([&](auto ic) {
        constexpr int i = decltype(ic)::value;
        constexpr int kl = i / PER, ks = 9 * HK + kl, sel = PAIR ? (i >> 1) & 1 : i & 1, t = PAIR ? i & 1 : 0;
        constexpr int g = PAIR ? i : 2 * i;
        constexpr int x = g1_x_at(U, g), dm = g1_dma_at(U, g);
        if constexpr (x >= 0) { g1_ldx(xb[0][x], cx.xs, cx.lo, x); g1_ldx(xb[1][x], cx.xs + KS * 1024, cx.lo, x); }
        if constexpr (dm >= 0) g1_piece(cx, dm >> 3, dm & 7);
        if constexpr ((U == 0 || U == 2) && (i & 3) == 0) {
            constexpr int young = g1_x_younger(ks, U, g);
            asm volatile("s_waitcnt vmcnt(%2)" : "+v"(xb[0][ks]), "+v"(xb[1][ks]) : "n"(young));
        }
        if constexpr (i + G1_AHEAD < N) fr[(i + G1_AHEAD) & 3] = rd(i + G1_AHEAD);
        c[sel][t][0] = MFMA32K(fr[i & 3], xb[0][ks], c[sel][t][0]);
        c[sel][t][1] = MFMA32K(fr[i & 3], xb[1][ks], c[sel][t][1]);
        if constexpr (EPI && PAIR) {
            constexpr int sl = 36 * HK + i - 4;
            if constexpr (sl >= 0 && sl < 64) g1_epi_slice(sl, cprev, ep, hprev, cx.lo);
        } else if constexpr (EPI) {
            constexpr int sl = 2 * (18 * HK + i - 2);
            if constexpr (sl >= 0 && sl < 64) { g1_epi_slice(sl, cprev, ep, hprev, cx.lo); g1_epi_slice(sl + 1, cprev, ep, hprev, cx.lo); }
        }
        SBAR();
    });
}
DI void g1_epi_qkv(const f32x4 (&c)[2][2][2], char* ws, int h, int tb, int lane) {
    asm volatile("" : "+v"(lane));
    const int t = lane & 15, g4 = lane >> 4;
    char* qf1 = ws + WS_QF1 + ((size_t)h * NTB + tb) * 1024;
    char* rec = ws + WS_KV + ((size_t)h * NTB + tb) * KVREC;
    float kmax = 0.f;
    unsigned q2[2];
#pragma unroll
    for (int nh = 0; nh < 2; ++nh) {
        const int r = 16 * nh + t;
        const unsigned fo = (r + 32 * (g4 >> 1)) * 16 + (g4 & 1) * 8;
        const f32x4 q = c[0][0][nh], k = c[0][1][nh], v = c[1][0][nh], e = c[1][1][nh];
        gstore8(qf1, fo, (u32x2){cvtpk(q[0] * QSCALE, q[1] * QSCALE), cvtpk(q[2] * QSCALE, q[3] * QSCALE)});
        const u32x2 kp = {cvtpk(k[0], k[1]), cvtpk(k[2], k[3])};
        gstore8(rec, fo, kp);
        gstore8(rec, 1280 + r * 40 + g4 * 8, (u32x2){cvtpk(v[0], v[1]), cvtpk(v[2], v[3])});
        const unsigned e01 = cvtpk(e[0], e[1]), e23 = cvtpk(e[2], e[3]);
        char* pm = g4 == 0 ? rec + 1024 + r * 8 : (g4 == 1 ? rec + 1280 + r * 40 + 32 : ws + WS_TRASH + lane * 8);
        const u32x2 vm = g4 == 0 ? (u32x2){e23, 0u} : (u32x2){e01, 0x00003f80u};
        asm volatile("global_store_dwordx2 %0, %1, off" :: "v"(pm), "v"(vm) : "memory");
        q2[nh] = cvtpk(e[0] * QSCALE, e[1] * QSCALE);
        float n2 = g4 == 0 ? bf_lo(e23) * bf_lo(e23) + bf_hi(e23) * bf_hi(e23) : 0.f;
        n2 += bf_lo(kp[0]) * bf_lo(kp[0]) + bf_hi(kp[0]) * bf_hi(kp[0]) + bf_lo(kp[1]) * bf_lo(kp[1]) + bf_hi(kp[1]) * bf_hi(kp[1]);
        n2 += __shfl_xor(n2, 16);
        n2 += __shfl_xor(n2, 32);
        kmax = fmaxf(kmax, n2);
    }
#pragma unroll
    for (int o = 8; o > 0; o >>= 1) kmax = fmaxf(kmax, __shfl_xor(kmax, o));
#pragma unroll
    for (int nh = 0; nh < 2; ++nh) {
        char* p = g4 == 0 ? ws + WS_QF2 + ((size_t)h * S + tb * 32 + 16 * nh + t) * 4 : (g4 == 1 ? ws + WS_KMX + ((size_t)h * NTB + tb) * 4 : ws + WS_TRASH + 512 + lane * 4);
        const unsigned val = g4 == 0 ? q2[nh] : __float_as_uint(kmax);
        asm volatile("global_store_dword %0, %1, off" :: "v"(p), "v"(val) : "memory");
    }
}

__global__ __launch_bounds__(512, 2) void k_gemm1(const char* wg, const char* wu, char* ws) {
    extern __shared__ __attribute__((aligned(16))) char lds[];
    STAMP_BEGIN;
    const int tid = threadIdx.x, lane = tid & 63;
    const int w = __builtin_amdgcn_readfirstlane(tid >> 6);
    const int xcd = blockIdx.x & 7, idx = blockIdx.x >> 3;
    const int cg = 4 * xcd + (idx & 3), rg = idx >> 2, tb = 8 * rg + w;
    const bool late = w >= 4;
    MARKS; MARK(0);

    const int odd = cg & 1;
    G1Ctx cx;
    cx.w = w; cx.lds0 = (unsigned)(uintptr_t)lds; cx.lo = lane * 16;
    cx.fbase = 144 * cg + 16 * odd; cx.fsingle = odd ? 144 * cg - 16 : 144 * cg + 128;
    cx.wq = ws + WS_WQP + (size_t)cg * KS * 4096;
    cx.xs = ws + WS_XNF + (size_t)(2 * tb) * KS * 1024;
    cx.wrow = ((w & 1) ? wu : wg) + (size_t)(32 * (w >> 2) + 16 * ((w >> 1) & 1)) * (F * 2);
    cx.dsto = (w >> 2) * 4096 + (w & 1) * 2048 + ((w >> 1) & 1) * 1024;
    cx.voff = (lane >> 2) * (F * 2) + (((lane & 3) ^ (2 * ((lane >> 4) & 1))) * 16);
    cx.hdst = ws + WS_HF + ((size_t)(2 * tb) * FS + cx.fbase / 32) * 1024;

#pragma unroll
    for (int m = 0; m < 5; ++m) g1_piece(cx, 0, m);
    bf16x8 xb[2][KS];
#pragma unroll
    for (int s = 0; s < 6; ++s) { g1_ldx(xb[0][s], cx.xs, cx.lo, s); g1_ldx(xb[1][s], cx.xs + KS * 1024, cx.lo, s); }

    const int aoff0 = img_aoff(lane);
    const lds_cptr A0 = (lds_cptr)lds + aoff0, A1 = (lds_cptr)lds + (aoff0 ^ 32);
    f32x4 ca[2][2][2], cb[2][2][2];

    WAITV_BAR(12);
    MARK(1);
    g1_unit<0>(cx, A0, A1, xb, ca, cb); WAITV_BAR(5);
    g1_unit<1>(cx, A0, A1, xb, cb, ca); WAITV_BAR(18);
    MARK(2);
    g1_unit<2>(cx, A0, A1, xb, ca, cb); WAITV_BAR(5);
    MARK(3);
    if (!late) g1_epi_qkv(ca, ws, cg, tb, lane);
    g1_unit<3>(cx, A0, A1, xb, cb, ca);
    if (late) g1_epi_qkv(ca, ws, cg, tb, lane);
    WAITV_BAR(5);
    MARK(4);
    g1_unit<4>(cx, A0, A1, xb, ca, cb); WAITV_BAR(5);
    g1_unit<5>(cx, A0, A1, xb, ca, cb); WAITV_BAR(5);
    MARK(5);
    g1_unit<6>(cx, A0, A1, xb, cb, ca); WAITV_BAR(5);
    g1_unit<7>(cx, A0, A1, xb, cb, ca); WAITV_BAR(5);
    MARK(6);
    g1_unit<8>(cx, A0, A1, xb, ca, cb); WAITV_BAR(5);
    g1_unit<9>(cx, A0, A1, xb, ca, cb); WAITV_BAR(5);
    MARK(7);
    const lds_cptr AS = odd ? A1 : A0;
    g1_unit<10>(cx, AS, AS, xb, cb, ca); WAITV_BAR(0);
    g1_unit<11>(cx, AS, AS, xb, cb, ca);
    {
        char* hs = ws + WS_HF + ((size_t)(2 * tb) * FS + cx.fsingle / 32) * 1024 + odd * 8;
#pragma unroll
        for (int nh = 0; nh < 2; ++nh) {
            float h[4];
#pragma unroll
            for (int j = 0; j < 4; ++j) h[j] = glu(cb[0][0][nh][j], cb[1][0][nh][j]);
            gstore8(hs + (size_t)nh * FS * 1024, cx.lo, (u32x2){cvtpk(h[0], h[1]), cvtpk(h[2], h[3])});
        }
    }
    MARK(8); MARK_DUMP(1, 9);
    STAMP_END(1);
}

constexpr int AT_STAGE = 8 * KVREC;
constexpr float AT_BOUND_MAX = 50.0f;

struct KFrag { bf16x8 k1; bf16x4 k2; };
struct VFrag { bf16x8 v0, v1; };
DI KFrag at_readk(lds_cptr rec, int lane) {
    KFrag k;
    k.k1 = *(const __attribute__((address_space(3))) bf16x8*)(rec + lane * 16);
    k.k2 = *(const __attribute__((address_space(3))) bf16x4*)(rec + 1024 + (lane & 31) * 8);
    return k;
}
DI VFrag at_readv(lds_cptr recv) {
    VFrag v;
    v.v0 = trfrag(recv, recv + 8 * 40);
    v.v1 = trfrag(recv + 16 * 40, recv + 24 * 40);
    return v;
}
DI bf16x8 packp(const f32x16& p, int s) {
    return __builtin_bit_cast(bf16x8, (u32x4){cvtpk(p[8 * s], p[8 * s + 1]), cvtpk(p[8 * s + 2], p[8 * s + 3]), cvtpk(p[8 * s + 4], p[8 * s + 5]), cvtpk(p[8 * s + 6], p[8 * s + 7])});
}

DI void at_dma(const char* kvh, int stage, unsigned ldsb, int w, int lane) {
    const char* src = kvh + (size_t)stage * AT_STAGE + w * 5 * 1024;
#pragma unroll
    for (int i = 0; i < 5; ++i) glds16(src + i * 1024, lane * 16, ldsb + (w * 5 + i) * 1024);
}

#define AMFMA16_NEW(d, a, b, c) asm volatile("v_mfma_f32_32x32x16_bf16 %0, %1, %2, %3" : "=&v"(d) : "v"(a), "v"(b), "v"(c))
#define AMFMA16_ACC(d, a, b) asm volatile("v_mfma_f32_32x32x16_bf16 %0, %1, %2, %0" : "+v"(d) : "v"(a), "v"(b))
#define AMFMA8_ACC(d, a, b) asm volatile("v_mfma_f32_32x32x8_bf16 %0, %1, %2, %0" : "+v"(d) : "v"(a), "v"(b))
#define AT_EXP4(E, SX, a) do { _Pragma("unroll") for (int i_ = 0; i_ < 4; ++i_) asm volatile("v_exp_f32 %0, %1" : "=v"(E[i_]) : "v"(SX[a + i_])); } while (0)
#define AT_PK2(DST, k, E) do { asm volatile("v_cvt_pk_bf16_f32 %0, %1, %2" : "=v"(DST[k]) : "v"(E[0]), "v"(E[1]));   \
                               asm volatile("v_cvt_pk_bf16_f32 %0, %1, %2" : "=v"(DST[k + 1]) : "v"(E[2]), "v"(E[3])); } while (0)
#define AT_STEP(SX, PX0, PX1, SY, CY, QY1, QY2, KF, OY, PY0, PY1, VF, LOADS)                                   \
    do {                                                                                                       \
        float e0_[4], e1_[4], e2_[4];                                                                          \
        AMFMA16_NEW(SY, KF.k1, QY1, CY);                                                                       \
        LOADS;                                                                                                 \
        SBAR();                                                                                                \
        AT_EXP4(e0_, SX, 0);                                                                                   \
        AT_PK2(PY1, 2, etail);                                                                                 \
        AMFMA8_ACC(SY, KF.k2, QY2);                                                                            \
        AT_EXP4(e1_, SX, 4);                                                                                   \
        AT_PK2(PX0, 0, e0_);                                                                                   \
        AMFMA16_ACC(OY, VF.v0, PY0);                                                                           \
        AT_EXP4(e2_, SX, 8);                                                                                   \
        AT_PK2(PX0, 2, e1_);                                                                                   \
        AMFMA16_ACC(OY, VF.v1, PY1);                                                                           \
        AT_EXP4(etail, SX, 12);                                                                                \
        AT_PK2(PX1, 0, e2_);                                                                                   \
        SBAR();                                                                                                \
    } while (0)

DI void at_online(const KFrag& k, const VFrag& v, bf16x8 q1, bf16x4 q2, float& m, f32x16& o) {
    f32x16 z = {};
    f32x16 s = MFMA16(k.k1, q1, z);
    s = MFMA8(k.k2, q2, s);
    float t = s[0];
#pragma unroll
    for (int i = 1; i < 16; ++i) t = fmaxf(t, s[i]);
    t = fmaxf(t, swap32(t));
    const float mn = fmaxf(m, t);
    const float alpha = __builtin_amdgcn_exp2f(m - mn);
    m = mn;
#pragma unroll
    for (int i = 0; i < 16; ++i) { s[i] = __builtin_amdgcn_exp2f(s[i] - mn); o[i] *= alpha; }
    o = MFMA16(v.v0, packp(s, 0), o);
    o = MFMA16(v.v1, packp(s, 1), o);
}

DI void at_store(const f32x16& o, char* ws, int h, int tb, int lane) {
    const float inv = 1.0f / o[10];
    const int r = lane & 31, hi = lane >> 5;
    char* of = ws + WS_OF + (size_t)(2 * tb + (r >> 4)) * KS * 1024;
    *(u32x4*)(of + (h >> 1) * 1024 + ((r & 15) + 16 * (2 * (h & 1) + hi)) * 16) =
        (u32x4){cvtpk(o[0] * inv, o[1] * inv), cvtpk(o[2] * inv, o[3] * inv), cvtpk(o[4] * inv, o[5] * inv), cvtpk(o[6] * inv, o[7] * inv)};
    if (hi == 0) *(unsigned*)(of + (16 + (h >> 4)) * 1024 + ((r & 15) + 16 * ((h >> 2) & 3)) * 16 + (h & 3) * 4) = cvtpk(o[8] * inv, o[9] * inv);
}

DI float at_qnorm2(bf16x8 q1, bf16x4 q2) {
    const u32x4 a = __builtin_bit_cast(u32x4, q1); const u32x2 b = __builtin_bit_cast(u32x2, q2);
    float n = bf_lo(b[0]) * bf_lo(b[0]) + bf_hi(b[0]) * bf_hi(b[0]);
#pragma unroll
    for (int j = 0; j < 4; ++j) n += bf_lo(a[j]) * bf_lo(a[j]) + bf_hi(a[j]) * bf_hi(a[j]);
    return n + swap32(n);
}

DI void attn_block(char* ws, char* lds, int bid) {
    const int tid = threadIdx.x, lane = tid & 63, hi = lane >> 5;
    const int w = __builtin_amdgcn_readfirstlane(tid >> 6);
    const int xcd = bid & 7, idx = bid >> 3;
    const int h = 4 * xcd + (idx & 3), qg = idx >> 2;
    const int tbA = 8 * qg + 2 * w, tbB = tbA + 1;
    const unsigned lds0 = (unsigned)(uintptr_t)lds;
    const char* kvh = ws + WS_KV + (size_t)h * NTB * KVREC;
    MARKS; MARK(0);

    at_dma(kvh, 0, lds0, w, lane);
    at_dma(kvh, 1, lds0 + AT_STAGE, w, lane);

    const bf16x8 qA1 = *(const bf16x8*)(ws + WS_QF1 + ((size_t)h * NTB + tbA) * 1024 + lane * 16);
    const bf16x8 qB1 = *(const bf16x8*)(ws + WS_QF1 + ((size_t)h * NTB + tbB) * 1024 + lane * 16);
    const unsigned qa2 = *(const unsigned*)(ws + WS_QF2 + ((size_t)h * S + tbA * 32 + (lane & 31)) * 4);
    const unsigned qb2 = *(const unsigned*)(ws + WS_QF2 + ((size_t)h * S + tbB * 32 + (lane & 31)) * 4);
    float km = *(const float*)(ws + WS_KMX + ((size_t)h * NTB + lane) * 4);
    const bf16x4 qA2 = __builtin_bit_cast(bf16x4, (u32x2){hi ? 0u : qa2, 0u});
    const bf16x4 qB2 = __builtin_bit_cast(bf16x4, (u32x2){hi ? 0u : qb2, 0u});
#pragma unroll
    for (int o = 32; o > 0; o >>= 1) km = fmaxf(km, __shfl_xor(km, o));
    const float mA = __builtin_sqrtf(at_qnorm2(qA1, qA2) * km) * 1.001f, mB = __builtin_sqrtf(at_qnorm2(qB1, qB2) * km) * 1.001f;
    const bool fast = __all(mA <= AT_BOUND_MAX && mB <= AT_BOUND_MAX);

    const int g16 = (lane >> 4) & 1, q = (lane & 15) >> 2, p = lane & 3;
    const int voffs = 1280 + (4 * hi + q) * 40 + (g16 ? 32 : p * 8);
    const lds_cptr L = (lds_cptr)lds;
    f32x16 oA = {}, oB = {};

    if (fast) {
        f32x16 cA, cB;
#pragma unroll
        for (int i = 0; i < 16; ++i) { cA[i] = -mA; cB[i] = -mB; }
        asm volatile("" : "+v"(cA), "+v"(cB));
        f32x16 sA, sB;
        u32x4 pA0 = {}, pA1 = {}, pB0 = {}, pB1 = {};
        float etail[4] = {0.f, 0.f, 0.f, 0.f};
        KFrag k0, k1; VFrag v0, v1;
        MARK(1);
        WAITV_BAR(5);
        MARK(2);
        k0 = at_readk(L, lane);
        v1 = at_readv(L + voffs);
        sA = MFMA16(k0.k1, qA1, cA); sA = MFMA8(k0.k2, qA2, sA);
        asm volatile("s_nop 15" : "+v"(sA));
        int slot = 0;
        for (int st = 0; st < 8; ++st) {
            const lds_cptr cur = L + slot * AT_STAGE;
            const int nslot = slot == 2 ? 0 : slot + 1;
#pragma unroll
            for (int t = 0; t < 8; t += 2) {
                AT_STEP(sA, pA0, pA1, sB, cB, qB1, qB2, k0, oB, pB0, pB1, v1,
                        (k1 = at_readk(cur + (t + 1) * KVREC, lane), v0 = at_readv(cur + t * KVREC + voffs)));
                AT_STEP(sB, pB0, pB1, sA, cA, qA1, qA2, k1, oA, pA0, pA1, v0, (void)0);
                lds_cptr nk = cur + (t + 2) * KVREC;
                if (t == 6) {
                    if (st < 7) {
                        if (st < 6) { WAITV_BAR(0); at_dma(kvh, st + 2, lds0 + (nslot == 2 ? 0 : nslot + 1) * AT_STAGE, w, lane); }
                        else WAITV_BAR(0);
                        nk = L + nslot * AT_STAGE;
                    } else nk = cur;
                }
                AT_STEP(sA, pA0, pA1, sB, cB, qB1, qB2, k1, oB, pB0, pB1, v0,
                        (k0 = at_readk(nk, lane), v1 = at_readv(cur + (t + 1) * KVREC + voffs)));
                AT_STEP(sB, pB0, pB1, sA, cA, qA1, qA2, k0, oA, pA0, pA1, v1, (void)0);
            }
            slot = nslot;
            MARK(3 + st);
        }
        asm volatile("s_nop 1");
        AT_PK2(pB1, 2, etail);
        asm volatile("s_nop 1" : "+v"(pB1));
        AMFMA16_ACC(oB, v1.v0, pB0);
        AMFMA16_ACC(oB, v1.v1, pB1);
        asm volatile("s_nop 15\n\ts_nop 7" : "+v"(oA), "+v"(oB));
    } else {
        float mxA = -INFINITY, mxB = -INFINITY;
        WAITV_BAR(5);
        int slot = 0;
        for (int st = 0; st < 8; ++st) {
            const lds_cptr cur = L + slot * AT_STAGE;
            const int nslot = slot == 2 ? 0 : slot + 1;
            for (int t = 0; t < 8; ++t) {
                const KFrag k = at_readk(cur + t * KVREC, lane);
                const VFrag v = at_readv(cur + t * KVREC + voffs);
                at_online(k, v, qA1, qA2, mxA, oA);
                at_online(k, v, qB1, qB2, mxB, oB);
            }
            if (st < 7) {
                WAITV_BAR(0);
                if (st < 6) at_dma(kvh, st + 2, lds0 + (nslot == 2 ? 0 : nslot + 1) * AT_STAGE, w, lane);
            }
            slot = nslot;
        }
    }
    at_store(oA, ws, h, tbA, lane);
    at_store(oB, ws, h, tbB, lane);
    MARK(11); MARK_DUMP(2, 12);
}

__global__ __launch_bounds__(256, 2) void k_attn(char* ws) {
    extern __shared__ __attribute__((aligned(16))) char lds[];
    STAMP_BEGIN;
    attn_block(ws, lds, blockIdx.x);
    STAMP_END(2);
}

constexpr int DN_STEP = 32 * 288, DN_STAGE = 4 * DN_STEP;

DI void down_block(const char* wd, char* ws, char* lds, int bid) {
    const int tid = threadIdx.x, lane = tid & 63;
    const int w = __builtin_amdgcn_readfirstlane(tid >> 6);
    const int xcd = bid & 7, idx = bid >> 3;
    const int kq = xcd >> 1, tg = (xcd & 1) * 8 + (idx >> 2), dq = idx & 3;
    const int tb16 = 8 * tg + 2 * w, fs0 = 36 * kq;
    const unsigned lds0 = (unsigned)(uintptr_t)lds;
    unsigned voff[9];
#pragma unroll
    for (int i = 0; i < 9; ++i) { const int n = 64 * i + lane; voff[i] = (n / 18) * (D * 2) + (n % 18) * 16; }
    const char* wsrc = wd + (size_t)(fs0 + w) * 32 * (D * 2) + dq * 288;
    const lds_cptr A = (lds_cptr)lds + (4 * (lane >> 4) + ((lane & 15) >> 2)) * 288 + (lane & 3) * 8;
    const char* hs = ws + WS_HF + ((size_t)tb16 * FS + fs0) * 1024;
    const unsigned lo = lane * 16;
    MARKS; MARK(0);

    f32x4 acc[9][2];
#pragma unroll
    for (int i = 0; i < 9; ++i) { acc[i][0] = (f32x4){}; acc[i][1] = (f32x4){}; }
    bf16x8 b[2][2][4];
    auto ldb = [&](int set, int st) {
#pragma unroll
        for (int k = 0; k < 4; ++k) {
            asm volatile("global_load_dwordx4 %0, %1, %2 offset:%3" : "=v"(b[set][0][k]) : "v"(lo), "s"(hs + (size_t)st * 4096), "n"(k * 1024));
            asm volatile("global_load_dwordx4 %0, %1, %2 offset:%3" : "=v"(b[set][1][k]) : "v"(lo), "s"(hs + (size_t)FS * 1024 + (size_t)st * 4096), "n"(k * 1024));
        }
    };
    auto dma = [&](int st, int i) { glds16(wsrc + (size_t)st * 4 * 32 * (D * 2), voff[i], lds0 + (st & 1) * DN_STAGE + w * DN_STEP + i * 1024); };
#pragma unroll
    for (int i = 0; i < 9; ++i) dma(0, i);
    ldb(0, 0);
    auto stage = [&](int st, auto setc, auto lastc) {
        constexpr int set = decltype(setc)::value;
        constexpr bool last = decltype(lastc)::value;
        asm volatile("s_waitcnt vmcnt(0)\n\ts_barrier"
                     : "+v"(b[set][0][0]), "+v"(b[set][0][1]), "+v"(b[set][0][2]), "+v"(b[set][0][3]), "+v"(b[set][1][0]), "+v"(b[set][1][1]), "+v"(b[set][1][2]), "+v"(b[set][1][3])
                     :: "memory");
        if (!last) ldb(set ^ 1, st + 1);
        const lds_cptr a = A + set * DN_STAGE;
        bf16x8 fr[8];
        auto rd = [&](int i) { const lds_cptr pa = a + (i / 9) * DN_STEP + (i % 9) * 32; return trfrag(pa, pa + 16 * 288); };
#pragma unroll
        for (int i = 0; i < 6; ++i) fr[i] = rd(i);
#pragma unroll
        for (int i = 0; i < 36; ++i) {
            if (!last && (i % 3) == 0 && i / 3 < 9) dma(st + 1, i / 3);
            if (i + 6 < 36) fr[(i + 6) & 7] = rd(i + 6);
            acc[i % 9][0] = MFMA32K(fr[i & 7], b[set][0][i / 9], acc[i % 9][0]);
            acc[i % 9][1] = MFMA32K(fr[i & 7], b[set][1][i / 9], acc[i % 9][1]);
            SBAR();
        }
        asm volatile("s_waitcnt lgkmcnt(0)" ::: "memory");
        MARK(1 + st);
    };
    for (int st = 0; st < 8; st += 2) {
        stage(st, std::integral_constant<int, 0>{}, std::false_type{});
        stage(st + 1, std::integral_constant<int, 1>{}, std::false_type{});
    }
    stage(8, std::integral_constant<int, 0>{}, std::true_type{});
    char* dst = ws + WS_PART + (((size_t)kq * NT16 + tb16) * 36 + 9 * dq) * 1024;
#pragma unroll
    for (int nh = 0; nh < 2; ++nh)
#pragma unroll
        for (int i = 0; i < 9; ++i) gstore16(dst + (size_t)nh * 36 * 1024 + i * 1024, lo, __builtin_bit_cast(u32x4, acc[i][nh]));
    MARK(10); MARK_DUMP(3, 11);
}

__global__ __launch_bounds__(256, 2) void k_down(const char* wd, char* ws) {
    extern __shared__ __attribute__((aligned(16))) char lds[];
    STAMP_BEGIN;
    down_block(wd, ws, lds, blockIdx.x);
    STAMP_END(3);
}

#define AT_PRIO 2
#define MIX_MODE 0
__global__ __launch_bounds__(256, 2) void k_mix(const char* wd, char* ws) {
    extern __shared__ __attribute__((aligned(16))) char lds[];
    STAMP_BEGIN;
    const int b = blockIdx.x;
    const bool attn = b < 256; const int bid = b & 255;
    if (attn) { __builtin_amdgcn_s_setprio(AT_PRIO); attn_block(ws, lds, bid); } else down_block(wd, ws, lds, bid);
    STAMP_END(5);
}

__global__ __launch_bounds__(256) void k_final(const char* wo, const unsigned short* x, char* ws, unsigned short* out) {
    __shared__ __attribute__((aligned(16))) char lds[KS * 2048];
    STAMP_BEGIN;
    const int tid = threadIdx.x, lane = tid & 63;
    const int w = __builtin_amdgcn_readfirstlane(tid >> 6);
    const int xcd = blockIdx.x & 7, idx = blockIdx.x >> 3;
    const int tg = 2 * xcd + idx / 18, dt = idx % 18, tb16 = 8 * tg + 2 * w;
    const unsigned lds0 = (unsigned)(uintptr_t)lds;
    {
        const unsigned pc = ((lane & 3) ^ (2 * ((lane >> 4) & 1))) * 16;
        const int rl = lane >> 2, kg = rl >> 2, q = rl & 3;
#pragma unroll
        for (int i = 0; i < 9; ++i) {
            const int n = w * 9 + i, ks = n >> 1, jj = n & 1;
            const int row = ks < 16 ? DH * (2 * ks + (kg >> 1)) + 8 * jj + 4 * (kg & 1) + q : DH * (16 * (ks - 16) + 4 * kg + 2 * jj + (q >> 1)) + 16 + (q & 1);
            glds16(wo + dt * 64, row * (D * 2) + pc, lds0 + n * 1024);
        }
    }
    const char* os = ws + WS_OF + (size_t)tb16 * KS * 1024 + lane * 16;
    bf16x8 b[2][KS];
#pragma unroll
    for (int s = 0; s < KS; ++s) { b[0][s] = *(const bf16x8*)(os + s * 1024); b[1][s] = *(const bf16x8*)(os + (KS + s) * 1024); }
    f32x4 part[2][2];
    u32x2 xv[2][2];
#pragma unroll
    for (int nh = 0; nh < 2; ++nh)
#pragma unroll
        for (int t = 0; t < 2; ++t) {
            const char* pp = ws + WS_PART + ((size_t)(tb16 + nh) * 36 + 2 * dt + t) * 1024 + lane * 16;
            constexpr size_t QS = (size_t)NT16 * 36 * 1024;
            part[nh][t] = (*(const f32x4*)pp + *(const f32x4*)(pp + QS)) + (*(const f32x4*)(pp + 2 * QS) + *(const f32x4*)(pp + 3 * QS));
            xv[nh][t] = *(const u32x2*)(x + (size_t)((tb16 + nh) * 16 + (lane & 15)) * D + dt * 32 + 16 * t + 4 * (lane >> 4));
        }
    asm volatile("s_waitcnt vmcnt(0)\n\ts_barrier" ::: "memory");
    const int aoff0 = img_aoff(lane);
    const lds_cptr A0 = (lds_cptr)lds + aoff0, A1 = (lds_cptr)lds + (aoff0 ^ 32);
    f32x4 acc[2][2] = {};
#pragma unroll
    for (int s = 0; s < KS; ++s) {
        const bf16x8 a0 = trfrag(A0 + s * 2048, A0 + s * 2048 + 1024), a1 = trfrag(A1 + s * 2048, A1 + s * 2048 + 1024);
        acc[0][0] = MFMA32K(a0, b[0][s], acc[0][0]); acc[1][0] = MFMA32K(a0, b[1][s], acc[1][0]);
        acc[0][1] = MFMA32K(a1, b[0][s], acc[0][1]); acc[1][1] = MFMA32K(a1, b[1][s], acc[1][1]);
    }
#pragma unroll
    for (int nh = 0; nh < 2; ++nh)
#pragma unroll
        for (int t = 0; t < 2; ++t) {
            float o[4];
#pragma unroll
            for (int c = 0; c < 4; ++c) {
                const float f = rbf(part[nh][t][c]);
                const float g = rbf(f * __builtin_amdgcn_rcpf(1.0f + __builtin_amdgcn_exp2f(f * -LOG2E)));
                const float xx = (c & 1) ? bf_hi(xv[nh][t][c >> 1]) : bf_lo(xv[nh][t][c >> 1]);
                o[c] = xx + rbf(rbf(acc[nh][t][c]) * g);
            }
            *(u32x2*)(out + (size_t)((tb16 + nh) * 16 + (lane & 15)) * D + dt * 32 + 16 * t + 4 * (lane >> 4)) = (u32x2){cvtpk(o[0], o[1]), cvtpk(o[2], o[3])};
        }
    STAMP_END(4);
}

extern "C" void kernel_launch(void* const* d_in, const int* in_sizes, int n_in, void* d_out, int out_size, void* d_ws, size_t ws_size, hipStream_t stream) {
    const unsigned short* x = (const unsigned short*)d_in[0];
    const unsigned short* wqkv = (const unsigned short*)d_in[1];
    const char* wo = (const char*)d_in[2];
    const char* wg = (const char*)d_in[4];
    const char* wu = (const char*)d_in[5];
    const char* wd = (const char*)d_in[6];
    char* ws = (char*)d_ws;
    (void)hipFuncSetAttribute((const void*)k_gemm1, hipFuncAttributeMaxDynamicSharedMemorySize, 4 * G1_SLOT);
    (void)hipFuncSetAttribute((const void*)k_attn, hipFuncAttributeMaxDynamicSharedMemorySize, 3 * AT_STAGE);
    (void)hipFuncSetAttribute((const void*)k_down, hipFuncAttributeMaxDynamicSharedMemorySize, 2 * DN_STAGE);
    k_prep<<<400, 256, 0, stream>>>(x, wqkv, ws);
    k_gemm1<<<256, 512, 4 * G1_SLOT, stream>>>(wg, wu, ws);
    (void)hipFuncSetAttribute((const void*)k_mix, hipFuncAttributeMaxDynamicSharedMemorySize, 2 * DN_STAGE);
    k_mix<<<512, 256, 2 * DN_STAGE, stream>>>(wd, ws);
    k_final<<<288, 256, 0, stream>>>(wo, x, ws, (unsigned short*)d_out);
}
```

```cpp
#define NO_PF0
#define NO_PF1
#define DIAG 0
#include <hip/hip_runtime.h>
#include <hip/hip_bf16.h>
#include <stdint.h>
#include <type_traits>

typedef __attribute__((ext_vector_type(8))) short bf16x8;
typedef __attribute__((ext_vector_type(4))) short bf16x4;
typedef __attribute__((ext_vector_type(2))) __bf16 bf16p;
typedef __attribute__((ext_vector_type(2))) float f32x2;
typedef __attribute__((ext_vector_type(4))) float f32x4;
typedef __attribute__((ext_vector_type(16))) float f32x16;
typedef __attribute__((ext_vector_type(2))) unsigned u32x2;
typedef __attribute__((ext_vector_type(4))) unsigned u32x4;
typedef __attribute__((address_space(3))) const char* lds_cptr;
typedef __attribute__((address_space(3))) char* lds_ptr;
typedef short v4i16_t __attribute__((ext_vector_type(4)));

#define DI __device__ __forceinline__
#define MFMA16(a, b, c) __builtin_amdgcn_mfma_f32_32x32x16_bf16((a), (b), (c), 0, 0, 0)
#define MFMA32K(a, b, c) __builtin_amdgcn_mfma_f32_16x16x32_bf16((a), (b), (c), 0, 0, 0)
#define MFMA8(a, b, c) __builtin_amdgcn_mfma_f32_32x32x8bf16_1k((a), (b), (c), 0, 0, 0)
#define SBAR() __builtin_amdgcn_sched_barrier(0)

template <int I, int N, class Fn> DI void static_for(Fn&& fn) {
    if constexpr (I < N) { fn(std::integral_constant<int, I>{}); static_for<I + 1, N>(fn); }
}

constexpr int S = 2048, D = 576, NH = 32, DH = 18, F = 4608;
constexpr int NTB = S / 32;
constexpr int NT16 = S / 16;
constexpr int KS = D / 32;
constexpr int FS = F / 32;

constexpr int KVREC = 2560;
constexpr size_t WS_XNF = 0;
constexpr size_t WS_WQP = WS_XNF + (size_t)NT16 * KS * 1024;
constexpr int QREC = 1280;
constexpr size_t WS_WOP = WS_WQP + (size_t)NH * KS * 4096;
constexpr size_t WS_Q = WS_WOP + (size_t)36 * KS * 1024;
constexpr size_t WS_KV = WS_Q + (size_t)NH * NTB * QREC;
constexpr size_t WS_OF = WS_KV + (size_t)NH * NTB * KVREC;
constexpr size_t WS_HF = WS_OF + (size_t)NT16 * KS * 1024;
constexpr size_t WS_PART = WS_HF + (size_t)NT16 * FS * 1024;
constexpr size_t WS_G = WS_PART + (size_t)4 * NT16 * 36 * 1024;
constexpr size_t WS_CNT = WS_G + (size_t)NT16 * 36 * 512;
constexpr size_t WS_END = WS_CNT + 64 * 256;

#define STAMP_BEGIN
#define STAMP_END(k)
#define MARKS
#define MARK(i)
#define MARK_DUMP(k, n)

constexpr size_t WS_DIAG = 190u << 20;
DI void diag_stamp(char* ws, int k) {
    if (blockIdx.x == 0 && threadIdx.x == 0) { const unsigned long long t = __builtin_amdgcn_s_memrealtime(); __hip_atomic_store((unsigned long long*)(ws + WS_DIAG) + k, t, __ATOMIC_RELAXED, __HIP_MEMORY_SCOPE_AGENT); }
}
#define DIAG_STAMP(k) diag_stamp(ws, k)
constexpr float LOG2E = 1.4426950408889634f;
constexpr float QSCALE = 0.23570226039551584f * LOG2E;

DI unsigned cvtpk(float lo, float hi) { return __builtin_bit_cast(unsigned, __builtin_convertvector((f32x2){lo, hi}, bf16p)); }
DI float bf_lo(unsigned u) { return __uint_as_float(u << 16); }
DI float bf_hi(unsigned u) { return __uint_as_float(u & 0xffff0000u); }
DI float rbf(float x) { return bf_lo(cvtpk(x, x)); }
DI bf16x4 trread(lds_cptr p) { return __builtin_bit_cast(bf16x4, __builtin_amdgcn_ds_read_tr16_b64_v4i16((__attribute__((address_space(3))) v4i16_t*)p)); }
DI bf16x8 trfrag(lds_cptr p0, lds_cptr p1) { const bf16x4 a = trread(p0), b = trread(p1); return (bf16x8){a[0], a[1], a[2], a[3], b[0], b[1], b[2], b[3]}; }
DI bf16x8 join(bf16x4 a, bf16x4 b) { return (bf16x8){a[0], a[1], a[2], a[3], b[0], b[1], b[2], b[3]}; }
DI float swap16(float x) {
    auto r = __builtin_amdgcn_permlane16_swap(__float_as_uint(x), __float_as_uint(x), false, false);
    return __uint_as_float((threadIdx.x & 16) ? r[0] : r[1]);
}
template <int CTRL> DI float dppf(float x) { return __uint_as_float(__builtin_amdgcn_update_dpp(0u, __float_as_uint(x), CTRL, 0xf, 0xf, true)); }
DI float rowmax16(float x) {
    x = fmaxf(x, dppf<0xB1>(x));
    x = fmaxf(x, dppf<0x4E>(x));
    x = fmaxf(x, dppf<0x141>(x));
    return fmaxf(x, dppf<0x140>(x));
}
DI float swap32(float x) { auto r = __builtin_amdgcn_permlane32_swap(__float_as_uint(x), __float_as_uint(x), false, false); return __uint_as_float((threadIdx.x & 32) ? r[0] : r[1]); }

DI void glds16(const void* sbase, unsigned voff, unsigned lds_base) {
    asm volatile("s_mov_b32 m0, %2\n\ts_nop 0\n\tglobal_load_lds_dwordx4 %0, %1" :: "v"(voff), "s"(sbase), "s"(lds_base) : "memory");
}
#define STPOL "sc1"
DI void gstore16(void* sbase, unsigned voff, u32x4 v) { asm volatile("global_store_dwordx4 %0, %1, %2 " STPOL "\n\ts_nop 1" :: "v"(voff), "v"(v), "s"(sbase) : "memory"); }
DI void gstore8(void* sbase, unsigned voff, u32x2 v) { asm volatile("global_store_dwordx2 %0, %1, %2" :: "v"(voff), "v"(v), "s"(sbase) : "memory"); }
DI void gstore4(void* sbase, unsigned voff, unsigned v) { asm volatile("global_store_dword %0, %1, %2" :: "v"(voff), "v"(v), "s"(sbase) : "memory"); }
#define WAITV(n) asm volatile("s_waitcnt vmcnt(" #n ")" ::: "memory")
#define WAITV_BAR(n) asm volatile("s_waitcnt vmcnt(" #n ") lgkmcnt(0)\n\ts_barrier" ::: "memory")

DI int wo_row(int ks, int kg, int j) {
    return ks < 16 ? DH * (2 * ks + (kg >> 1)) + 8 * (j >> 2) + 4 * (kg & 1) + (j & 3) : DH * (16 * (ks - 16) + 4 * kg + (j >> 1)) + 16 + (j & 1);
}
constexpr int PREP_PF = 64, PREP_WO = 162, PREP_LN = 256, PREP_WQ = 144;

__global__ __launch_bounds__(256) void k_prep(const unsigned short* __restrict__ x, const unsigned short* __restrict__ wqkv, const unsigned short* __restrict__ wo,
                                              const char* __restrict__ wg, const char* __restrict__ wu, char* __restrict__ ws) {
    STAMP_BEGIN; DIAG_STAMP(0);
    const int tid = threadIdx.x;
    if (blockIdx.x < PREP_PF) {
    } else if (blockIdx.x < PREP_PF + PREP_WO) {
        const int n = (blockIdx.x - PREP_PF) * 256 + tid;
        const int lane = n & 63, fs = n >> 6, ks = fs % KS, dt = fs / KS;
        unsigned short v[8];
#pragma unroll
        for (int j = 0; j < 8; ++j) v[j] = wo[(size_t)wo_row(ks, lane >> 4, j) * D + 16 * dt + (lane & 15)];
        *(u32x4*)(ws + WS_WOP + (size_t)n * 16) = (u32x4){v[0] | ((unsigned)v[1] << 16), v[2] | ((unsigned)v[3] << 16), v[4] | ((unsigned)v[5] << 16), v[6] | ((unsigned)v[7] << 16)};
    } else if (blockIdx.x < PREP_PF + PREP_WO + PREP_LN) {
        if (blockIdx.x == PREP_PF + PREP_WO && tid < 64) *(unsigned*)(ws + WS_CNT + tid * 256) = 0u;
        const int row = (blockIdx.x - (PREP_PF + PREP_WO)) * 8 + (tid >> 5), t = tid & 31;
        const u32x4* xr = (const u32x4*)(x + (size_t)row * D);
        u32x4 v[3];
        v[0] = xr[t]; v[1] = xr[t + 32]; v[2] = (t < 8) ? xr[t + 64] : (u32x4){0u, 0u, 0u, 0u};
        float sum = 0.f;
#pragma unroll
        for (int i = 0; i < 3; ++i)
#pragma unroll
            for (int j = 0; j < 4; ++j) sum += bf_lo(v[i][j]) + bf_hi(v[i][j]);
#pragma unroll
        for (int o = 16; o > 0; o >>= 1) sum += __shfl_xor(sum, o);
        const float mean = sum * (1.0f / D);
        float sq = 0.f;
#pragma unroll
        for (int i = 0; i < 3; ++i)
            if (i < 2 || t < 8)
#pragma unroll
                for (int j = 0; j < 4; ++j) { const float a = bf_lo(v[i][j]) - mean, b = bf_hi(v[i][j]) - mean; sq += a * a + b * b; }
#pragma unroll
        for (int o = 16; o > 0; o >>= 1) sq += __shfl_xor(sq, o);
        const float mb = rbf(mean);
        const float den = rbf(__fsqrt_rn(rbf(rbf(sq * (1.0f / D)) + 1e-5f)));
        char* dst = ws + WS_XNF + (size_t)(row >> 4) * KS * 1024 + (row & 15) * 16;
#pragma unroll
        for (int i = 0; i < 3; ++i) {
            if (i < 2 || t < 8) {
                const int p = t + 32 * i;
                unsigned o[4];
#pragma unroll
                for (int j = 0; j < 4; ++j) o[j] = cvtpk(__fdiv_rn(rbf(bf_lo(v[i][j]) - mb), den), __fdiv_rn(rbf(bf_hi(v[i][j]) - mb), den));
                char* d = dst + (p >> 2) * 1024 + (p & 1) * 512 + ((p >> 1) & 1) * 8;
                *(u32x2*)d = (u32x2){o[0], o[1]};
                *(u32x2*)(d + 256) = (u32x2){o[2], o[3]};
            }
        }
    } else {
        const int base = (blockIdx.x - (PREP_PF + PREP_WO + PREP_LN)) * 1024 + tid;
#pragma unroll
        for (int i = 0; i < 4; ++i) {
            const int n = base + 256 * i;
            const int pc = n & 3, r = (n >> 2) & 31, sel = (n >> 7) & 1, hs = n >> 8, s = hs % KS, h = hs / KS;
            const int c = pc ^ (2 * ((r >> 2) & 1)), t = c >> 1, g = c & 1;
            const unsigned* w = (const unsigned*)(wqkv + (size_t)(32 * s + r) * (3 * D) + DH * h);
            u32x4 o = {0u, 0u, 0u, 0u};
            if (sel == 0 || t == 0) {
                const int col = (sel == 0 ? t * D : 2 * D) + 8 * g;
                o = (u32x4){w[col / 2], w[col / 2 + 1], w[col / 2 + 2], w[col / 2 + 3]};
            } else if (g == 0) {
                o = (u32x4){w[8], w[D / 2 + 8], w[D + 8], 0u};
            }
            *(u32x4*)(ws + WS_WQP + (size_t)n * 16) = o;
        }
    }
    STAMP_END(0);
}

constexpr int G1_SLOT = 9 * 4096;
constexpr int G1_AHEAD = 3;

DI int img_aoff(int lane) {
    const int kg = lane >> 4, q = (lane & 15) >> 2, p = lane & 3;
    return (4 * kg + q) * 64 + (((p >> 1) ^ (2 * (kg & 1))) * 16) + 8 * (p & 1);
}
DI void g1_ldx(bf16x8& d, const char* base, unsigned lo, int ks) {
    asm volatile("global_load_dwordx4 %0, %1, %2 offset:%3" : "=v"(d) : "v"(lo), "s"(base + (ks >> 2) * 4096), "n"((ks & 3) * 1024));
}
DI float glu(float g, float u) { return u * __builtin_amdgcn_rcpf(1.0f + __builtin_amdgcn_exp2f(g * -LOG2E)); }

constexpr int g1_group(int u) { return u < 4 ? (u & 1) : u / 2; }
constexpr int g1_half(int u) { return u < 4 ? (u >> 1) : (u & 1); }
constexpr int g1_dma_at(int u, int g) {
    if (u == 0) return (g >= 12 && g <= 20 && !(g & 1)) ? 1 * 8 + (g - 12) / 2 : (g >= 22 && g <= 30 && !(g & 1)) ? 2 * 8 + (g - 22) / 2 : -1;
    if (u == 1) return -1;
    if (u == 2) return (g <= 16 && !(g & 3)) ? 3 * 8 + g / 4 : (g >= 20 && g <= 28 && !(g & 1)) ? 4 * 8 + (g - 20) / 2 : -1;
    if (u <= 9) { const int sp = 5; return (g % sp == 0 && g / sp < 5) ? (u + 2) * 8 + g / sp : -1; }
    return -1;
}
constexpr int g1_x_at(int u, int g) {
    if (g & 3) return -1;
    if (u == 0) return g / 4 < 3 ? 6 + g / 4 : -1;
    if (u == 1) return 9 + g / 4;
    return -1;
}
constexpr int g1_x_younger(int ks, int u, int g) {
    int n = 0; bool seen = ks < 6;
    if (ks < 6) n += 2 * (5 - ks);
    for (int uu = 0; uu <= u; ++uu)
        for (int gg = 0; gg < 36 && (uu < u || gg <= g); ++gg) {
            const int x = g1_x_at(uu, gg);
            if (x >= 0) { if (seen) n += 2; if (x == ks) seen = true; }
            if (g1_dma_at(uu, gg) >= 0 && seen) n += 1;
        }
    return n;
}

struct G1Ctx {
    const char *wrow, *wq, *xs;
    char* hdst;
    unsigned lds0, dsto, voff, lo;
    int fbase, fsingle, w;
};
DI void g1_piece(const G1Ctx& c, int unit, int m) {
    const int grp = g1_group(unit), hk = g1_half(unit);
    const unsigned slot = c.lds0 + (unit & 3) * G1_SLOT;
    const int mm = (m == 4 && c.w >= 4) ? 3 : m;
    if (grp == 0) {
        const int pi = c.w + 8 * mm;
        glds16(c.wq + hk * G1_SLOT + pi * 1024, c.lo, slot + pi * 1024);
    } else {
        const int f0 = grp == 5 ? c.fsingle : c.fbase + 32 * (grp - 1);
        glds16(c.wrow + (size_t)(288 * hk + 64 * mm) * (F * 2) + f0 * 2, c.voff, slot + c.dsto + mm * 8192);
    }
}

struct G1Epi { float e[2], h[2]; u32x4 pk; };
DI void g1_epi_slice(int s, const f32x4 (&c)[2][2][2], G1Epi& t, char* hdst, unsigned lo) {
    const int v = s >> 2, nh = v >> 3, j = v & 7, ph = s & 3;
    if (ph == 0) { t.e[v & 1] = __builtin_amdgcn_exp2f(c[0][j >> 2][nh][j & 3] * -LOG2E); asm volatile("" : "+v"(t.e[v & 1])); }
    if (ph == 1) { t.e[v & 1] = __builtin_amdgcn_rcpf(1.0f + t.e[v & 1]); asm volatile("" : "+v"(t.e[v & 1])); }
    if (ph == 2) { t.h[v & 1] = c[1][j >> 2][nh][j & 3] * t.e[v & 1]; asm volatile("" : "+v"(t.h[v & 1])); }
    if (ph == 3 && (v & 1)) {
        t.pk[j >> 1] = cvtpk(t.h[0], t.h[1]);
        asm volatile("" : "+v"(t.pk[j >> 1]));
        if (j == 7) gstore16(hdst + (size_t)nh * FS * 1024, lo, t.pk);
    }
}

template <int U>
DI void g1_unit(const G1Ctx& cx, lds_cptr A0, lds_cptr A1, bf16x8 (&xb)[2][KS], f32x4 (&c)[2][2][2], const f32x4 (&cprev)[2][2][2]) {
    constexpr int GRP = g1_group(U), HK = g1_half(U);
    constexpr bool PAIR = GRP != 5;
    constexpr int PER = PAIR ? 4 : 2, N = 9 * PER;
    constexpr bool EPI = U >= 4;
    bf16x4 flo[4], fhi[4];
    lds_cptr a0 = A0 + (U & 3) * G1_SLOT, a1 = A1 + (U & 3) * G1_SLOT;
    asm volatile("" : "+v"(a0), "+v"(a1));
    auto addr = [&](int i) {
        const int ks = i / PER, sel = PAIR ? (i >> 1) & 1 : i & 1, t = PAIR ? i & 1 : 0;
        return (t ? a1 : a0) + ks * 4096 + sel * 2048;
    };
#pragma unroll
    for (int i = 0; i < G1_AHEAD; ++i) { flo[i] = trread(addr(i)); fhi[i] = trread(addr(i) + 1024); }
    if (HK == 0) {
#pragma unroll
        for (int i = 0; i < 8; ++i) c[i >> 2][(i >> 1) & 1][i & 1] = (f32x4){};
    }
    G1Epi ep;
    char* hprev = cx.hdst + (U / 2 - 2) * 1024;
    static_for<0, N>([&](auto ic) {
        constexpr int i = decltype(ic)::value;
        constexpr int kl = i / PER, ks = 9 * HK + kl, sel = PAIR ? (i >> 1) & 1 : i & 1, t = PAIR ? i & 1 : 0;
        constexpr int g = PAIR ? i : 2 * i;
        constexpr int x = g1_x_at(U, g), dm = g1_dma_at(U, g);
        if constexpr (x >= 0) { g1_ldx(xb[0][x], cx.xs, cx.lo, x); g1_ldx(xb[1][x], cx.xs + KS * 1024, cx.lo, x); }
        if constexpr (dm >= 0) g1_piece(cx, dm >> 3, dm & 7);
        if constexpr ((U == 0 || U == 2) && (i & 3) == 0) {
            constexpr int young = g1_x_younger(ks, U, g);
            asm volatile("s_waitcnt vmcnt(%2)" : "+v"(xb[0][ks]), "+v"(xb[1][ks]) : "n"(young));
        }
        constexpr bool SWAP = GRP == 0 && sel == 1 && t == 0;
        const bf16x8 fr = join(flo[i & 3], fhi[i & 3]);
        c[sel][t][0] = SWAP ? MFMA32K(xb[0][ks], fr, c[sel][t][0]) : MFMA32K(fr, xb[0][ks], c[sel][t][0]);
        SBAR();
        if constexpr (i + G1_AHEAD < N) flo[(i + G1_AHEAD) & 3] = trread(addr(i + G1_AHEAD));
        SBAR();
        c[sel][t][1] = SWAP ? MFMA32K(xb[1][ks], fr, c[sel][t][1]) : MFMA32K(fr, xb[1][ks], c[sel][t][1]);
        SBAR();
        if constexpr (i + G1_AHEAD < N) fhi[(i + G1_AHEAD) & 3] = trread(addr(i + G1_AHEAD) + 1024);
        SBAR();
        if constexpr (EPI && PAIR) {
            constexpr int sl = 36 * HK + i - 4;
            if constexpr (sl >= 0 && sl < 64) g1_epi_slice(sl, cprev, ep, hprev, cx.lo);
        } else if constexpr (EPI) {
            constexpr int sl = 2 * (18 * HK + i - 2);
            if constexpr (sl >= 0 && sl < 64) { g1_epi_slice(sl, cprev, ep, hprev, cx.lo); g1_epi_slice(sl + 1, cprev, ep, hprev, cx.lo); }
        }
        SBAR();
    });
}
DI void g1_epi_qkv(const f32x4 (&c)[2][2][2], char* ws, int h, int tb, int lane, lds_ptr stg) {
    typedef __attribute__((address_space(3))) u32x2* l64;
    typedef __attribute__((address_space(3))) unsigned* l32;
    typedef __attribute__((address_space(3))) unsigned short* l16;
    asm volatile("" : "+v"(lane));
    const int t = lane & 15, g4 = lane >> 4;
    const lds_ptr rec = stg + QREC;
    float kmax = 0.f;
#pragma unroll
    for (int nh = 0; nh < 2; ++nh) {
        const int r = 16 * nh + t;
        const unsigned fo = (r + 32 * (g4 >> 1)) * 16 + (g4 & 1) * 8;
        const f32x4 q = c[0][0][nh], k = c[0][1][nh], v = c[1][0][nh], e = c[1][1][nh];
        *(l64)(stg + fo) = (u32x2){cvtpk(q[0] * QSCALE, q[1] * QSCALE), cvtpk(q[2] * QSCALE, q[3] * QSCALE)};
        const u32x2 kp = {cvtpk(k[0], k[1]), cvtpk(k[2], k[3])};
        *(l64)(rec + fo) = kp;
        *(l64)(rec + 1280 + nh * 640 + (g4 & 1) * 320 + t * 16 + (g4 >> 1) * 8) = (u32x2){cvtpk(v[0], v[1]), cvtpk(v[2], v[3])};
        const unsigned e01 = cvtpk(e[0], e[1]), e23 = cvtpk(e[2], e[3]);
        const lds_ptr vt = rec + 1280 + nh * 640 + ((t >> 2) & 1) * 320 + (4 * (t >> 3) + (t & 3)) * 2;
        if (g4 == 0) {
            *(l64)(rec + 1024 + r * 8) = (u32x2){e23, 0u};
            *(l32)(stg + 1024 + r * 4) = cvtpk(e[0] * QSCALE, e[1] * QSCALE);
        } else {
            *(l16)(vt + (g4 == 1 ? 16 : 16 + g4) * 16) = (unsigned short)(g4 == 1 ? e01 : (g4 == 2 ? 0x3f80u : 0u));
            if (g4 == 1) *(l16)(vt + 17 * 16) = (unsigned short)(e01 >> 16);
        }
        float n2 = g4 == 0 ? bf_lo(e23) * bf_lo(e23) + bf_hi(e23) * bf_hi(e23) : 0.f;
        n2 += bf_lo(kp[0]) * bf_lo(kp[0]) + bf_hi(kp[0]) * bf_hi(kp[0]) + bf_lo(kp[1]) * bf_lo(kp[1]) + bf_hi(kp[1]) * bf_hi(kp[1]);
        n2 += swap16(n2);
        n2 += swap32(n2);
        kmax = fmaxf(kmax, n2);
    }
    kmax = rowmax16(kmax);
    if (lane == 0) *(l32)(stg + 1152) = __float_as_uint(kmax);
    typedef __attribute__((address_space(3))) const u32x4* l128;
    char* qg = ws + WS_Q + ((size_t)h * NTB + tb) * QREC;
    char* kg = ws + WS_KV + ((size_t)h * NTB + tb) * KVREC;
    const unsigned o0 = lane * 16, oq = min(lane, 8) * 16 + 1024, ok = min(lane, 31) * 16 + 2048;
    const u32x4 d0 = *(l128)(stg + o0), d1 = *(l128)(stg + oq), d2 = *(l128)(rec + o0), d3 = *(l128)(rec + 1024 + o0), d4 = *(l128)(rec + ok);
    gstore16(qg, o0, d0);
    gstore16(qg, oq, d1);
    gstore16(kg, o0, d2);
    gstore16(kg, o0 + 1024, d3);
    gstore16(kg, ok, d4);
}

__global__ __launch_bounds__(512, 2) void k_gemm1(const char* wg, const char* wu, const char* wd, char* ws) {
    extern __shared__ __attribute__((aligned(16))) char lds[];
    STAMP_BEGIN; DIAG_STAMP(1);
    const int tid = threadIdx.x, lane = tid & 63;
    const int w = __builtin_amdgcn_readfirstlane(tid >> 6);
    const int xcd = blockIdx.x & 7, idx = blockIdx.x >> 3;
    const int cg = 4 * xcd + (idx & 3), rg = idx >> 2, tb = 8 * rg + w;
    const bool late = w >= 4;
    MARKS; MARK(0);

    const int odd = cg & 1;
    G1Ctx cx;
    cx.w = w; cx.lds0 = (unsigned)(uintptr_t)lds; cx.lo = lane * 16;
    cx.fbase = 144 * cg + 16 * odd; cx.fsingle = odd ? 144 * cg - 16 : 144 * cg + 128;
    cx.wq = ws + WS_WQP + (size_t)cg * KS * 4096;
    cx.xs = ws + WS_XNF + (size_t)(2 * tb) * KS * 1024;
    cx.wrow = ((w & 1) ? wu : wg) + (size_t)(32 * (w >> 2) + 16 * ((w >> 1) & 1)) * (F * 2);
    cx.dsto = (w >> 2) * 4096 + (w & 1) * 2048 + ((w >> 1) & 1) * 1024;
    cx.voff = (lane >> 2) * (F * 2) + (((lane & 3) ^ (2 * ((lane >> 4) & 1))) * 16);
    cx.hdst = ws + WS_HF + ((size_t)(2 * tb) * FS + cx.fbase / 32) * 1024;

#pragma unroll
    for (int m = 0; m < 5; ++m) g1_piece(cx, 0, m);
    bf16x8 xb[2][KS];
#pragma unroll
    for (int s = 0; s < 6; ++s) { g1_ldx(xb[0][s], cx.xs, cx.lo, s); g1_ldx(xb[1][s], cx.xs + KS * 1024, cx.lo, s); }

    const int aoff0 = img_aoff(lane);
    const lds_cptr A0 = (lds_cptr)lds + aoff0, A1 = (lds_cptr)lds + (aoff0 ^ 32);
    f32x4 ca[2][2][2], cb[2][2][2];
    u32x4 dead0, dead1, dead2;

    WAITV_BAR(12);
    MARK(1);
    g1_unit<0>(cx, A0, A1, xb, ca, cb); WAITV_BAR(5);
    g1_unit<1>(cx, A0, A1, xb, cb, ca); WAITV_BAR(18);
    MARK(2);
    g1_unit<2>(cx, A0, A1, xb, ca, cb); WAITV_BAR(5);
    MARK(3);
    const lds_ptr stg = (lds_ptr)lds + 2 * G1_SLOT + w * 4608;
    if (!late) g1_epi_qkv(ca, ws, cg, tb, lane, stg);
    g1_unit<3>(cx, A0, A1, xb, cb, ca);
    if (late) { g1_epi_qkv(ca, ws, cg, tb, lane, stg); WAITV_BAR(10); } else { WAITV_BAR(5); }
    MARK(4);
    g1_unit<4>(cx, A0, A1, xb, ca, cb); WAITV_BAR(5);
    g1_unit<5>(cx, A0, A1, xb, ca, cb); WAITV_BAR(5);
    MARK(5);
    g1_unit<6>(cx, A0, A1, xb, cb, ca); WAITV_BAR(5);
    g1_unit<7>(cx, A0, A1, xb, cb, ca); WAITV_BAR(5);
    MARK(6);
    g1_unit<8>(cx, A0, A1, xb, ca, cb);
    WAITV_BAR(5);
    g1_unit<9>(cx, A0, A1, xb, ca, cb); WAITV_BAR(5);
    MARK(7);
    const lds_cptr AS = odd ? A1 : A0;
    g1_unit<10>(cx, AS, AS, xb, cb, ca); WAITV_BAR(0);
    g1_unit<11>(cx, AS, AS, xb, cb, ca);
    {
        char* hs = ws + WS_HF + ((size_t)(2 * tb) * FS + cx.fsingle / 32) * 1024 + odd * 8;
#pragma unroll
        for (int nh = 0; nh < 2; ++nh) {
            float h[4];
#pragma unroll
            for (int j = 0; j < 4; ++j) h[j] = glu(cb[0][0][nh][j], cb[1][0][nh][j]);
            gstore8(hs + (size_t)nh * FS * 1024, cx.lo, (u32x2){cvtpk(h[0], h[1]), cvtpk(h[2], h[3])});
        }
    }
    MARK(8); MARK_DUMP(1, 9);
    STAMP_END(1);
}

constexpr int AT_STAGE = 8 * KVREC;
constexpr float AT_BOUND_MAX = 50.0f;

struct KFrag { bf16x8 k1; bf16x4 k2; };
struct VFrag { bf16x8 v0, v1; };
DI KFrag at_readk(lds_cptr rec, int lane) {
    KFrag k;
    k.k1 = *(const __attribute__((address_space(3))) bf16x8*)(rec + lane * 16);
    k.k2 = *(const __attribute__((address_space(3))) bf16x4*)(rec + 1024 + (lane & 31) * 8);
    return k;
}
DI VFrag at_readv(lds_cptr recv) {
    VFrag v;
    v.v0 = *(const __attribute__((address_space(3))) bf16x8*)(recv);
    v.v1 = *(const __attribute__((address_space(3))) bf16x8*)(recv + 640);
    return v;
}
DI bf16x8 packp(const f32x16& p, int s) {
    return __builtin_bit_cast(bf16x8, (u32x4){cvtpk(p[8 * s], p[8 * s + 1]), cvtpk(p[8 * s + 2], p[8 * s + 3]), cvtpk(p[8 * s + 4], p[8 * s + 5]), cvtpk(p[8 * s + 6], p[8 * s + 7])});
}

DI void at_dma(const char* kvh, int stage, unsigned ldsb, int w, int lane) {
    const char* src = kvh + (size_t)stage * AT_STAGE + w * 5 * 1024;
    const unsigned dst = ldsb + w * 5 * 1024, lo = lane * 16;
    asm volatile("s_mov_b32 m0, %2\n\ts_nop 0\n\t"
                 "global_load_lds_dwordx4 %0, %1\n\tglobal_load_lds_dwordx4 %0, %1 offset:1024\n\t"
                 "global_load_lds_dwordx4 %0, %1 offset:2048\n\tglobal_load_lds_dwordx4 %0, %1 offset:3072"
                 :: "v"(lo), "s"(src), "s"(dst) : "memory");
    glds16(src + 4096, lo, dst + 4096);
}

#define AMFMA16_NEW(d, a, b, c) asm volatile("v_mfma_f32_32x32x16_bf16 %0, %1, %2, %3" : "=&v"(d) : "v"(a), "v"(b), "v"(c))
#define AMFMA16_ACC(d, a, b) asm volatile("v_mfma_f32_32x32x16_bf16 %0, %1, %2, %0" : "+v"(d) : "v"(a), "v"(b))
#define AMFMA8_ACC(d, a, b) asm volatile("v_mfma_f32_32x32x8_bf16 %0, %1, %2, %0" : "+v"(d) : "v"(a), "v"(b))
#define AT_EXP4(E, SX, a) do { _Pragma("unroll") for (int i_ = 0; i_ < 4; ++i_) asm volatile("v_exp_f32 %0, %1" : "=v"(E[i_]) : "v"(SX[a + i_])); } while (0)
#define AT_PK2(DST, k, E) do { asm volatile("v_cvt_pk_bf16_f32 %0, %1, %2" : "=v"(DST[k]) : "v"(E[0]), "v"(E[1]));   \
                               asm volatile("v_cvt_pk_bf16_f32 %0, %1, %2" : "=v"(DST[k + 1]) : "v"(E[2]), "v"(E[3])); } while (0)
#define AT_STEP(SX, PX0, PX1, SY, CY, QY1, QY2, KF, OY, PY0, PY1, VF, LOADS)                                   \
    do {                                                                                                       \
        float e0_[4], e1_[4], e2_[4];                                                                          \
        AMFMA16_NEW(SY, KF.k1, QY1, CY);                                                                       \
        LOADS;                                                                                                 \
        SBAR();                                                                                                \
        AT_EXP4(e0_, SX, 0);                                                                                   \
        AT_PK2(PY1, 2, etail);                                                                                 \
        AMFMA8_ACC(SY, KF.k2, QY2);                                                                            \
        AT_EXP4(e1_, SX, 4);                                                                                   \
        AT_PK2(PX0, 0, e0_);                                                                                   \
        AMFMA16_ACC(OY, VF.v0, PY0);                                                                           \
        AT_EXP4(e2_, SX, 8);                                                                                   \
        AT_PK2(PX0, 2, e1_);                                                                                   \
        AMFMA16_ACC(OY, VF.v1, PY1);                                                                           \
        AT_EXP4(etail, SX, 12);                                                                                \
        AT_PK2(PX1, 0, e2_);                                                                                   \
        SBAR();                                                                                                \
    } while (0)

DI void at_online(const KFrag& k, const VFrag& v, bf16x8 q1, bf16x4 q2, float& m, f32x16& o) {
    f32x16 z = {};
    f32x16 s = MFMA16(k.k1, q1, z);
    s = MFMA8(k.k2, q2, s);
    float t = s[0];
#pragma unroll
    for (int i = 1; i < 16; ++i) t = fmaxf(t, s[i]);
    t = fmaxf(t, swap32(t));
    const float mn = fmaxf(m, t);
    const float alpha = __builtin_amdgcn_exp2f(m - mn);
    m = mn;
#pragma unroll
    for (int i = 0; i < 16; ++i) { s[i] = __builtin_amdgcn_exp2f(s[i] - mn); o[i] *= alpha; }
    o = MFMA16(v.v0, packp(s, 0), o);
    o = MFMA16(v.v1, packp(s, 1), o);
}

DI void at_store(const f32x16& o, char* ws, int h, int tb, int lane) {
    const int r = lane & 31, hi = lane >> 5;
    const float other = swap32(o[10]);
    const float inv = 1.0f / (hi ? other : o[10]);
    char* of = ws + WS_OF + (size_t)(2 * tb + (r >> 4)) * KS * 1024;
    *(u32x4*)(of + (h >> 1) * 1024 + ((r & 15) + 16 * (2 * (h & 1) + hi)) * 16) =
        (u32x4){cvtpk(o[0] * inv, o[1] * inv), cvtpk(o[2] * inv, o[3] * inv), cvtpk(o[4] * inv, o[5] * inv), cvtpk(o[6] * inv, o[7] * inv)};
    if (hi == 0) *(unsigned*)(of + (16 + (h >> 4)) * 1024 + ((r & 15) + 16 * ((h >> 2) & 3)) * 16 + (h & 3) * 4) = cvtpk(o[8] * inv, o[9] * inv);
}

DI float at_qnorm2(bf16x8 q1, bf16x4 q2) {
    const u32x4 a = __builtin_bit_cast(u32x4, q1); const u32x2 b = __builtin_bit_cast(u32x2, q2);
    float n = bf_lo(b[0]) * bf_lo(b[0]) + bf_hi(b[0]) * bf_hi(b[0]);
#pragma unroll
    for (int j = 0; j < 4; ++j) n += bf_lo(a[j]) * bf_lo(a[j]) + bf_hi(a[j]) * bf_hi(a[j]);
    return n + swap32(n);
}

DI void attn_block(char* ws, char* lds, int bid) {
    const int tid = threadIdx.x, lane = tid & 63, hi = lane >> 5;
    const int w = __builtin_amdgcn_readfirstlane(tid >> 6);
    const int xcd = bid & 7, idx = bid >> 3;
    const int h = 4 * xcd + (idx & 3), qg = idx >> 2;
    const int tbA = 8 * qg + 2 * w, tbB = tbA + 1;
    const unsigned lds0 = (unsigned)(uintptr_t)lds;
    const char* kvh = ws + WS_KV + (size_t)h * NTB * KVREC;
    MARKS; MARK(0);

    bf16x8 qA1, qB1; unsigned qa2, qb2; float km;
    {
        const char* q1 = ws + WS_Q + ((size_t)h * NTB + tbA) * QREC;
        const char* kx = ws + WS_Q + (size_t)h * NTB * QREC + 1152;
        const unsigned l16 = lane * 16, l4 = (lane & 31) * 4 + 1024, lk = lane * QREC;
        asm volatile("global_load_dword %0, %1, %2" : "=v"(km) : "v"(lk), "s"(kx));
        asm volatile("global_load_dwordx4 %0, %1, %2" : "=v"(qA1) : "v"(l16), "s"(q1));
        asm volatile("global_load_dwordx4 %0, %1, %2 offset:%3" : "=v"(qB1) : "v"(l16), "s"(q1), "n"(QREC));
        asm volatile("global_load_dword %0, %1, %2" : "=v"(qa2) : "v"(l4), "s"(q1));
        asm volatile("global_load_dword %0, %1, %2 offset:%3" : "=v"(qb2) : "v"(l4), "s"(q1), "n"(QREC));
    }
    at_dma(kvh, 0, lds0, w, lane);
    at_dma(kvh, 1, lds0 + AT_STAGE, w, lane);
    asm volatile("s_waitcnt vmcnt(10)" : "+v"(km), "+v"(qA1), "+v"(qB1), "+v"(qa2), "+v"(qb2));
    const bf16x4 qA2 = __builtin_bit_cast(bf16x4, (u32x2){hi ? 0u : qa2, 0u});
    const bf16x4 qB2 = __builtin_bit_cast(bf16x4, (u32x2){hi ? 0u : qb2, 0u});
    km = rowmax16(km);
    km = fmaxf(km, swap16(km));
    km = fmaxf(km, swap32(km));
    const float mA = __builtin_amdgcn_sqrtf(at_qnorm2(qA1, qA2) * km) * 1.001f, mB = __builtin_amdgcn_sqrtf(at_qnorm2(qB1, qB2) * km) * 1.001f;
    const bool fast = __all(mA <= AT_BOUND_MAX && mB <= AT_BOUND_MAX);

    const int voffs = 1280 + hi * 320 + min(lane & 31, 19) * 16;
    const lds_cptr L = (lds_cptr)lds;
    f32x16 oA = {}, oB = {};

    if (fast) {
        f32x16 cA, cB;
#pragma unroll
        for (int i = 0; i < 16; ++i) { cA[i] = -mA; cB[i] = -mB; }
        asm volatile("" : "+v"(cA), "+v"(cB));
        f32x16 sA, sB;
        u32x4 pA0 = {}, pA1 = {}, pB0 = {}, pB1 = {};
        float etail[4] = {0.f, 0.f, 0.f, 0.f};
        KFrag k0, k1; VFrag v0, v1;
        MARK(1);
        WAITV_BAR(5);
        MARK(2);
        k0 = at_readk(L, lane);
        v1 = at_readv(L + voffs);
        sA = MFMA16(k0.k1, qA1, cA); sA = MFMA8(k0.k2, qA2, sA);
        asm volatile("s_nop 15" : "+v"(sA));
        int slot = 0;
        for (int st = 0; st < 8; ++st) {
            const lds_cptr cur = L + slot * AT_STAGE;
            const int nslot = slot == 2 ? 0 : slot + 1;
#pragma unroll
            for (int t = 0; t < 8; t += 2) {
                AT_STEP(sA, pA0, pA1, sB, cB, qB1, qB2, k0, oB, pB0, pB1, v1,
                        (k1 = at_readk(cur + (t + 1) * KVREC, lane), v0 = at_readv(cur + t * KVREC + voffs)));
                AT_STEP(sB, pB0, pB1, sA, cA, qA1, qA2, k1, oA, pA0, pA1, v0, (void)0);
                lds_cptr nk = cur + (t + 2) * KVREC;
                if (t == 6) {
                    if (st < 7) {
                        if (st < 6) { WAITV_BAR(0); at_dma(kvh, st + 2, lds0 + (nslot == 2 ? 0 : nslot + 1) * AT_STAGE, w, lane); }
                        else WAITV_BAR(0);
                        nk = L + nslot * AT_STAGE;
                    } else nk = cur;
                }
                AT_STEP(sA, pA0, pA1, sB, cB, qB1, qB2, k1, oB, pB0, pB1, v0,
                        (k0 = at_readk(nk, lane), v1 = at_readv(cur + (t + 1) * KVREC + voffs)));
                AT_STEP(sB, pB0, pB1, sA, cA, qA1, qA2, k0, oA, pA0, pA1, v1, (void)0);
            }
            slot = nslot;
            MARK(3 + st);
        }
        asm volatile("s_nop 1");
        AT_PK2(pB1, 2, etail);
        asm volatile("s_nop 1" : "+v"(pB1));
        AMFMA16_ACC(oB, v1.v0, pB0);
        AMFMA16_ACC(oB, v1.v1, pB1);
        asm volatile("s_nop 15\n\ts_nop 7" : "+v"(oA), "+v"(oB));
    } else {
        float mxA = -INFINITY, mxB = -INFINITY;
        WAITV_BAR(5);
        int slot = 0;
        for (int st = 0; st < 8; ++st) {
            const lds_cptr cur = L + slot * AT_STAGE;
            const int nslot = slot == 2 ? 0 : slot + 1;
            for (int t = 0; t < 8; ++t) {
                const KFrag k = at_readk(cur + t * KVREC, lane);
                const VFrag v = at_readv(cur + t * KVREC + voffs);
                at_online(k, v, qA1, qA2, mxA, oA);
                at_online(k, v, qB1, qB2, mxB, oB);
            }
            if (st < 7) {
                WAITV_BAR(0);
                if (st < 6) at_dma(kvh, st + 2, lds0 + (nslot == 2 ? 0 : nslot + 1) * AT_STAGE, w, lane);
            }
            slot = nslot;
        }
    }
    at_store(oA, ws, h, tbA, lane);
    at_store(oB, ws, h, tbB, lane);
    MARK(11); MARK_DUMP(2, 12);
}

__global__ __launch_bounds__(256, 2) void k_attn(char* ws) {
    extern __shared__ __attribute__((aligned(16))) char lds[];
    STAMP_BEGIN;
    attn_block(ws, lds, blockIdx.x);
    STAMP_END(2);
}

constexpr int DN_STEP = 32 * 288, DN_SLOT = 2 * DN_STEP, DN_LDS = 4 * DN_SLOT, DN_NST = 18;
constexpr int dn_nb(int st) { return st + 2 >= 0 && st + 2 < DN_NST ? 4 : 0; }
constexpr int dn_nd(int st) { return st + 3 >= 0 && st + 3 < DN_NST ? 5 : 0; }

DI void dn_ldb(bf16x8 (&b)[2][2], const char* hs, unsigned lo, int st) {
#pragma unroll
    for (int k = 0; k < 2; ++k) {
        asm volatile("global_load_dwordx4 %0, %1, %2 offset:%3" : "=v"(b[0][k]) : "v"(lo), "s"(hs + (size_t)st * 2048), "n"(k * 1024));
        asm volatile("global_load_dwordx4 %0, %1, %2 offset:%3" : "=v"(b[1][k]) : "v"(lo), "s"(hs + (size_t)FS * 1024 + (size_t)st * 2048), "n"(k * 1024));
    }
}
template <int N> DI void dn_wait(bf16x8 (&b)[2][2]) {
    asm volatile("s_waitcnt vmcnt(%4) lgkmcnt(0)\n\ts_barrier" : "+v"(b[0][0]), "+v"(b[0][1]), "+v"(b[1][0]), "+v"(b[1][1]) : "n"(N) : "memory");
}

DI void down_block(const char* wd, char* ws, char* lds, int bid) {
    const int tid = threadIdx.x, lane = tid & 63;
    const int w = __builtin_amdgcn_readfirstlane(tid >> 6);
    const int xcd = bid & 7, idx = bid >> 3;
    const int kq = xcd >> 1, tg = (xcd & 1) * 8 + (idx >> 2), dq = idx & 3;
    const int tb16 = 8 * tg + 2 * w, fs0 = 36 * kq;
    const unsigned lds0 = (unsigned)(uintptr_t)lds;
    unsigned voff[5], doff[5];
#pragma unroll
    for (int m = 0; m < 5; ++m) { const int i = min(5 * (w & 1) + m, 8), n = 64 * i + lane; voff[m] = (n / 18) * (D * 2) + (n % 18) * 16; doff[m] = (w >> 1) * DN_STEP + i * 1024; }
    const char* wsrc = wd + (size_t)(fs0 + (w >> 1)) * 32 * (D * 2) + dq * 288;
    const lds_cptr A = (lds_cptr)lds + (4 * (lane >> 4) + ((lane & 15) >> 2)) * 288 + (lane & 3) * 8;
    const char* hs = ws + WS_HF + ((size_t)tb16 * FS + fs0) * 1024;
    const unsigned lo = lane * 16;
    MARKS; MARK(0);

    f32x4 acc[9][2];
#pragma unroll
    for (int i = 0; i < 9; ++i) { acc[i][0] = (f32x4){}; acc[i][1] = (f32x4){}; }
    bf16x8 b[3][2][2];
    auto dma = [&](int st, int m) { glds16(wsrc + (size_t)st * 2 * 32 * (D * 2), voff[m], lds0 + (st & 3) * DN_SLOT + doff[m]); };
#pragma unroll
    for (int m = 0; m < 5; ++m) dma(0, m);
    dn_ldb(b[0], hs, lo, 0);
#pragma unroll
    for (int m = 0; m < 5; ++m) dma(1, m);
    dn_ldb(b[1], hs, lo, 1);
#pragma unroll
    for (int m = 0; m < 5; ++m) dma(2, m);
    static_for<0, DN_NST>([&](auto stc) {
        constexpr int st = decltype(stc)::value, set = st % 3;
        constexpr int young = dn_nd(st - 2) + dn_nb(st - 1) + dn_nd(st - 1);
        dn_wait<young>(b[set]);
        if constexpr (dn_nb(st) > 0) dn_ldb(b[(st + 2) % 3], hs, lo, st + 2);
        lds_cptr a = A + (st & 3) * DN_SLOT;
        asm volatile("" : "+v"(a));
        bf16x4 flo[4], fhi[4];
        auto addr = [&](int i) { return a + (i / 9) * DN_STEP + (i % 9) * 32; };
#pragma unroll
        for (int i = 0; i < 3; ++i) { flo[i] = trread(addr(i)); fhi[i] = trread(addr(i) + 16 * 288); }
#pragma unroll
        for (int i = 0; i < 18; ++i) {
            if (dn_nd(st) > 0 && (i % 3) == 0 && i / 3 < 5) dma(st + 3, i / 3);
            const bf16x8 fr = join(flo[i & 3], fhi[i & 3]);
            acc[i % 9][0] = MFMA32K(fr, b[set][0][i / 9], acc[i % 9][0]);
            SBAR();
            if (i + 3 < 18) flo[(i + 3) & 3] = trread(addr(i + 3));
            SBAR();
            acc[i % 9][1] = MFMA32K(fr, b[set][1][i / 9], acc[i % 9][1]);
            SBAR();
            if (i + 3 < 18) fhi[(i + 3) & 3] = trread(addr(i + 3) + 16 * 288);
            SBAR();
        }
    });
    MARK(1);
    char* dst = ws + WS_PART + (((size_t)kq * NT16 + tb16) * 36 + 9 * dq) * 1024;
#pragma unroll
    for (int nh = 0; nh < 2; ++nh)
#pragma unroll
        for (int i = 0; i < 9; ++i) gstore16(dst + (size_t)nh * 36 * 1024 + i * 1024, lo, __builtin_bit_cast(u32x4, acc[i][nh]));
    MARK(2);
    asm volatile("s_waitcnt vmcnt(0)\n\ts_barrier" ::: "memory");
    if (w == 0) {
        typedef __attribute__((address_space(1))) unsigned gu32;
        gu32* cnt = (gu32*)(ws + WS_CNT + (tg * 4 + dq) * 256);
        if (lane == 0) __hip_atomic_fetch_add(cnt, 1u, __ATOMIC_RELAXED, __HIP_MEMORY_SCOPE_AGENT);
        for (int spins = 0; spins < (1 << 20); ++spins) {
            if ((unsigned)__builtin_amdgcn_readfirstlane(__hip_atomic_load(cnt, __ATOMIC_RELAXED, __HIP_MEMORY_SCOPE_AGENT)) >= 4u) break;
            __builtin_amdgcn_s_sleep(4);
        }
        __builtin_amdgcn_fence(__ATOMIC_ACQUIRE, "agent");
        asm volatile("s_waitcnt vmcnt(0)" ::: "memory");
    }
    asm volatile("s_barrier" ::: "memory");
    MARK(3);
    f32x4 pq[5][4];
#pragma unroll
    for (int m = 0; m < 5; ++m) {
        const int idx = min(w + 4 * m, 17), nh = idx >= 9, i = idx - 9 * nh;
        const char* pp = ws + WS_PART + ((size_t)(8 * tg + 2 * kq + nh) * 36 + 9 * dq + i) * 1024 + lo;
#pragma unroll
        for (int k = 0; k < 4; ++k) pq[m][k] = *(const f32x4*)(pp + (size_t)k * NT16 * 36 * 1024);
    }
    asm volatile("" ::: "memory");
    SBAR();
#pragma unroll
    for (int m = 0; m < 5; ++m) {
        const int idx = min(w + 4 * m, 17), nh = idx >= 9, i = idx - 9 * nh;
        const f32x4 ffn = (pq[m][0] + pq[m][1]) + (pq[m][2] + pq[m][3]);
        float g[4];
#pragma unroll
        for (int c = 0; c < 4; ++c) { const float f = rbf(ffn[c]); g[c] = f * __builtin_amdgcn_rcpf(1.0f + __builtin_amdgcn_exp2f(f * -LOG2E)); }
        *(u32x2*)(ws + WS_G + ((size_t)(8 * tg + 2 * kq + nh) * 36 + 9 * dq + i) * 512 + lane * 8) = (u32x2){cvtpk(g[0], g[1]), cvtpk(g[2], g[3])};
    }
    MARK(4); MARK_DUMP(3, 5);
}

__global__ __launch_bounds__(256, 2) void k_down(const char* wd, char* ws) {
    extern __shared__ __attribute__((aligned(16))) char lds[];
    STAMP_BEGIN;
    down_block(wd, ws, lds, blockIdx.x);
    STAMP_END(3);
}

#define AT_PRIO 2
#define MIX_MODE 0
__global__ __launch_bounds__(256, 2) void k_mix(const char* wd, char* ws) {
    extern __shared__ __attribute__((aligned(16))) char lds[];
    STAMP_BEGIN; DIAG_STAMP(2);
    const int b = blockIdx.x;
    const bool attn = b < 256; const int bid = b & 255;
    if (attn) { __builtin_amdgcn_s_setprio(AT_PRIO); attn_block(ws, lds, bid); } else down_block(wd, ws, lds, bid);
    STAMP_END(5);
}

constexpr int FN_LDS = 3 * KS * 1024;
__global__ __launch_bounds__(256, 1) void k_final(const unsigned short* x, char* ws, unsigned short* out) {
    extern __shared__ __attribute__((aligned(16))) char lds[];
    STAMP_BEGIN;
    const int tid = threadIdx.x, lane = tid & 63;
    const int w = __builtin_amdgcn_readfirstlane(tid >> 6);
    const int xcd = blockIdx.x & 7, idx = blockIdx.x >> 3;
    const int tg = 2 * xcd + idx / 12, dg = idx % 12, tb16 = 8 * tg + 2 * w;
    const unsigned lds0 = (unsigned)(uintptr_t)lds, lo = lane * 16;
    MARKS; MARK(0);
    const unsigned long long t3 = __builtin_amdgcn_s_memrealtime();
    {
        const char* src = ws + WS_WOP + (size_t)dg * FN_LDS;
#pragma unroll
        for (int i = 0; i < 14; ++i) { const int n = min(w * 14 + i, 3 * KS - 1); glds16(src + n * 1024, lo, lds0 + n * 1024); }
    }
    const char* os = ws + WS_OF + (size_t)tb16 * KS * 1024 + lo;
    bf16x8 b[2][KS];
#pragma unroll
    for (int s = 0; s < KS; ++s) { b[0][s] = *(const bf16x8*)(os + s * 1024); b[1][s] = *(const bf16x8*)(os + (KS + s) * 1024); }
    u32x2 gv[2][3], xv[2][3];
#pragma unroll
    for (int nh = 0; nh < 2; ++nh)
#pragma unroll
        for (int t = 0; t < 3; ++t) {
            gv[nh][t] = *(const u32x2*)(ws + WS_G + ((size_t)(tb16 + nh) * 36 + 3 * dg + t) * 512 + lane * 8);
            xv[nh][t] = *(const u32x2*)(x + (size_t)((tb16 + nh) * 16 + (lane & 15)) * D + dg * 48 + 16 * t + 4 * (lane >> 4));
        }
    MARK(1);
    asm volatile("s_waitcnt vmcnt(48)\n\ts_barrier" ::: "memory");
    MARK(2);
    SBAR();
    const lds_cptr A = (lds_cptr)lds + lo;
    f32x4 acc[2][3] = {};
#pragma unroll
    for (int s = 0; s < KS; ++s)
#pragma unroll
        for (int t = 0; t < 3; ++t) {
            const bf16x8 a = *(const __attribute__((address_space(3))) bf16x8*)(A + (t * KS + s) * 1024);
            acc[0][t] = MFMA32K(a, b[0][s], acc[0][t]);
            acc[1][t] = MFMA32K(a, b[1][s], acc[1][t]);
        }
    MARK(3);
#pragma unroll
    for (int nh = 0; nh < 2; ++nh)
#pragma unroll
        for (int t = 0; t < 3; ++t) {
            float o[4];
#pragma unroll
            for (int c = 0; c < 4; ++c) {
                const float g = (c & 1) ? bf_hi(gv[nh][t][c >> 1]) : bf_lo(gv[nh][t][c >> 1]);
                const float xx = (c & 1) ? bf_hi(xv[nh][t][c >> 1]) : bf_lo(xv[nh][t][c >> 1]);
                o[c] = xx + rbf(rbf(acc[nh][t][c]) * g);
            }
            *(u32x2*)(out + (size_t)((tb16 + nh) * 16 + (lane & 15)) * D + dg * 48 + 16 * t + 4 * (lane >> 4)) = (u32x2){cvtpk(o[0], o[1]), cvtpk(o[2], o[3])};
        }
    MARK(4);
    if (blockIdx.x == 0 && threadIdx.x < 64 && DIAG > 0) {
        const unsigned long long* st = (const unsigned long long*)(ws + WS_DIAG);
        const unsigned long long t4 = __builtin_amdgcn_s_memrealtime();
        const unsigned long long t0 = st[0], t1 = st[1], t2 = st[2];
        unsigned long long dl = DIAG == 1 ? t1 - t0 : DIAG == 2 ? t2 - t1 : DIAG == 3 ? t3 - t2 : t4 - t3;
        if (dl > 5000) dl = 5000;
        while (__builtin_amdgcn_s_memrealtime() - t4 < 10 * dl) __builtin_amdgcn_s_sleep(8);
    }
    MARK(5); MARK_DUMP(4, 6);
    STAMP_END(4);
}

extern "C" void kernel_launch(void* const* d_in, const int* in_sizes, int n_in, void* d_out, int out_size, void* d_ws, size_t ws_size, hipStream_t stream) {
    const unsigned short* x = (const unsigned short*)d_in[0];
    const unsigned short* wqkv = (const unsigned short*)d_in[1];
    const char* wo = (const char*)d_in[2];
    const char* wg = (const char*)d_in[4];
    const char* wu = (const char*)d_in[5];
    const char* wd = (const char*)d_in[6];
    char* ws = (char*)d_ws;
    (void)hipFuncSetAttribute((const void*)k_gemm1, hipFuncAttributeMaxDynamicSharedMemorySize, 4 * G1_SLOT);
    (void)hipFuncSetAttribute((const void*)k_attn, hipFuncAttributeMaxDynamicSharedMemorySize, 3 * AT_STAGE);
    (void)hipFuncSetAttribute((const void*)k_down, hipFuncAttributeMaxDynamicSharedMemorySize, DN_LDS);
    k_prep<<<PREP_PF + PREP_WO + PREP_LN + PREP_WQ, 256, 0, stream>>>(x, wqkv, (const unsigned short*)wo, wg, wu, ws);
    k_gemm1<<<256, 512, 4 * G1_SLOT, stream>>>(wg, wu, wd, ws);
    (void)hipFuncSetAttribute((const void*)k_mix, hipFuncAttributeMaxDynamicSharedMemorySize, DN_LDS);
    k_mix<<<512, 256, DN_LDS, stream>>>(wd, ws);
    (void)hipFuncSetAttribute((const void*)k_final, hipFuncAttributeMaxDynamicSharedMemorySize, FN_LDS);
    k_final<<<192, 256, FN_LDS, stream>>>(x, ws, (unsigned short*)d_out);
}
```

```cpp
#define NO_PF0
#define DIAG 0
#include <hip/hip_runtime.h>
#include <hip/hip_bf16.h>
#include <stdint.h>
#include <type_traits>

typedef __attribute__((ext_vector_type(8))) short bf16x8;
typedef __attribute__((ext_vector_type(4))) short bf16x4;
typedef __attribute__((ext_vector_type(2))) __bf16 bf16p;
typedef __attribute__((ext_vector_type(2))) float f32x2;
typedef __attribute__((ext_vector_type(4))) float f32x4;
typedef __attribute__((ext_vector_type(16))) float f32x16;
typedef __attribute__((ext_vector_type(2))) unsigned u32x2;
typedef __attribute__((ext_vector_type(4))) unsigned u32x4;
typedef __attribute__((address_space(3))) const char* lds_cptr;
typedef __attribute__((address_space(3))) char* lds_ptr;
typedef short v4i16_t __attribute__((ext_vector_type(4)));

#define DI __device__ __forceinline__
#define MFMA16(a, b, c) __builtin_amdgcn_mfma_f32_32x32x16_bf16((a), (b), (c), 0, 0, 0)
#define MFMA32K(a, b, c) __builtin_amdgcn_mfma_f32_16x16x32_bf16((a), (b), (c), 0, 0, 0)
#define MFMA8(a, b, c) __builtin_amdgcn_mfma_f32_32x32x8bf16_1k((a), (b), (c), 0, 0, 0)
#define SBAR() __builtin_amdgcn_sched_barrier(0)

template <int I, int N, class Fn> DI void static_for(Fn&& fn) {
    if constexpr (I < N) { fn(std::integral_constant<int, I>{}); static_for<I + 1, N>(fn); }
}

constexpr int S = 2048, D = 576, NH = 32, DH = 18, F = 4608;
constexpr int NTB = S / 32;
constexpr int NT16 = S / 16;
constexpr int KS = D / 32;
constexpr int FS = F / 32;

constexpr int KVREC = 2560;
constexpr size_t WS_XNF = 0;
constexpr size_t WS_WQP = WS_XNF + (size_t)NT16 * KS * 1024;
constexpr int QREC = 1280;
constexpr size_t WS_WOP = WS_WQP + (size_t)NH * KS * 4096;
constexpr size_t WS_Q = WS_WOP + (size_t)36 * KS * 1024;
constexpr size_t WS_KV = WS_Q + (size_t)NH * NTB * QREC;
constexpr size_t WS_OF = WS_KV + (size_t)NH * NTB * KVREC;
constexpr size_t WS_HF = WS_OF + (size_t)NT16 * KS * 1024;
constexpr size_t WS_PART = WS_HF + (size_t)NT16 * FS * 1024;
constexpr size_t WS_G = WS_PART + (size_t)4 * NT16 * 36 * 1024;
constexpr size_t WS_CNT = WS_G + (size_t)NT16 * 36 * 512;
constexpr size_t WS_END = WS_CNT + 64 * 256;

#define STAMP_BEGIN
#define STAMP_END(k)
#define MARKS
#define MARK(i)
#define MARK_DUMP(k, n)

constexpr size_t WS_DIAG = 190u << 20;
DI void diag_stamp(char* ws, int k, int blk) {
    if ((int)blockIdx.x == blk && threadIdx.x == 0) { const unsigned long long t = __builtin_amdgcn_s_memrealtime(); __hip_atomic_store((unsigned long long*)(ws + WS_DIAG) + k, t, __ATOMIC_RELAXED, __HIP_MEMORY_SCOPE_AGENT); }
}
constexpr float LOG2E = 1.4426950408889634f;
constexpr float QSCALE = 0.23570226039551584f * LOG2E;

DI unsigned cvtpk(float lo, float hi) { return __builtin_bit_cast(unsigned, __builtin_convertvector((f32x2){lo, hi}, bf16p)); }
DI float bf_lo(unsigned u) { return __uint_as_float(u << 16); }
DI float bf_hi(unsigned u) { return __uint_as_float(u & 0xffff0000u); }
DI float rbf(float x) { return bf_lo(cvtpk(x, x)); }
DI bf16x4 trread(lds_cptr p) { return __builtin_bit_cast(bf16x4, __builtin_amdgcn_ds_read_tr16_b64_v4i16((__attribute__((address_space(3))) v4i16_t*)p)); }
DI bf16x8 trfrag(lds_cptr p0, lds_cptr p1) { const bf16x4 a = trread(p0), b = trread(p1); return (bf16x8){a[0], a[1], a[2], a[3], b[0], b[1], b[2], b[3]}; }
DI bf16x8 join(bf16x4 a, bf16x4 b) { return (bf16x8){a[0], a[1], a[2], a[3], b[0], b[1], b[2], b[3]}; }
DI float swap16(float x) {
    auto r = __builtin_amdgcn_permlane16_swap(__float_as_uint(x), __float_as_uint(x), false, false);
    return __uint_as_float((threadIdx.x & 16) ? r[0] : r[1]);
}
template <int CTRL> DI float dppf(float x) { return __uint_as_float(__builtin_amdgcn_update_dpp(0u, __float_as_uint(x), CTRL, 0xf, 0xf, true)); }
DI float rowmax16(float x) {
    x = fmaxf(x, dppf<0xB1>(x));
    x = fmaxf(x, dppf<0x4E>(x));
    x = fmaxf(x, dppf<0x141>(x));
    return fmaxf(x, dppf<0x140>(x));
}
DI float swap32(float x) { auto r = __builtin_amdgcn_permlane32_swap(__float_as_uint(x), __float_as_uint(x), false, false); return __uint_as_float((threadIdx.x & 32) ? r[0] : r[1]); }

DI void glds16(const void* sbase, unsigned voff, unsigned lds_base) {
    asm volatile("s_mov_b32 m0, %2\n\ts_nop 0\n\tglobal_load_lds_dwordx4 %0, %1" :: "v"(voff), "s"(sbase), "s"(lds_base) : "memory");
}
#define STPOL "sc1"
DI void gstore16(void* sbase, unsigned voff, u32x4 v) { asm volatile("global_store_dwordx4 %0, %1, %2 " STPOL "\n\ts_nop 1" :: "v"(voff), "v"(v), "s"(sbase) : "memory"); }
DI void gstore8(void* sbase, unsigned voff, u32x2 v) { asm volatile("global_store_dwordx2 %0, %1, %2" :: "v"(voff), "v"(v), "s"(sbase) : "memory"); }
DI void gstore4(void* sbase, unsigned voff, unsigned v) { asm volatile("global_store_dword %0, %1, %2" :: "v"(voff), "v"(v), "s"(sbase) : "memory"); }
#define WAITV(n) asm volatile("s_waitcnt vmcnt(" #n ")" ::: "memory")
#define WAITV_BAR(n) asm volatile("s_waitcnt vmcnt(" #n ") lgkmcnt(0)\n\ts_barrier" ::: "memory")

DI int wo_row(int ks, int kg, int j) {
    return ks < 16 ? DH * (2 * ks + (kg >> 1)) + 8 * (j >> 2) + 4 * (kg & 1) + (j & 3) : DH * (16 * (ks - 16) + 4 * kg + (j >> 1)) + 16 + (j & 1);
}
constexpr int PREP_PF = 64, PREP_WO = 162, PREP_LN = 256, PREP_WQ = 144;

__global__ __launch_bounds__(256) void k_prep(const unsigned short* __restrict__ x, const unsigned short* __restrict__ wqkv, const unsigned short* __restrict__ wo,
                                              const char* __restrict__ wg, const char* __restrict__ wu, char* __restrict__ ws) {
    STAMP_BEGIN;
    const int tid = threadIdx.x;
    if (blockIdx.x < PREP_PF) {
    } else if (blockIdx.x < PREP_PF + PREP_WO) {
        const int n = (blockIdx.x - PREP_PF) * 256 + tid;
        const int lane = n & 63, fs = n >> 6, ks = fs % KS, dt = fs / KS;
        unsigned short v[8];
#pragma unroll
        for (int j = 0; j < 8; ++j) v[j] = wo[(size_t)wo_row(ks, lane >> 4, j) * D + 16 * dt + (lane & 15)];
        *(u32x4*)(ws + WS_WOP + (size_t)n * 16) = (u32x4){v[0] | ((unsigned)v[1] << 16), v[2] | ((unsigned)v[3] << 16), v[4] | ((unsigned)v[5] << 16), v[6] | ((unsigned)v[7] << 16)};
    } else if (blockIdx.x < PREP_PF + PREP_WO + PREP_LN) {
        if (blockIdx.x == PREP_PF + PREP_WO && tid < 64) *(unsigned*)(ws + WS_CNT + tid * 256) = 0u;
        const int row = (blockIdx.x - (PREP_PF + PREP_WO)) * 8 + (tid >> 5), t = tid & 31;
        const u32x4* xr = (const u32x4*)(x + (size_t)row * D);
        u32x4 v[3];
        v[0] = xr[t]; v[1] = xr[t + 32]; v[2] = (t < 8) ? xr[t + 64] : (u32x4){0u, 0u, 0u, 0u};
        float sum = 0.f;
#pragma unroll
        for (int i = 0; i < 3; ++i)
#pragma unroll
            for (int j = 0; j < 4; ++j) sum += bf_lo(v[i][j]) + bf_hi(v[i][j]);
#pragma unroll
        for (int o = 16; o > 0; o >>= 1) sum += __shfl_xor(sum, o);
        const float mean = sum * (1.0f / D);
        float sq = 0.f;
#pragma unroll
        for (int i = 0; i < 3; ++i)
            if (i < 2 || t < 8)
#pragma unroll
                for (int j = 0; j < 4; ++j) { const float a = bf_lo(v[i][j]) - mean, b = bf_hi(v[i][j]) - mean; sq += a * a + b * b; }
#pragma unroll
        for (int o = 16; o > 0; o >>= 1) sq += __shfl_xor(sq, o);
        const float mb = rbf(mean);
        const float den = rbf(__fsqrt_rn(rbf(rbf(sq * (1.0f / D)) + 1e-5f)));
        char* dst = ws + WS_XNF + (size_t)(row >> 4) * KS * 1024 + (row & 15) * 16;
#pragma unroll
        for (int i = 0; i < 3; ++i) {
            if (i < 2 || t < 8) {
                const int p = t + 32 * i;
                unsigned o[4];
#pragma unroll
                for (int j = 0; j < 4; ++j) o[j] = cvtpk(__fdiv_rn(rbf(bf_lo(v[i][j]) - mb), den), __fdiv_rn(rbf(bf_hi(v[i][j]) - mb), den));
                char* d = dst + (p >> 2) * 1024 + (p & 1) * 512 + ((p >> 1) & 1) * 8;
                *(u32x2*)d = (u32x2){o[0], o[1]};
                *(u32x2*)(d + 256) = (u32x2){o[2], o[3]};
            }
        }
    } else {
        const int base = (blockIdx.x - (PREP_PF + PREP_WO + PREP_LN)) * 1024 + tid;
#pragma unroll
        for (int i = 0; i < 4; ++i) {
            const int n = base + 256 * i;
            const int pc = n & 3, r = (n >> 2) & 31, sel = (n >> 7) & 1, hs = n >> 8, s = hs % KS, h = hs / KS;
            const int c = pc ^ (2 * ((r >> 2) & 1)), t = c >> 1, g = c & 1;
            const unsigned* w = (const unsigned*)(wqkv + (size_t)(32 * s + r) * (3 * D) + DH * h);
            u32x4 o = {0u, 0u, 0u, 0u};
            if (sel == 0 || t == 0) {
                const int col = (sel == 0 ? t * D : 2 * D) + 8 * g;
                o = (u32x4){w[col / 2], w[col / 2 + 1], w[col / 2 + 2], w[col / 2 + 3]};
            } else if (g == 0) {
                o = (u32x4){w[8], w[D / 2 + 8], w[D + 8], 0u};
            }
            *(u32x4*)(ws + WS_WQP + (size_t)n * 16) = o;
        }
    }
    STAMP_END(0);
}

constexpr int G1_SLOT = 9 * 4096;
constexpr int G1_AHEAD = 3;

DI int img_aoff(int lane) {
    const int kg = lane >> 4, q = (lane & 15) >> 2, p = lane & 3;
    return (4 * kg + q) * 64 + (((p >> 1) ^ (2 * (kg & 1))) * 16) + 8 * (p & 1);
}
DI void g1_ldx(bf16x8& d, const char* base, unsigned lo, int ks) {
    asm volatile("global_load_dwordx4 %0, %1, %2 offset:%3" : "=v"(d) : "v"(lo), "s"(base + (ks >> 2) * 4096), "n"((ks & 3) * 1024));
}
DI float glu(float g, float u) { return u * __builtin_amdgcn_rcpf(1.0f + __builtin_amdgcn_exp2f(g * -LOG2E)); }

constexpr int g1_group(int u) { return u < 4 ? (u & 1) : u / 2; }
constexpr int g1_half(int u) { return u < 4 ? (u >> 1) : (u & 1); }
constexpr int g1_dma_at(int u, int g) {
    if (u == 0) return (g >= 12 && g <= 20 && !(g & 1)) ? 1 * 8 + (g - 12) / 2 : (g >= 22 && g <= 30 && !(g & 1)) ? 2 * 8 + (g - 22) / 2 : -1;
    if (u == 1) return -1;
    if (u == 2) return (g <= 16 && !(g & 3)) ? 3 * 8 + g / 4 : (g >= 20 && g <= 28 && !(g & 1)) ? 4 * 8 + (g - 20) / 2 : -1;
    if (u <= 9) { const int sp = 5; return (g % sp == 0 && g / sp < 5) ? (u + 2) * 8 + g / sp : -1; }
    return -1;
}
constexpr int g1_x_at(int u, int g) {
    if (g & 3) return -1;
    if (u == 0) return g / 4 < 3 ? 6 + g / 4 : -1;
    if (u == 1) return 9 + g / 4;
    return -1;
}
constexpr int g1_x_younger(int ks, int u, int g) {
    int n = 0; bool seen = ks < 6;
    if (ks < 6) n += 2 * (5 - ks);
    for (int uu = 0; uu <= u; ++uu)
        for (int gg = 0; gg < 36 && (uu < u || gg <= g); ++gg) {
            const int x = g1_x_at(uu, gg);
            if (x >= 0) { if (seen) n += 2; if (x == ks) seen = true; }
            if (g1_dma_at(uu, gg) >= 0 && seen) n += 1;
        }
    return n;
}

struct G1Ctx {
    const char *wrow, *wq, *xs;
    char* hdst;
    unsigned lds0, dsto, voff, lo;
    int fbase, fsingle, w;
};
DI void g1_piece(const G1Ctx& c, int unit, int m) {
    const int grp = g1_group(unit), hk = g1_half(unit);
    const unsigned slot = c.lds0 + (unit & 3) * G1_SLOT;
    const int mm = (m == 4 && c.w >= 4) ? 3 : m;
    if (grp == 0) {
        const int pi = c.w + 8 * mm;
        glds16(c.wq + hk * G1_SLOT + pi * 1024, c.lo, slot + pi * 1024);
    } else {
        const int f0 = grp == 5 ? c.fsingle : c.fbase + 32 * (grp - 1);
        glds16(c.wrow + (size_t)(288 * hk + 64 * mm) * (F * 2) + f0 * 2, c.voff, slot + c.dsto + mm * 8192);
    }
}

struct G1Epi { float e[2], h[2]; u32x4 pk; };
DI void g1_epi_slice(int s, const f32x4 (&c)[2][2][2], G1Epi& t, char* hdst, unsigned lo) {
    const int v = s >> 2, nh = v >> 3, j = v & 7, ph = s & 3;
    if (ph == 0) { t.e[v & 1] = __builtin_amdgcn_exp2f(c[0][j >> 2][nh][j & 3] * -LOG2E); asm volatile("" : "+v"(t.e[v & 1])); }
    if (ph == 1) { t.e[v & 1] = __builtin_amdgcn_rcpf(1.0f + t.e[v & 1]); asm volatile("" : "+v"(t.e[v & 1])); }
    if (ph == 2) { t.h[v & 1] = c[1][j >> 2][nh][j & 3] * t.e[v & 1]; asm volatile("" : "+v"(t.h[v & 1])); }
    if (ph == 3 && (v & 1)) {
        t.pk[j >> 1] = cvtpk(t.h[0], t.h[1]);
        asm volatile("" : "+v"(t.pk[j >> 1]));
        if (j == 7) gstore16(hdst + (size_t)nh * FS * 1024, lo, t.pk);
    }
}

template <int U>
DI void g1_unit(const G1Ctx& cx, lds_cptr A0, lds_cptr A1, bf16x8 (&xb)[2][KS], f32x4 (&c)[2][2][2], const f32x4 (&cprev)[2][2][2]) {
    constexpr int GRP = g1_group(U), HK = g1_half(U);
    constexpr bool PAIR = GRP != 5;
    constexpr int PER = PAIR ? 4 : 2, N = 9 * PER;
    constexpr bool EPI = U >= 4;
    bf16x4 flo[4], fhi[4];
    lds_cptr a0 = A0 + (U & 3) * G1_SLOT, a1 = A1 + (U & 3) * G1_SLOT;
    asm volatile("" : "+v"(a0), "+v"(a1));
    auto addr = [&](int i) {
        const int ks = i / PER, sel = PAIR ? (i >> 1) & 1 : i & 1, t = PAIR ? i & 1 : 0;
        return (t ? a1 : a0) + ks * 4096 + sel * 2048;
    };
#pragma unroll
    for (int i = 0; i < G1_AHEAD; ++i) { flo[i] = trread(addr(i)); fhi[i] = trread(addr(i) + 1024); }
    if (HK == 0) {
#pragma unroll
        for (int i = 0; i < 8; ++i) c[i >> 2][(i >> 1) & 1][i & 1] = (f32x4){};
    }
    G1Epi ep;
    char* hprev = cx.hdst + (U / 2 - 2) * 1024;
    static_for<0, N>([&](auto ic) {
        constexpr int i = decltype(ic)::value;
        constexpr int kl = i / PER, ks = 9 * HK + kl, sel = PAIR ? (i >> 1) & 1 : i & 1, t = PAIR ? i & 1 : 0;
        constexpr int g = PAIR ? i : 2 * i;
        constexpr int x = g1_x_at(U, g), dm = g1_dma_at(U, g);
        if constexpr (x >= 0) { g1_ldx(xb[0][x], cx.xs, cx.lo, x); g1_ldx(xb[1][x], cx.xs + KS * 1024, cx.lo, x); }
        if constexpr (dm >= 0) g1_piece(cx, dm >> 3, dm & 7);
        if constexpr ((U == 0 || U == 2) && (i & 3) == 0) {
            constexpr int young = g1_x_younger(ks, U, g);
            asm volatile("s_waitcnt vmcnt(%2)" : "+v"(xb[0][ks]), "+v"(xb[1][ks]) : "n"(young));
        }
        constexpr bool SWAP = GRP == 0 && sel == 1 && t == 0;
        const bf16x8 fr = join(flo[i & 3], fhi[i & 3]);
        c[sel][t][0] = SWAP ? MFMA32K(xb[0][ks], fr, c[sel][t][0]) : MFMA32K(fr, xb[0][ks], c[sel][t][0]);
        SBAR();
        if constexpr (i + G1_AHEAD < N) flo[(i + G1_AHEAD) & 3] = trread(addr(i + G1_AHEAD));
        SBAR();
        c[sel][t][1] = SWAP ? MFMA32K(xb[1][ks], fr, c[sel][t][1]) : MFMA32K(fr, xb[1][ks], c[sel][t][1]);
        SBAR();
        if constexpr (i + G1_AHEAD < N) fhi[(i + G1_AHEAD) & 3] = trread(addr(i + G1_AHEAD) + 1024);
        SBAR();
        if constexpr (EPI && PAIR) {
            constexpr int sl = 36 * HK + i - 4;
            if constexpr (sl >= 0 && sl < 64) g1_epi_slice(sl, cprev, ep, hprev, cx.lo);
        } else if constexpr (EPI) {
            constexpr int sl = 2 * (18 * HK + i - 2);
            if constexpr (sl >= 0 && sl < 64) { g1_epi_slice(sl, cprev, ep, hprev, cx.lo); g1_epi_slice(sl + 1, cprev, ep, hprev, cx.lo); }
        }
        SBAR();
    });
}
DI void g1_epi_qkv(const f32x4 (&c)[2][2][2], char* ws, int h, int tb, int lane, lds_ptr stg) {
    typedef __attribute__((address_space(3))) u32x2* l64;
    typedef __attribute__((address_space(3))) unsigned* l32;
    typedef __attribute__((address_space(3))) unsigned short* l16;
    asm volatile("" : "+v"(lane));
    const int t = lane & 15, g4 = lane >> 4;
    const lds_ptr rec = stg + QREC;
    float kmax = 0.f;
#pragma unroll
    for (int nh = 0; nh < 2; ++nh) {
        const int r = 16 * nh + t;
        const unsigned fo = (r + 32 * (g4 >> 1)) * 16 + (g4 & 1) * 8;
        const f32x4 q = c[0][0][nh], k = c[0][1][nh], v = c[1][0][nh], e = c[1][1][nh];
        *(l64)(stg + fo) = (u32x2){cvtpk(q[0] * QSCALE, q[1] * QSCALE), cvtpk(q[2] * QSCALE, q[3] * QSCALE)};
        const u32x2 kp = {cvtpk(k[0], k[1]), cvtpk(k[2], k[3])};
        *(l64)(rec + fo) = kp;
        *(l64)(rec + 1280 + nh * 640 + (g4 & 1) * 320 + t * 16 + (g4 >> 1) * 8) = (u32x2){cvtpk(v[0], v[1]), cvtpk(v[2], v[3])};
        const unsigned e01 = cvtpk(e[0], e[1]), e23 = cvtpk(e[2], e[3]);
        const lds_ptr vt = rec + 1280 + nh * 640 + ((t >> 2) & 1) * 320 + (4 * (t >> 3) + (t & 3)) * 2;
        if (g4 == 0) {
            *(l64)(rec + 1024 + r * 8) = (u32x2){e23, 0u};
            *(l32)(stg + 1024 + r * 4) = cvtpk(e[0] * QSCALE, e[1] * QSCALE);
        } else {
            *(l16)(vt + (g4 == 1 ? 16 : 16 + g4) * 16) = (unsigned short)(g4 == 1 ? e01 : (g4 == 2 ? 0x3f80u : 0u));
            if (g4 == 1) *(l16)(vt + 17 * 16) = (unsigned short)(e01 >> 16);
        }
        float n2 = g4 == 0 ? bf_lo(e23) * bf_lo(e23) + bf_hi(e23) * bf_hi(e23) : 0.f;
        n2 += bf_lo(kp[0]) * bf_lo(kp[0]) + bf_hi(kp[0]) * bf_hi(kp[0]) + bf_lo(kp[1]) * bf_lo(kp[1]) + bf_hi(kp[1]) * bf_hi(kp[1]);
        n2 += swap16(n2);
        n2 += swap32(n2);
        kmax = fmaxf(kmax, n2);
    }
    kmax = rowmax16(kmax);
    if (lane == 0) *(l32)(stg + 1152) = __float_as_uint(kmax);
    typedef __attribute__((address_space(3))) const u32x4* l128;
    char* qg = ws + WS_Q + ((size_t)h * NTB + tb) * QREC;
    char* kg = ws + WS_KV + ((size_t)h * NTB + tb) * KVREC;
    const unsigned o0 = lane * 16, oq = min(lane, 8) * 16 + 1024, ok = min(lane, 31) * 16 + 2048;
    const u32x4 d0 = *(l128)(stg + o0), d1 = *(l128)(stg + oq), d2 = *(l128)(rec + o0), d3 = *(l128)(rec + 1024 + o0), d4 = *(l128)(rec + ok);
    gstore16(qg, o0, d0);
    gstore16(qg, oq, d1);
    gstore16(kg, o0, d2);
    gstore16(kg, o0 + 1024, d3);
    gstore16(kg, ok, d4);
}

__global__ __launch_bounds__(512, 2) void k_gemm1(const char* wg, const char* wu, const char* wd, char* ws) {
    extern __shared__ __attribute__((aligned(16))) char lds[];
    STAMP_BEGIN;
    const int tid = threadIdx.x, lane = tid & 63;
    const int w = __builtin_amdgcn_readfirstlane(tid >> 6);
    const int xcd = blockIdx.x & 7, idx = blockIdx.x >> 3;
    const int cg = 4 * xcd + (idx & 3), rg = idx >> 2, tb = 8 * rg + w;
    const bool late = w >= 4;
    MARKS; MARK(0);

    const int odd = cg & 1;
    G1Ctx cx;
    cx.w = w; cx.lds0 = (unsigned)(uintptr_t)lds; cx.lo = lane * 16;
    cx.fbase = 144 * cg + 16 * odd; cx.fsingle = odd ? 144 * cg - 16 : 144 * cg + 128;
    cx.wq = ws + WS_WQP + (size_t)cg * KS * 4096;
    cx.xs = ws + WS_XNF + (size_t)(2 * tb) * KS * 1024;
    cx.wrow = ((w & 1) ? wu : wg) + (size_t)(32 * (w >> 2) + 16 * ((w >> 1) & 1)) * (F * 2);
    cx.dsto = (w >> 2) * 4096 + (w & 1) * 2048 + ((w >> 1) & 1) * 1024;
    cx.voff = (lane >> 2) * (F * 2) + (((lane & 3) ^ (2 * ((lane >> 4) & 1))) * 16);
    cx.hdst = ws + WS_HF + ((size_t)(2 * tb) * FS + cx.fbase / 32) * 1024;

#pragma unroll
    for (int m = 0; m < 5; ++m) g1_piece(cx, 0, m);
    bf16x8 xb[2][KS];
#pragma unroll
    for (int s = 0; s < 6; ++s) { g1_ldx(xb[0][s], cx.xs, cx.lo, s); g1_ldx(xb[1][s], cx.xs + KS * 1024, cx.lo, s); }

    const int aoff0 = img_aoff(lane);
    const lds_cptr A0 = (lds_cptr)lds + aoff0, A1 = (lds_cptr)lds + (aoff0 ^ 32);
    f32x4 ca[2][2][2], cb[2][2][2];
    u32x4 dead0, dead1, dead2;

    WAITV_BAR(12);
    MARK(1);
    g1_unit<0>(cx, A0, A1, xb, ca, cb); WAITV_BAR(5);
    g1_unit<1>(cx, A0, A1, xb, cb, ca); WAITV_BAR(18);
    MARK(2);
    g1_unit<2>(cx, A0, A1, xb, ca, cb); WAITV_BAR(5);
    MARK(3);
    const lds_ptr stg = (lds_ptr)lds + 2 * G1_SLOT + w * 4608;
    if (!late) g1_epi_qkv(ca, ws, cg, tb, lane, stg);
    g1_unit<3>(cx, A0, A1, xb, cb, ca);
    if (late) { g1_epi_qkv(ca, ws, cg, tb, lane, stg); WAITV_BAR(10); } else { WAITV_BAR(5); }
    MARK(4);
    g1_unit<4>(cx, A0, A1, xb, ca, cb); WAITV_BAR(5);
    g1_unit<5>(cx, A0, A1, xb, ca, cb); WAITV_BAR(5);
    MARK(5);
    g1_unit<6>(cx, A0, A1, xb, cb, ca); WAITV_BAR(5);
    g1_unit<7>(cx, A0, A1, xb, cb, ca); WAITV_BAR(5);
    MARK(6);
    g1_unit<8>(cx, A0, A1, xb, ca, cb);
    {
        const char* src = wd + (size_t)(xcd >> 1) * (1152 * D * 2) + ((xcd & 1) * 256 + idx * 8 + w) * 2592;
        asm volatile("global_load_dwordx4 %0, %3, %4\n\tglobal_load_dwordx4 %1, %3, %4 offset:1024\n\tglobal_load_dwordx4 %2, %3, %4 offset:1568"
                     : "=&v"(dead0), "=&v"(dead1), "=&v"(dead2) : "v"(cx.lo), "s"(src) : "memory");
        WAITV_BAR(8);
    }
    g1_unit<9>(cx, A0, A1, xb, ca, cb); WAITV_BAR(5);
    asm volatile("" :: "v"(dead0), "v"(dead1), "v"(dead2));
    MARK(7);
    const lds_cptr AS = odd ? A1 : A0;
    g1_unit<10>(cx, AS, AS, xb, cb, ca); WAITV_BAR(0);
    g1_unit<11>(cx, AS, AS, xb, cb, ca);
    {
        char* hs = ws + WS_HF + ((size_t)(2 * tb) * FS + cx.fsingle / 32) * 1024 + odd * 8;
#pragma unroll
        for (int nh = 0; nh < 2; ++nh) {
            float h[4];
#pragma unroll
            for (int j = 0; j < 4; ++j) h[j] = glu(cb[0][0][nh][j], cb[1][0][nh][j]);
            gstore8(hs + (size_t)nh * FS * 1024, cx.lo, (u32x2){cvtpk(h[0], h[1]), cvtpk(h[2], h[3])});
        }
    }
    MARK(8); MARK_DUMP(1, 9);
    STAMP_END(1);
}

constexpr int AT_STAGE = 8 * KVREC;
constexpr float AT_BOUND_MAX = 50.0f;

struct KFrag { bf16x8 k1; bf16x4 k2; };
struct VFrag { bf16x8 v0, v1; };
DI KFrag at_readk(lds_cptr rec, int lane) {
    KFrag k;
    k.k1 = *(const __attribute__((address_space(3))) bf16x8*)(rec + lane * 16);
    k.k2 = *(const __attribute__((address_space(3))) bf16x4*)(rec + 1024 + (lane & 31) * 8);
    return k;
}
DI VFrag at_readv(lds_cptr recv) {
    VFrag v;
    v.v0 = *(const __attribute__((address_space(3))) bf16x8*)(recv);
    v.v1 = *(const __attribute__((address_space(3))) bf16x8*)(recv + 640);
    return v;
}
DI bf16x8 packp(const f32x16& p, int s) {
    return __builtin_bit_cast(bf16x8, (u32x4){cvtpk(p[8 * s], p[8 * s + 1]), cvtpk(p[8 * s + 2], p[8 * s + 3]), cvtpk(p[8 * s + 4], p[8 * s + 5]), cvtpk(p[8 * s + 6], p[8 * s + 7])});
}

DI void at_dma(const char* kvh, int stage, unsigned ldsb, int w, int lane) {
    const char* src = kvh + (size_t)stage * AT_STAGE + w * 5 * 1024;
    const unsigned dst = ldsb + w * 5 * 1024, lo = lane * 16;
    asm volatile("s_mov_b32 m0, %2\n\ts_nop 0\n\t"
                 "global_load_lds_dwordx4 %0, %1\n\tglobal_load_lds_dwordx4 %0, %1 offset:1024\n\t"
                 "global_load_lds_dwordx4 %0, %1 offset:2048\n\tglobal_load_lds_dwordx4 %0, %1 offset:3072"
                 :: "v"(lo), "s"(src), "s"(dst) : "memory");
    glds16(src + 4096, lo, dst + 4096);
}

#define AMFMA16_NEW(d, a, b, c) asm volatile("v_mfma_f32_32x32x16_bf16 %0, %1, %2, %3" : "=&v"(d) : "v"(a), "v"(b), "v"(c))
#define AMFMA16_ACC(d, a, b) asm volatile("v_mfma_f32_32x32x16_bf16 %0, %1, %2, %0" : "+v"(d) : "v"(a), "v"(b))
#define AMFMA8_ACC(d, a, b) asm volatile("v_mfma_f32_32x32x8_bf16 %0, %1, %2, %0" : "+v"(d) : "v"(a), "v"(b))
#define AT_EXP4(E, SX, a) do { _Pragma("unroll") for (int i_ = 0; i_ < 4; ++i_) asm volatile("v_exp_f32 %0, %1" : "=v"(E[i_]) : "v"(SX[a + i_])); } while (0)
#define AT_PK2(DST, k, E) do { asm volatile("v_cvt_pk_bf16_f32 %0, %1, %2" : "=v"(DST[k]) : "v"(E[0]), "v"(E[1]));   \
                               asm volatile("v_cvt_pk_bf16_f32 %0, %1, %2" : "=v"(DST[k + 1]) : "v"(E[2]), "v"(E[3])); } while (0)
#define AT_STEP(SX, PX0, PX1, SY, CY, QY1, QY2, KF, OY, PY0, PY1, VF, LOADS)                                   \
    do {                                                                                                       \
        float e0_[4], e1_[4], e2_[4];                                                                          \
        AMFMA16_NEW(SY, KF.k1, QY1, CY);                                                                       \
        LOADS;                                                                                                 \
        SBAR();                                                                                                \
        AT_EXP4(e0_, SX, 0);                                                                                   \
        AT_PK2(PY1, 2, etail);                                                                                 \
        AMFMA8_ACC(SY, KF.k2, QY2);                                                                            \
        AT_EXP4(e1_, SX, 4);                                                                                   \
        AT_PK2(PX0, 0, e0_);                                                                                   \
        AMFMA16_ACC(OY, VF.v0, PY0);                                                                           \
        AT_EXP4(e2_, SX, 8);                                                                                   \
        AT_PK2(PX0, 2, e1_);                                                                                   \
        AMFMA16_ACC(OY, VF.v1, PY1);                                                                           \
        AT_EXP4(etail, SX, 12);                                                                                \
        AT_PK2(PX1, 0, e2_);                                                                                   \
        SBAR();                                                                                                \
    } while (0)

DI void at_online(const KFrag& k, const VFrag& v, bf16x8 q1, bf16x4 q2, float& m, f32x16& o) {
    f32x16 z = {};
    f32x16 s = MFMA16(k.k1, q1, z);
    s = MFMA8(k.k2, q2, s);
    float t = s[0];
#pragma unroll
    for (int i = 1; i < 16; ++i) t = fmaxf(t, s[i]);
    t = fmaxf(t, swap32(t));
    const float mn = fmaxf(m, t);
    const float alpha = __builtin_amdgcn_exp2f(m - mn);
    m = mn;
#pragma unroll
    for (int i = 0; i < 16; ++i) { s[i] = __builtin_amdgcn_exp2f(s[i] - mn); o[i] *= alpha; }
    o = MFMA16(v.v0, packp(s, 0), o);
    o = MFMA16(v.v1, packp(s, 1), o);
}

DI void at_store(const f32x16& o, char* ws, int h, int tb, int lane) {
    const int r = lane & 31, hi = lane >> 5;
    const float other = swap32(o[10]);
    const float inv = 1.0f / (hi ? other : o[10]);
    char* of = ws + WS_OF + (size_t)(2 * tb + (r >> 4)) * KS * 1024;
    *(u32x4*)(of + (h >> 1) * 1024 + ((r & 15) + 16 * (2 * (h & 1) + hi)) * 16) =
        (u32x4){cvtpk(o[0] * inv, o[1] * inv), cvtpk(o[2] * inv, o[3] * inv), cvtpk(o[4] * inv, o[5] * inv), cvtpk(o[6] * inv, o[7] * inv)};
    if (hi == 0) *(unsigned*)(of + (16 + (h >> 4)) * 1024 + ((r & 15) + 16 * ((h >> 2) & 3)) * 16 + (h & 3) * 4) = cvtpk(o[8] * inv, o[9] * inv);
}

DI float at_qnorm2(bf16x8 q1, bf16x4 q2) {
    const u32x4 a = __builtin_bit_cast(u32x4, q1); const u32x2 b = __builtin_bit_cast(u32x2, q2);
    float n = bf_lo(b[0]) * bf_lo(b[0]) + bf_hi(b[0]) * bf_hi(b[0]);
#pragma unroll
    for (int j = 0; j < 4; ++j) n += bf_lo(a[j]) * bf_lo(a[j]) + bf_hi(a[j]) * bf_hi(a[j]);
    return n + swap32(n);
}

DI void attn_block(char* ws, char* lds, int bid) {
    const int tid = threadIdx.x, lane = tid & 63, hi = lane >> 5;
    const int w = __builtin_amdgcn_readfirstlane(tid >> 6);
    const int xcd = bid & 7, idx = bid >> 3;
    const int h = 4 * xcd + (idx & 3), qg = idx >> 2;
    const int tbA = 8 * qg + 2 * w, tbB = tbA + 1;
    const unsigned lds0 = (unsigned)(uintptr_t)lds;
    const char* kvh = ws + WS_KV + (size_t)h * NTB * KVREC;
    MARKS; MARK(0);

    bf16x8 qA1, qB1; unsigned qa2, qb2; float km;
    {
        const char* q1 = ws + WS_Q + ((size_t)h * NTB + tbA) * QREC;
        const char* kx = ws + WS_Q + (size_t)h * NTB * QREC + 1152;
        const unsigned l16 = lane * 16, l4 = (lane & 31) * 4 + 1024, lk = lane * QREC;
        asm volatile("global_load_dword %0, %1, %2" : "=v"(km) : "v"(lk), "s"(kx));
        asm volatile("global_load_dwordx4 %0, %1, %2" : "=v"(qA1) : "v"(l16), "s"(q1));
        asm volatile("global_load_dwordx4 %0, %1, %2 offset:%3" : "=v"(qB1) : "v"(l16), "s"(q1), "n"(QREC));
        asm volatile("global_load_dword %0, %1, %2" : "=v"(qa2) : "v"(l4), "s"(q1));
        asm volatile("global_load_dword %0, %1, %2 offset:%3" : "=v"(qb2) : "v"(l4), "s"(q1), "n"(QREC));
    }
    at_dma(kvh, 0, lds0, w, lane);
    at_dma(kvh, 1, lds0 + AT_STAGE, w, lane);
    asm volatile("s_waitcnt vmcnt(10)" : "+v"(km), "+v"(qA1), "+v"(qB1), "+v"(qa2), "+v"(qb2));
    const bf16x4 qA2 = __builtin_bit_cast(bf16x4, (u32x2){hi ? 0u : qa2, 0u});
    const bf16x4 qB2 = __builtin_bit_cast(bf16x4, (u32x2){hi ? 0u : qb2, 0u});
    km = rowmax16(km);
    km = fmaxf(km, swap16(km));
    km = fmaxf(km, swap32(km));
    const float mA = __builtin_amdgcn_sqrtf(at_qnorm2(qA1, qA2) * km) * 1.001f, mB = __builtin_amdgcn_sqrtf(at_qnorm2(qB1, qB2) * km) * 1.001f;
    const bool fast = __all(mA <= AT_BOUND_MAX && mB <= AT_BOUND_MAX);

    const int voffs = 1280 + hi * 320 + min(lane & 31, 19) * 16;
    const lds_cptr L = (lds_cptr)lds;
    f32x16 oA = {}, oB = {};

    if (fast) {
        f32x16 cA, cB;
#pragma unroll
        for (int i = 0; i < 16; ++i) { cA[i] = -mA; cB[i] = -mB; }
        asm volatile("" : "+v"(cA), "+v"(cB));
        f32x16 sA, sB;
        u32x4 pA0 = {}, pA1 = {}, pB0 = {}, pB1 = {};
        float etail[4] = {0.f, 0.f, 0.f, 0.f};
        KFrag k0, k1; VFrag v0, v1;
        MARK(1);
        WAITV_BAR(5);
        MARK(2);
        k0 = at_readk(L, lane);
        v1 = at_readv(L + voffs);
        sA = MFMA16(k0.k1, qA1, cA); sA = MFMA8(k0.k2, qA2, sA);
        asm volatile("s_nop 15" : "+v"(sA));
        int slot = 0;
        for (int st = 0; st < 8; ++st) {
            const lds_cptr cur = L + slot * AT_STAGE;
            const int nslot = slot == 2 ? 0 : slot + 1;
#pragma unroll
            for (int t = 0; t < 8; t += 2) {
                AT_STEP(sA, pA0, pA1, sB, cB, qB1, qB2, k0, oB, pB0, pB1, v1,
                        (k1 = at_readk(cur + (t + 1) * KVREC, lane), v0 = at_readv(cur + t * KVREC + voffs)));
                AT_STEP(sB, pB0, pB1, sA, cA, qA1, qA2, k1, oA, pA0, pA1, v0, (void)0);
                lds_cptr nk = cur + (t + 2) * KVREC;
                if (t == 6) {
                    if (st < 7) {
                        if (st < 6) { WAITV_BAR(0); at_dma(kvh, st + 2, lds0 + (nslot == 2 ? 0 : nslot + 1) * AT_STAGE, w, lane); }
                        else WAITV_BAR(0);
                        nk = L + nslot * AT_STAGE;
                    } else nk = cur;
                }
                AT_STEP(sA, pA0, pA1, sB, cB, qB1, qB2, k1, oB, pB0, pB1, v0,
                        (k0 = at_readk(nk, lane), v1 = at_readv(cur + (t + 1) * KVREC + voffs)));
                AT_STEP(sB, pB0, pB1, sA, cA, qA1, qA2, k0, oA, pA0, pA1, v1, (void)0);
            }
            slot = nslot;
            MARK(3 + st);
        }
        asm volatile("s_nop 1");
        AT_PK2(pB1, 2, etail);
        asm volatile("s_nop 1" : "+v"(pB1));
        AMFMA16_ACC(oB, v1.v0, pB0);
        AMFMA16_ACC(oB, v1.v1, pB1);
        asm volatile("s_nop 15\n\ts_nop 7" : "+v"(oA), "+v"(oB));
    } else {
        float mxA = -INFINITY, mxB = -INFINITY;
        WAITV_BAR(5);
        int slot = 0;
        for (int st = 0; st < 8; ++st) {
            const lds_cptr cur = L + slot * AT_STAGE;
            const int nslot = slot == 2 ? 0 : slot + 1;
            for (int t = 0; t < 8; ++t) {
                const KFrag k = at_readk(cur + t * KVREC, lane);
                const VFrag v = at_readv(cur + t * KVREC + voffs);
                at_online(k, v, qA1, qA2, mxA, oA);
                at_online(k, v, qB1, qB2, mxB, oB);
            }
            if (st < 7) {
                WAITV_BAR(0);
                if (st < 6) at_dma(kvh, st + 2, lds0 + (nslot == 2 ? 0 : nslot + 1) * AT_STAGE, w, lane);
            }
            slot = nslot;
        }
    }
    at_store(oA, ws, h, tbA, lane);
    at_store(oB, ws, h, tbB, lane);
    diag_stamp(ws, 1, 0);
    MARK(11); MARK_DUMP(2, 12);
}

__global__ __launch_bounds__(256, 2) void k_attn(char* ws) {
    extern __shared__ __attribute__((aligned(16))) char lds[];
    STAMP_BEGIN;
    attn_block(ws, lds, blockIdx.x);
    STAMP_END(2);
}

constexpr int DN_STEP = 32 * 288, DN_SLOT = 2 * DN_STEP, DN_LDS = 4 * DN_SLOT, DN_NST = 18;
constexpr int dn_nb(int st) { return st + 2 >= 0 && st + 2 < DN_NST ? 4 : 0; }
constexpr int dn_nd(int st) { return st + 3 >= 0 && st + 3 < DN_NST ? 5 : 0; }

DI void dn_ldb(bf16x8 (&b)[2][2], const char* hs, unsigned lo, int st) {
#pragma unroll
    for (int k = 0; k < 2; ++k) {
        asm volatile("global_load_dwordx4 %0, %1, %2 offset:%3" : "=v"(b[0][k]) : "v"(lo), "s"(hs + (size_t)st * 2048), "n"(k * 1024));
        asm volatile("global_load_dwordx4 %0, %1, %2 offset:%3" : "=v"(b[1][k]) : "v"(lo), "s"(hs + (size_t)FS * 1024 + (size_t)st * 2048), "n"(k * 1024));
    }
}
template <int N> DI void dn_wait(bf16x8 (&b)[2][2]) {
    asm volatile("s_waitcnt vmcnt(%4) lgkmcnt(0)\n\ts_barrier" : "+v"(b[0][0]), "+v"(b[0][1]), "+v"(b[1][0]), "+v"(b[1][1]) : "n"(N) : "memory");
}

DI void down_block(const char* wd, char* ws, char* lds, int bid) {
    const int tid = threadIdx.x, lane = tid & 63;
    const int w = __builtin_amdgcn_readfirstlane(tid >> 6);
    const int xcd = bid & 7, idx = bid >> 3;
    const int kq = xcd >> 1, tg = (xcd & 1) * 8 + (idx >> 2), dq = idx & 3;
    const int tb16 = 8 * tg + 2 * w, fs0 = 36 * kq;
    const unsigned lds0 = (unsigned)(uintptr_t)lds;
    unsigned voff[5], doff[5];
#pragma unroll
    for (int m = 0; m < 5; ++m) { const int i = min(5 * (w & 1) + m, 8), n = 64 * i + lane; voff[m] = (n / 18) * (D * 2) + (n % 18) * 16; doff[m] = (w >> 1) * DN_STEP + i * 1024; }
    const char* wsrc = wd + (size_t)(fs0 + (w >> 1)) * 32 * (D * 2) + dq * 288;
    const lds_cptr A = (lds_cptr)lds + (4 * (lane >> 4) + ((lane & 15) >> 2)) * 288 + (lane & 3) * 8;
    const char* hs = ws + WS_HF + ((size_t)tb16 * FS + fs0) * 1024;
    const unsigned lo = lane * 16;
    MARKS; MARK(0);

    f32x4 acc[9][2];
#pragma unroll
    for (int i = 0; i < 9; ++i) { acc[i][0] = (f32x4){}; acc[i][1] = (f32x4){}; }
    bf16x8 b[3][2][2];
    auto dma = [&](int st, int m) { glds16(wsrc + (size_t)st * 2 * 32 * (D * 2), voff[m], lds0 + (st & 3) * DN_SLOT + doff[m]); };
#pragma unroll
    for (int m = 0; m < 5; ++m) dma(0, m);
    dn_ldb(b[0], hs, lo, 0);
#pragma unroll
    for (int m = 0; m < 5; ++m) dma(1, m);
    dn_ldb(b[1], hs, lo, 1);
#pragma unroll
    for (int m = 0; m < 5; ++m) dma(2, m);
    static_for<0, DN_NST>([&](auto stc) {
        constexpr int st = decltype(stc)::value, set = st % 3;
        constexpr int young = dn_nd(st - 2) + dn_nb(st - 1) + dn_nd(st - 1);
        dn_wait<young>(b[set]);
        if constexpr (dn_nb(st) > 0) dn_ldb(b[(st + 2) % 3], hs, lo, st + 2);
        lds_cptr a = A + (st & 3) * DN_SLOT;
        asm volatile("" : "+v"(a));
        bf16x4 flo[4], fhi[4];
        auto addr = [&](int i) { return a + (i / 9) * DN_STEP + (i % 9) * 32; };
#pragma unroll
        for (int i = 0; i < 3; ++i) { flo[i] = trread(addr(i)); fhi[i] = trread(addr(i) + 16 * 288); }
#pragma unroll
        for (int i = 0; i < 18; ++i) {
            if (dn_nd(st) > 0 && (i % 3) == 0 && i / 3 < 5) dma(st + 3, i / 3);
            const bf16x8 fr = join(flo[i & 3], fhi[i & 3]);
            acc[i % 9][0] = MFMA32K(fr, b[set][0][i / 9], acc[i % 9][0]);
            SBAR();
            if (i + 3 < 18) flo[(i + 3) & 3] = trread(addr(i + 3));
            SBAR();
            acc[i % 9][1] = MFMA32K(fr, b[set][1][i / 9], acc[i % 9][1]);
            SBAR();
            if (i + 3 < 18) fhi[(i + 3) & 3] = trread(addr(i + 3) + 16 * 288);
            SBAR();
        }
    });
    MARK(1); diag_stamp(ws, 2, 256);
    char* dst = ws + WS_PART + (((size_t)kq * NT16 + tb16) * 36 + 9 * dq) * 1024;
#pragma unroll
    for (int nh = 0; nh < 2; ++nh)
#pragma unroll
        for (int i = 0; i < 9; ++i) gstore16(dst + (size_t)nh * 36 * 1024 + i * 1024, lo, __builtin_bit_cast(u32x4, acc[i][nh]));
    MARK(2);
    asm volatile("s_waitcnt vmcnt(0)\n\ts_barrier" ::: "memory");
    if (w == 0) {
        typedef __attribute__((address_space(1))) unsigned gu32;
        gu32* cnt = (gu32*)(ws + WS_CNT + (tg * 4 + dq) * 256);
        if (lane == 0) __hip_atomic_fetch_add(cnt, 1u, __ATOMIC_RELAXED, __HIP_MEMORY_SCOPE_AGENT);
        for (int spins = 0; spins < (1 << 20); ++spins) {
            if ((unsigned)__builtin_amdgcn_readfirstlane(__hip_atomic_load(cnt, __ATOMIC_RELAXED, __HIP_MEMORY_SCOPE_AGENT)) >= 4u) break;
            __builtin_amdgcn_s_sleep(4);
        }
        __builtin_amdgcn_fence(__ATOMIC_ACQUIRE, "agent");
        asm volatile("s_waitcnt vmcnt(0)" ::: "memory");
    }
    asm volatile("s_barrier" ::: "memory");
    MARK(3);
    f32x4 pq[5][4];
#pragma unroll
    for (int m = 0; m < 5; ++m) {
        const int idx = min(w + 4 * m, 17), nh = idx >= 9, i = idx - 9 * nh;
        const char* pp = ws + WS_PART + ((size_t)(8 * tg + 2 * kq + nh) * 36 + 9 * dq + i) * 1024 + lo;
#pragma unroll
        for (int k = 0; k < 4; ++k) pq[m][k] = *(const f32x4*)(pp + (size_t)k * NT16 * 36 * 1024);
    }
    asm volatile("" ::: "memory");
    SBAR();
#pragma unroll
    for (int m = 0; m < 5; ++m) {
        const int idx = min(w + 4 * m, 17), nh = idx >= 9, i = idx - 9 * nh;
        const f32x4 ffn = (pq[m][0] + pq[m][1]) + (pq[m][2] + pq[m][3]);
        float g[4];
#pragma unroll
        for (int c = 0; c < 4; ++c) { const float f = rbf(ffn[c]); g[c] = f * __builtin_amdgcn_rcpf(1.0f + __builtin_amdgcn_exp2f(f * -LOG2E)); }
        *(u32x2*)(ws + WS_G + ((size_t)(8 * tg + 2 * kq + nh) * 36 + 9 * dq + i) * 512 + lane * 8) = (u32x2){cvtpk(g[0], g[1]), cvtpk(g[2], g[3])};
    }
    diag_stamp(ws, 3, 256);
    MARK(4); MARK_DUMP(3, 5);
}

__global__ __launch_bounds__(256, 2) void k_down(const char* wd, char* ws) {
    extern __shared__ __attribute__((aligned(16))) char lds[];
    STAMP_BEGIN;
    down_block(wd, ws, lds, blockIdx.x);
    STAMP_END(3);
}

#define AT_PRIO 2
#define MIX_MODE 0
__global__ __launch_bounds__(256, 2) void k_mix(const char* wd, char* ws) {
    extern __shared__ __attribute__((aligned(16))) char lds[];
    STAMP_BEGIN; diag_stamp(ws, 0, 0);
    const int b = blockIdx.x;
    const bool attn = b < 256; const int bid = b & 255;
    if (attn) { __builtin_amdgcn_s_setprio(AT_PRIO); attn_block(ws, lds, bid); } else down_block(wd, ws, lds, bid);
    STAMP_END(5);
}

constexpr int FN_LDS = 3 * KS * 1024;
__global__ __launch_bounds__(256, 1) void k_final(const unsigned short* x, char* ws, unsigned short* out) {
    extern __shared__ __attribute__((aligned(16))) char lds[];
    STAMP_BEGIN;
    const int tid = threadIdx.x, lane = tid & 63;
    const int w = __builtin_amdgcn_readfirstlane(tid >> 6);
    const int xcd = blockIdx.x & 7, idx = blockIdx.x >> 3;
    const int tg = 2 * xcd + idx / 12, dg = idx % 12, tb16 = 8 * tg + 2 * w;
    const unsigned lds0 = (unsigned)(uintptr_t)lds, lo = lane * 16;
    MARKS; MARK(0);
    const unsigned long long t3 = __builtin_amdgcn_s_memrealtime();
    {
        const char* src = ws + WS_WOP + (size_t)dg * FN_LDS;
#pragma unroll
        for (int i = 0; i < 14; ++i) { const int n = min(w * 14 + i, 3 * KS - 1); glds16(src + n * 1024, lo, lds0 + n * 1024); }
    }
    const char* os = ws + WS_OF + (size_t)tb16 * KS * 1024 + lo;
    bf16x8 b[2][KS];
#pragma unroll
    for (int s = 0; s < KS; ++s) { b[0][s] = *(const bf16x8*)(os + s * 1024); b[1][s] = *(const bf16x8*)(os + (KS + s) * 1024); }
    u32x2 gv[2][3], xv[2][3];
#pragma unroll
    for (int nh = 0; nh < 2; ++nh)
#pragma unroll
        for (int t = 0; t < 3; ++t) {
            gv[nh][t] = *(const u32x2*)(ws + WS_G + ((size_t)(tb16 + nh) * 36 + 3 * dg + t) * 512 + lane * 8);
            xv[nh][t] = *(const u32x2*)(x + (size_t)((tb16 + nh) * 16 + (lane & 15)) * D + dg * 48 + 16 * t + 4 * (lane >> 4));
        }
    MARK(1);
    asm volatile("s_waitcnt vmcnt(48)\n\ts_barrier" ::: "memory");
    MARK(2);
    SBAR();
    const lds_cptr A = (lds_cptr)lds + lo;
    f32x4 acc[2][3] = {};
#pragma unroll
    for (int s = 0; s < KS; ++s)
#pragma unroll
        for (int t = 0; t < 3; ++t) {
            const bf16x8 a = *(const __attribute__((address_space(3))) bf16x8*)(A + (t * KS + s) * 1024);
            acc[0][t] = MFMA32K(a, b[0][s], acc[0][t]);
            acc[1][t] = MFMA32K(a, b[1][s], acc[1][t]);
        }
    MARK(3);
#pragma unroll
    for (int nh = 0; nh < 2; ++nh)
#pragma unroll
        for (int t = 0; t < 3; ++t) {
            float o[4];
#pragma unroll
            for (int c = 0; c < 4; ++c) {
                const float g = (c & 1) ? bf_hi(gv[nh][t][c >> 1]) : bf_lo(gv[nh][t][c >> 1]);
                const float xx = (c & 1) ? bf_hi(xv[nh][t][c >> 1]) : bf_lo(xv[nh][t][c >> 1]);
                o[c] = xx + rbf(rbf(acc[nh][t][c]) * g);
            }
            *(u32x2*)(out + (size_t)((tb16 + nh) * 16 + (lane & 15)) * D + dg * 48 + 16 * t + 4 * (lane >> 4)) = (u32x2){cvtpk(o[0], o[1]), cvtpk(o[2], o[3])};
        }
    MARK(4);
    if (blockIdx.x == 0 && threadIdx.x < 64 && DIAG > 0) {
        const unsigned long long* st = (const unsigned long long*)(ws + WS_DIAG);
        const unsigned long long t4 = __builtin_amdgcn_s_memrealtime();
        const unsigned long long t0 = st[0];
        unsigned long long dl = DIAG == 1 ? st[1] - t0 : DIAG == 2 ? st[2] - t0 : DIAG == 3 ? st[3] - t0 : t3 - t0;
        if (dl > 5000) dl = 5000;
        while (__builtin_amdgcn_s_memrealtime() - t4 < 10 * dl) __builtin_amdgcn_s_sleep(8);
    }
    MARK(5); MARK_DUMP(4, 6);
    STAMP_END(4);
}

extern "C" void kernel_launch(void* const* d_in, const int* in_sizes, int n_in, void* d_out, int out_size, void* d_ws, size_t ws_size, hipStream_t stream) {
    const unsigned short* x = (const unsigned short*)d_in[0];
    const unsigned short* wqkv = (const unsigned short*)d_in[1];
    const char* wo = (const char*)d_in[2];
    const char* wg = (const char*)d_in[4];
    const char* wu = (const char*)d_in[5];
    const char* wd = (const char*)d_in[6];
    char* ws = (char*)d_ws;
    (void)hipFuncSetAttribute((const void*)k_gemm1, hipFuncAttributeMaxDynamicSharedMemorySize, 4 * G1_SLOT);
    (void)hipFuncSetAttribute((const void*)k_attn, hipFuncAttributeMaxDynamicSharedMemorySize, 3 * AT_STAGE);
    (void)hipFuncSetAttribute((const void*)k_down, hipFuncAttributeMaxDynamicSharedMemorySize, DN_LDS);
    k_prep<<<PREP_PF + PREP_WO + PREP_LN + PREP_WQ, 256, 0, stream>>>(x, wqkv, (const unsigned short*)wo, wg, wu, ws);
    k_gemm1<<<256, 512, 4 * G1_SLOT, stream>>>(wg, wu, wd, ws);
    (void)hipFuncSetAttribute((const void*)k_mix, hipFuncAttributeMaxDynamicSharedMemorySize, DN_LDS);
    k_mix<<<512, 256, DN_LDS, stream>>>(wd, ws);
    (void)hipFuncSetAttribute((const void*)k_final, hipFuncAttributeMaxDynamicSharedMemorySize, FN_LDS);
    k_final<<<192, 256, FN_LDS, stream>>>(x, ws, (unsigned short*)d_out);
}
```

```cpp
#include <hip/hip_runtime.h>
#include <hip/hip_bf16.h>
#include <stdint.h>
#include <type_traits>

typedef __attribute__((ext_vector_type(8))) short bf16x8;
typedef __attribute__((ext_vector_type(4))) short bf16x4;
typedef __attribute__((ext_vector_type(2))) __bf16 bf16p;
typedef __attribute__((ext_vector_type(2))) float f32x2;
typedef __attribute__((ext_vector_type(4))) float f32x4;
typedef __attribute__((ext_vector_type(16))) float f32x16;
typedef __attribute__((ext_vector_type(2))) unsigned u32x2;
typedef __attribute__((ext_vector_type(4))) unsigned u32x4;
typedef __attribute__((address_space(3))) const char* lds_cptr;
typedef __attribute__((address_space(3))) char* lds_ptr;
typedef short v4i16_t __attribute__((ext_vector_type(4)));

#define DI __device__ __forceinline__
#define MFMA16(a, b, c) __builtin_amdgcn_mfma_f32_32x32x16_bf16((a), (b), (c), 0, 0, 0)
#define MFMA32K(a, b, c) __builtin_amdgcn_mfma_f32_16x16x32_bf16((a), (b), (c), 0, 0, 0)
#define MFMA8(a, b, c) __builtin_amdgcn_mfma_f32_32x32x8bf16_1k((a), (b), (c), 0, 0, 0)
#define SBAR() __builtin_amdgcn_sched_barrier(0)

template <int I, int N, class Fn> DI void static_for(Fn&& fn) {
    if constexpr (I < N) { fn(std::integral_constant<int, I>{}); static_for<I + 1, N>(fn); }
}

constexpr int S = 2048, D = 576, NH = 32, DH = 18, F = 4608;
constexpr int NTB = S / 32;
constexpr int NT16 = S / 16;
constexpr int KS = D / 32;
constexpr int FS = F / 32;

constexpr int KVREC = 2560;
constexpr size_t WS_XNF = 0;
constexpr size_t WS_WQP = WS_XNF + (size_t)NT16 * KS * 1024;
constexpr int QREC = 1280;
constexpr size_t WS_WOP = WS_WQP + (size_t)NH * KS * 4096;
constexpr size_t WS_Q = WS_WOP + (size_t)36 * KS * 1024;
constexpr size_t WS_KV = WS_Q + (size_t)NH * NTB * QREC;
constexpr size_t WS_OF = WS_KV + (size_t)NH * NTB * KVREC;
constexpr size_t WS_HF = WS_OF + (size_t)NT16 * KS * 1024;
constexpr size_t WS_PART = WS_HF + (size_t)NT16 * FS * 1024;
constexpr size_t WS_G = WS_PART + (size_t)4 * NT16 * 36 * 1024;
constexpr size_t WS_CNT = WS_G + (size_t)NT16 * 36 * 512;
constexpr size_t WS_END = WS_CNT + 64 * 256;

#define STAMP_BEGIN
#define STAMP_END(k)
#define MARKS
#define MARK(i)
#define MARK_DUMP(k, n)

constexpr float LOG2E = 1.4426950408889634f;
constexpr float QSCALE = 0.23570226039551584f * LOG2E;

DI unsigned cvtpk(float lo, float hi) { return __builtin_bit_cast(unsigned, __builtin_convertvector((f32x2){lo, hi}, bf16p)); }
DI float bf_lo(unsigned u) { return __uint_as_float(u << 16); }
DI float bf_hi(unsigned u) { return __uint_as_float(u & 0xffff0000u); }
DI float rbf(float x) { return bf_lo(cvtpk(x, x)); }
DI bf16x4 trread(lds_cptr p) { return __builtin_bit_cast(bf16x4, __builtin_amdgcn_ds_read_tr16_b64_v4i16((__attribute__((address_space(3))) v4i16_t*)p)); }
DI bf16x8 trfrag(lds_cptr p0, lds_cptr p1) { const bf16x4 a = trread(p0), b = trread(p1); return (bf16x8){a[0], a[1], a[2], a[3], b[0], b[1], b[2], b[3]}; }
DI bf16x8 join(bf16x4 a, bf16x4 b) { return (bf16x8){a[0], a[1], a[2], a[3], b[0], b[1], b[2], b[3]}; }
DI float swap16(float x) {
    auto r = __builtin_amdgcn_permlane16_swap(__float_as_uint(x), __float_as_uint(x), false, false);
    return __uint_as_float((threadIdx.x & 16) ? r[0] : r[1]);
}
template <int CTRL> DI float dppf(float x) { return __uint_as_float(__builtin_amdgcn_update_dpp(0u, __float_as_uint(x), CTRL, 0xf, 0xf, true)); }
DI float rowmax16(float x) {
    x = fmaxf(x, dppf<0xB1>(x));
    x = fmaxf(x, dppf<0x4E>(x));
    x = fmaxf(x, dppf<0x141>(x));
    return fmaxf(x, dppf<0x140>(x));
}
DI float swap32(float x) { auto r = __builtin_amdgcn_permlane32_swap(__float_as_uint(x), __float_as_uint(x), false, false); return __uint_as_float((threadIdx.x & 32) ? r[0] : r[1]); }

DI void glds16(const void* sbase, unsigned voff, unsigned lds_base) {
    asm volatile("s_mov_b32 m0, %2\n\ts_nop 0\n\tglobal_load_lds_dwordx4 %0, %1" :: "v"(voff), "s"(sbase), "s"(lds_base) : "memory");
}
#define STPOL "sc1"
DI void gstore16(void* sbase, unsigned voff, u32x4 v) { asm volatile("global_store_dwordx4 %0, %1, %2 " STPOL "\n\ts_nop 1" :: "v"(voff), "v"(v), "s"(sbase) : "memory"); }
DI void gstore8(void* sbase, unsigned voff, u32x2 v) { asm volatile("global_store_dwordx2 %0, %1, %2" :: "v"(voff), "v"(v), "s"(sbase) : "memory"); }
DI void gstore4(void* sbase, unsigned voff, unsigned v) { asm volatile("global_store_dword %0, %1, %2" :: "v"(voff), "v"(v), "s"(sbase) : "memory"); }
#define WAITV(n) asm volatile("s_waitcnt vmcnt(" #n ")" ::: "memory")
#define WAITV_BAR(n) asm volatile("s_waitcnt vmcnt(" #n ") lgkmcnt(0)\n\ts_barrier" ::: "memory")

DI int wo_row(int ks, int kg, int j) {
    return ks < 16 ? DH * (2 * ks + (kg >> 1)) + 8 * (j >> 2) + 4 * (kg & 1) + (j & 3) : DH * (16 * (ks - 16) + 4 * kg + (j >> 1)) + 16 + (j & 1);
}
DI float sum32(float x) {
    x += dppf<0xB1>(x);
    x += dppf<0x4E>(x);
    x += dppf<0x141>(x);
    x += dppf<0x140>(x);
    return x + swap16(x);
}

__global__ __launch_bounds__(1024) void k_prep(const unsigned short* __restrict__ x, const unsigned short* __restrict__ wqkv, const unsigned short* __restrict__ wo, char* __restrict__ ws) {
    STAMP_BEGIN;
    const int tid = threadIdx.x;
    if (tid < 256) {
        if (blockIdx.x == 0 && tid < 64) *(unsigned*)(ws + WS_CNT + tid * 256) = 0u;
        const int row = blockIdx.x * 8 + (tid >> 5), t = tid & 31;
        const u32x4* xr = (const u32x4*)(x + (size_t)row * D);
        u32x4 v[3];
        v[0] = xr[t]; v[1] = xr[t + 32]; v[2] = (t < 8) ? xr[t + 64] : (u32x4){0u, 0u, 0u, 0u};
        float sum = 0.f;
#pragma unroll
        for (int i = 0; i < 3; ++i)
#pragma unroll
            for (int j = 0; j < 4; ++j) sum += bf_lo(v[i][j]) + bf_hi(v[i][j]);
        const float mean = sum32(sum) * (1.0f / D);
        float sq = 0.f;
#pragma unroll
        for (int i = 0; i < 3; ++i)
            if (i < 2 || t < 8)
#pragma unroll
                for (int j = 0; j < 4; ++j) { const float a = bf_lo(v[i][j]) - mean, b = bf_hi(v[i][j]) - mean; sq += a * a + b * b; }
        const float mb = rbf(mean);
        const float den = rbf(__fsqrt_rn(rbf(rbf(sum32(sq) * (1.0f / D)) + 1e-5f)));
        const float rc = __builtin_amdgcn_rcpf(den);
        auto quot = [&](float a) { const float q = a * rc; return __builtin_fmaf(__builtin_fmaf(-q, den, a), rc, q); };
        char* dst = ws + WS_XNF + (size_t)(row >> 4) * KS * 1024 + (row & 15) * 16;
#pragma unroll
        for (int i = 0; i < 3; ++i) {
            if (i < 2 || t < 8) {
                const int p = t + 32 * i;
                unsigned o[4];
#pragma unroll
                for (int j = 0; j < 4; ++j) o[j] = cvtpk(quot(rbf(bf_lo(v[i][j]) - mb)), quot(rbf(bf_hi(v[i][j]) - mb)));
                char* d = dst + (p >> 2) * 1024 + (p & 1) * 512 + ((p >> 1) & 1) * 8;
                *(u32x2*)d = (u32x2){o[0], o[1]};
                *(u32x2*)(d + 256) = (u32x2){o[2], o[3]};
            }
        }
    } else if (tid < 256 + 576) {
        const int n = blockIdx.x * 576 + (tid - 256);
        const int pc = n & 3, r = (n >> 2) & 31, sel = (n >> 7) & 1, hs = n >> 8, s = hs % KS, h = hs / KS;
        const int c = pc ^ (2 * ((r >> 2) & 1)), t = c >> 1, g = c & 1;
        const unsigned* w = (const unsigned*)(wqkv + (size_t)(32 * s + r) * (3 * D) + DH * h);
        u32x4 o = {0u, 0u, 0u, 0u};
        if (sel == 0 || t == 0) {
            const int col = (sel == 0 ? t * D : 2 * D) + 8 * g;
            o = (u32x4){w[col / 2], w[col / 2 + 1], w[col / 2 + 2], w[col / 2 + 3]};
        } else if (g == 0) {
            o = (u32x4){w[8], w[D / 2 + 8], w[D + 8], 0u};
        }
        *(u32x4*)(ws + WS_WQP + (size_t)n * 16) = o;
    } else if (tid < 256 + 576 + 162) {
        const int n = blockIdx.x * 162 + (tid - (256 + 576));
        const int lane = n & 63, fs = n >> 6, ks = fs % KS, dt = fs / KS;
        unsigned short v[8];
#pragma unroll
        for (int j = 0; j < 8; ++j) v[j] = wo[(size_t)wo_row(ks, lane >> 4, j) * D + 16 * dt + (lane & 15)];
        *(u32x4*)(ws + WS_WOP + (size_t)n * 16) = (u32x4){v[0] | ((unsigned)v[1] << 16), v[2] | ((unsigned)v[3] << 16), v[4] | ((unsigned)v[5] << 16), v[6] | ((unsigned)v[7] << 16)};
    }
    STAMP_END(0);
}

#define G1_MAP 1
constexpr int G1_SLOT = 9 * 4096;
constexpr int G1_AHEAD = 3;

DI int img_aoff(int lane) {
    const int kg = lane >> 4, q = (lane & 15) >> 2, p = lane & 3;
    return (4 * kg + q) * 64 + (((p >> 1) ^ (2 * (kg & 1))) * 16) + 8 * (p & 1);
}
DI void g1_ldx(bf16x8& d, const char* base, unsigned lo, int ks) {
    asm volatile("global_load_dwordx4 %0, %1, %2 offset:%3" : "=v"(d) : "v"(lo), "s"(base + (ks >> 2) * 4096), "n"((ks & 3) * 1024));
}
DI float glu(float g, float u) { return u * __builtin_amdgcn_rcpf(1.0f + __builtin_amdgcn_exp2f(g * -LOG2E)); }

constexpr int g1_group(int u) { return u < 4 ? (u & 1) : u / 2; }
constexpr int g1_half(int u) { return u < 4 ? (u >> 1) : (u & 1); }
constexpr int g1_dma_at(int u, int g) {
    if (u == 0) return (g >= 12 && g <= 20 && !(g & 1)) ? 1 * 8 + (g - 12) / 2 : (g >= 22 && g <= 30 && !(g & 1)) ? 2 * 8 + (g - 22) / 2 : -1;
    if (u == 1) return -1;
    if (u == 2) return (g <= 16 && !(g & 3)) ? 3 * 8 + g / 4 : (g >= 20 && g <= 28 && !(g & 1)) ? 4 * 8 + (g - 20) / 2 : -1;
    if (u <= 9) { const int sp = 5; return (g % sp == 0 && g / sp < 5) ? (u + 2) * 8 + g / sp : -1; }
    return -1;
}
constexpr int g1_x_at(int u, int g) {
    if (g & 3) return -1;
    if (u == 0) return g / 4 < 3 ? 6 + g / 4 : -1;
    if (u == 1) return 9 + g / 4;
    return -1;
}
constexpr int g1_x_younger(int ks, int u, int g) {
    int n = 0; bool seen = ks < 6;
    if (ks < 6) n += 2 * (5 - ks);
    for (int uu = 0; uu <= u; ++uu)
        for (int gg = 0; gg < 36 && (uu < u || gg <= g); ++gg) {
            const int x = g1_x_at(uu, gg);
            if (x >= 0) { if (seen) n += 2; if (x == ks) seen = true; }
            if (g1_dma_at(uu, gg) >= 0 && seen) n += 1;
        }
    return n;
}

struct G1Ctx {
    const char *wrow, *wq, *xs;
    char* hdst;
    unsigned lds0, dsto, voff, lo;
    int fbase, fsingle, w;
};
DI void g1_piece(const G1Ctx& c, int unit, int m) {
    const int grp = g1_group(unit), hk = g1_half(unit);
    const unsigned slot = c.lds0 + (unit & 3) * G1_SLOT;
    const int mm = (m == 4 && c.w >= 4) ? 3 : m;
    if (grp == 0) {
        const int pi = c.w + 8 * mm;
        glds16(c.wq + hk * G1_SLOT + pi * 1024, c.lo, slot + pi * 1024);
    } else {
        const int f0 = grp == 5 ? c.fsingle : c.fbase + 32 * (grp - 1);
        glds16(c.wrow + (size_t)(288 * hk + 64 * mm) * (F * 2) + f0 * 2, c.voff, slot + c.dsto + mm * 8192);
    }
}

struct G1Epi { float e[2], h[2]; u32x4 pk; };
DI void g1_epi_slice(int s, const f32x4 (&c)[2][2][2], G1Epi& t, char* hdst, unsigned lo) {
    const int v = s >> 2, nh = v >> 3, j = v & 7, ph = s & 3;
    if (ph == 0) { t.e[v & 1] = __builtin_amdgcn_exp2f(c[0][j >> 2][nh][j & 3] * -LOG2E); asm volatile("" : "+v"(t.e[v & 1])); }
    if (ph == 1) { t.e[v & 1] = __builtin_amdgcn_rcpf(1.0f + t.e[v & 1]); asm volatile("" : "+v"(t.e[v & 1])); }
    if (ph == 2) { t.h[v & 1] = c[1][j >> 2][nh][j & 3] * t.e[v & 1]; asm volatile("" : "+v"(t.h[v & 1])); }
    if (ph == 3 && (v & 1)) {
        t.pk[j >> 1] = cvtpk(t.h[0], t.h[1]);
        asm volatile("" : "+v"(t.pk[j >> 1]));
        if (j == 7) gstore16(hdst + (size_t)nh * FS * 1024, lo, t.pk);
    }
}

template <int U>
DI void g1_unit(const G1Ctx& cx, lds_cptr A0, lds_cptr A1, bf16x8 (&xb)[2][KS], f32x4 (&c)[2][2][2], const f32x4 (&cprev)[2][2][2]) {
    constexpr int GRP = g1_group(U), HK = g1_half(U);
    constexpr bool PAIR = GRP != 5;
    constexpr int PER = PAIR ? 4 : 2, N = 9 * PER;
    constexpr bool EPI = U >= 4;
    bf16x4 flo[4], fhi[4];
    lds_cptr a0 = A0 + (U & 3) * G1_SLOT, a1 = A1 + (U & 3) * G1_SLOT;
    asm volatile("" : "+v"(a0), "+v"(a1));
    auto addr = [&](int i) {
        const int ks = i / PER, sel = PAIR ? (i >> 1) & 1 : i & 1, t = PAIR ? i & 1 : 0;
        return (t ? a1 : a0) + ks * 4096 + sel * 2048;
    };
#pragma unroll
    for (int i = 0; i < G1_AHEAD; ++i) { flo[i] = trread(addr(i)); fhi[i] = trread(addr(i) + 1024); }
    if (HK == 0) {
#pragma unroll
        for (int i = 0; i < 8; ++i) c[i >> 2][(i >> 1) & 1][i & 1] = (f32x4){};
    }
    G1Epi ep;
    char* hprev = cx.hdst + (U / 2 - 2) * 1024;
    static_for<0, N>([&](auto ic) {
        constexpr int i = decltype(ic)::value;
        constexpr int kl = i / PER, ks = 9 * HK + kl, sel = PAIR ? (i >> 1) & 1 : i & 1, t = PAIR ? i & 1 : 0;
        constexpr int g = PAIR ? i : 2 * i;
        constexpr int x = g1_x_at(U, g), dm = g1_dma_at(U, g);
        if constexpr (x >= 0) { g1_ldx(xb[0][x], cx.xs, cx.lo, x); g1_ldx(xb[1][x], cx.xs + KS * 1024, cx.lo, x); }
        if constexpr (dm >= 0) g1_piece(cx, dm >> 3, dm & 7);
        if constexpr ((U == 0 || U == 2) && (i & 3) == 0) {
            constexpr int young = g1_x_younger(ks, U, g);
            asm volatile("s_waitcnt vmcnt(%2)" : "+v"(xb[0][ks]), "+v"(xb[1][ks]) : "n"(young));
        }
        constexpr bool SWAP = GRP == 0 && sel == 1 && t == 0;
        const bf16x8 fr = join(flo[i & 3], fhi[i & 3]);
        c[sel][t][0] = SWAP ? MFMA32K(xb[0][ks], fr, c[sel][t][0]) : MFMA32K(fr, xb[0][ks], c[sel][t][0]);
        SBAR();
        if constexpr (i + G1_AHEAD < N) flo[(i + G1_AHEAD) & 3] = trread(addr(i + G1_AHEAD));
        SBAR();
        c[sel][t][1] = SWAP ? MFMA32K(xb[1][ks], fr, c[sel][t][1]) : MFMA32K(fr, xb[1][ks], c[sel][t][1]);
        SBAR();
        if constexpr (i + G1_AHEAD < N) fhi[(i + G1_AHEAD) & 3] = trread(addr(i + G1_AHEAD) + 1024);
        SBAR();
        if constexpr (EPI && PAIR) {
            constexpr int sl = 36 * HK + i - 4;
            if constexpr (sl >= 0 && sl < 64) g1_epi_slice(sl, cprev, ep, hprev, cx.lo);
        } else if constexpr (EPI) {
            constexpr int sl = 2 * (18 * HK + i - 2);
            if constexpr (sl >= 0 && sl < 64) { g1_epi_slice(sl, cprev, ep, hprev, cx.lo); g1_epi_slice(sl + 1, cprev, ep, hprev, cx.lo); }
        }
        SBAR();
    });
}
DI void g1_epi_qkv(const f32x4 (&c)[2][2][2], char* ws, int h, int tb, int lane, lds_ptr stg) {
    typedef __attribute__((address_space(3))) u32x2* l64;
    typedef __attribute__((address_space(3))) unsigned* l32;
    typedef __attribute__((address_space(3))) unsigned short* l16;
    asm volatile("" : "+v"(lane));
    const int t = lane & 15, g4 = lane >> 4;
    const lds_ptr rec = stg + QREC;
    float kmax = 0.f;
#pragma unroll
    for (int nh = 0; nh < 2; ++nh) {
        const int r = 16 * nh + t;
        const unsigned fo = (r + 32 * (g4 >> 1)) * 16 + (g4 & 1) * 8;
        const f32x4 q = c[0][0][nh], k = c[0][1][nh], v = c[1][0][nh], e = c[1][1][nh];
        *(l64)(stg + fo) = (u32x2){cvtpk(q[0] * QSCALE, q[1] * QSCALE), cvtpk(q[2] * QSCALE, q[3] * QSCALE)};
        const u32x2 kp = {cvtpk(k[0], k[1]), cvtpk(k[2], k[3])};
        *(l64)(rec + fo) = kp;
        *(l64)(rec + 1280 + nh * 640 + (g4 & 1) * 320 + t * 16 + (g4 >> 1) * 8) = (u32x2){cvtpk(v[0], v[1]), cvtpk(v[2], v[3])};
        const unsigned e01 = cvtpk(e[0], e[1]), e23 = cvtpk(e[2], e[3]);
        const lds_ptr vt = rec + 1280 + nh * 640 + ((t >> 2) & 1) * 320 + (4 * (t >> 3) + (t & 3)) * 2;
        if (g4 == 0) {
            *(l64)(rec + 1024 + r * 8) = (u32x2){e23, 0u};
            *(l32)(stg + 1024 + r * 4) = cvtpk(e[0] * QSCALE, e[1] * QSCALE);
        } else {
            *(l16)(vt + (g4 == 1 ? 16 : 16 + g4) * 16) = (unsigned short)(g4 == 1 ? e01 : (g4 == 2 ? 0x3f80u : 0u));
            if (g4 == 1) *(l16)(vt + 17 * 16) = (unsigned short)(e01 >> 16);
        }
        float n2 = g4 == 0 ? bf_lo(e23) * bf_lo(e23) + bf_hi(e23) * bf_hi(e23) : 0.f;
        n2 += bf_lo(kp[0]) * bf_lo(kp[0]) + bf_hi(kp[0]) * bf_hi(kp[0]) + bf_lo(kp[1]) * bf_lo(kp[1]) + bf_hi(kp[1]) * bf_hi(kp[1]);
        n2 += swap16(n2);
        n2 += swap32(n2);
        kmax = fmaxf(kmax, n2);
    }
    kmax = rowmax16(kmax);
    if (lane == 0) *(l32)(stg + 1152) = __float_as_uint(kmax);
    typedef __attribute__((address_space(3))) const u32x4* l128;
    char* qg = ws + WS_Q + ((size_t)h * NTB + tb) * QREC;
    char* kg = ws + WS_KV + ((size_t)h * NTB + tb) * KVREC;
    const unsigned o0 = lane * 16, oq = min(lane, 8) * 16 + 1024, ok = min(lane, 31) * 16 + 2048;
    const u32x4 d0 = *(l128)(stg + o0), d1 = *(l128)(stg + oq), d2 = *(l128)(rec + o0), d3 = *(l128)(rec + 1024 + o0), d4 = *(l128)(rec + ok);
    gstore16(qg, o0, d0);
    gstore16(qg, oq, d1);
    gstore16(kg, o0, d2);
    gstore16(kg, o0 + 1024, d3);
    gstore16(kg, ok, d4);
}

__global__ __launch_bounds__(512, 2) void k_gemm1(const char* wg, const char* wu, const char* wd, char* ws) {
    extern __shared__ __attribute__((aligned(16))) char lds[];
    STAMP_BEGIN;
    const int tid = threadIdx.x, lane = tid & 63;
    const int w = __builtin_amdgcn_readfirstlane(tid >> 6);
    const int xcd = blockIdx.x & 7, idx = blockIdx.x >> 3;
    const int cg = 8 * (xcd >> 1) + (idx & 7), rg = 4 * (xcd & 1) + (idx >> 3), tb = 8 * rg + w;
    const bool late = w >= 4;
    MARKS; MARK(0);

    const int odd = cg & 1;
    G1Ctx cx;
    cx.w = w; cx.lds0 = (unsigned)(uintptr_t)lds; cx.lo = lane * 16;
    cx.fbase = 144 * cg + 16 * odd; cx.fsingle = odd ? 144 * cg - 16 : 144 * cg + 128;
    cx.wq = ws + WS_WQP + (size_t)cg * KS * 4096;
    cx.xs = ws + WS_XNF + (size_t)(2 * tb) * KS * 1024;
    cx.wrow = ((w & 1) ? wu : wg) + (size_t)(32 * (w >> 2) + 16 * ((w >> 1) & 1)) * (F * 2);
    cx.dsto = (w >> 2) * 4096 + (w & 1) * 2048 + ((w >> 1) & 1) * 1024;
    cx.voff = (lane >> 2) * (F * 2) + (((lane & 3) ^ (2 * ((lane >> 4) & 1))) * 16);
    cx.hdst = ws + WS_HF + ((size_t)(2 * tb) * FS + cx.fbase / 32) * 1024;

#pragma unroll
    for (int m = 0; m < 5; ++m) g1_piece(cx, 0, m);
    bf16x8 xb[2][KS];
#pragma unroll
    for (int s = 0; s < 6; ++s) { g1_ldx(xb[0][s], cx.xs, cx.lo, s); g1_ldx(xb[1][s], cx.xs + KS * 1024, cx.lo, s); }

    const int aoff0 = img_aoff(lane);
    const lds_cptr A0 = (lds_cptr)lds + aoff0, A1 = (lds_cptr)lds + (aoff0 ^ 32);
    f32x4 ca[2][2][2], cb[2][2][2];
    u32x4 dead0, dead1, dead2;

    WAITV_BAR(12);
    MARK(1);
    g1_unit<0>(cx, A0, A1, xb, ca, cb); WAITV_BAR(5);
    g1_unit<1>(cx, A0, A1, xb, cb, ca); WAITV_BAR(18);
    MARK(2);
    g1_unit<2>(cx, A0, A1, xb, ca, cb); WAITV_BAR(5);
    MARK(3);
    const lds_ptr stg = (lds_ptr)lds + 2 * G1_SLOT + w * 4608;
    if (!late) g1_epi_qkv(ca, ws, cg, tb, lane, stg);
    g1_unit<3>(cx, A0, A1, xb, cb, ca);
    if (late) { g1_epi_qkv(ca, ws, cg, tb, lane, stg); WAITV_BAR(10); } else { WAITV_BAR(5); }
    MARK(4);
    g1_unit<4>(cx, A0, A1, xb, ca, cb); WAITV_BAR(5);
    g1_unit<5>(cx, A0, A1, xb, ca, cb); WAITV_BAR(5);
    MARK(5);
    g1_unit<6>(cx, A0, A1, xb, cb, ca); WAITV_BAR(5);
    g1_unit<7>(cx, A0, A1, xb, cb, ca); WAITV_BAR(5);
    MARK(6);
    g1_unit<8>(cx, A0, A1, xb, ca, cb);
    {
        const char* src = wd + (size_t)(xcd >> 1) * (1152 * D * 2) + ((xcd & 1) * 256 + idx * 8 + w) * 2592;
        asm volatile("global_load_dwordx4 %0, %3, %4\n\tglobal_load_dwordx4 %1, %3, %4 offset:1024\n\tglobal_load_dwordx4 %2, %3, %4 offset:1568"
                     : "=&v"(dead0), "=&v"(dead1), "=&v"(dead2) : "v"(cx.lo), "s"(src) : "memory");
        WAITV_BAR(8);
    }
    g1_unit<9>(cx, A0, A1, xb, ca, cb); WAITV_BAR(5);
    asm volatile("" :: "v"(dead0), "v"(dead1), "v"(dead2));
    MARK(7);
    const lds_cptr AS = odd ? A1 : A0;
    g1_unit<10>(cx, AS, AS, xb, cb, ca); WAITV_BAR(0);
    g1_unit<11>(cx, AS, AS, xb, cb, ca);
    {
        char* hs = ws + WS_HF + ((size_t)(2 * tb) * FS + cx.fsingle / 32) * 1024 + odd * 8;
#pragma unroll
        for (int nh = 0; nh < 2; ++nh) {
            float h[4];
#pragma unroll
            for (int j = 0; j < 4; ++j) h[j] = glu(cb[0][0][nh][j], cb[1][0][nh][j]);
            gstore8(hs + (size_t)nh * FS * 1024, cx.lo, (u32x2){cvtpk(h[0], h[1]), cvtpk(h[2], h[3])});
        }
    }
    MARK(8); MARK_DUMP(1, 9);
    STAMP_END(1);
}

constexpr int AT_STAGE = 8 * KVREC;
constexpr float AT_BOUND_MAX = 50.0f;

struct KFrag { bf16x8 k1; bf16x4 k2; };
struct VFrag { bf16x8 v0, v1; };
DI KFrag at_readk(lds_cptr rec, int lane) {
    KFrag k;
    k.k1 = *(const __attribute__((address_space(3))) bf16x8*)(rec + lane * 16);
    k.k2 = *(const __attribute__((address_space(3))) bf16x4*)(rec + 1024 + (lane & 31) * 8);
    return k;
}
DI VFrag at_readv(lds_cptr recv) {
    VFrag v;
    v.v0 = *(const __attribute__((address_space(3))) bf16x8*)(recv);
    v.v1 = *(const __attribute__((address_space(3))) bf16x8*)(recv + 640);
    return v;
}
DI bf16x8 packp(const f32x16& p, int s) {
    return __builtin_bit_cast(bf16x8, (u32x4){cvtpk(p[8 * s], p[8 * s + 1]), cvtpk(p[8 * s + 2], p[8 * s + 3]), cvtpk(p[8 * s + 4], p[8 * s + 5]), cvtpk(p[8 * s + 6], p[8 * s + 7])});
}

DI void at_dma(const char* kvh, int stage, unsigned ldsb, int w, int lane) {
    const char* src = kvh + (size_t)stage * AT_STAGE + w * 5 * 1024;
    const unsigned dst = ldsb + w * 5 * 1024, lo = lane * 16;
    asm volatile("s_mov_b32 m0, %2\n\ts_nop 0\n\t"
                 "global_load_lds_dwordx4 %0, %1\n\tglobal_load_lds_dwordx4 %0, %1 offset:1024\n\t"
                 "global_load_lds_dwordx4 %0, %1 offset:2048\n\tglobal_load_lds_dwordx4 %0, %1 offset:3072"
                 :: "v"(lo), "s"(src), "s"(dst) : "memory");
    glds16(src + 4096, lo, dst + 4096);
}

#define AMFMA16_NEW(d, a, b, c) asm volatile("v_mfma_f32_32x32x16_bf16 %0, %1, %2, %3" : "=&v"(d) : "v"(a), "v"(b), "v"(c))
#define AMFMA16_ACC(d, a, b) asm volatile("v_mfma_f32_32x32x16_bf16 %0, %1, %2, %0" : "+v"(d) : "v"(a), "v"(b))
#define AMFMA8_ACC(d, a, b) asm volatile("v_mfma_f32_32x32x8_bf16 %0, %1, %2, %0" : "+v"(d) : "v"(a), "v"(b))
#define AT_EXP4(E, SX, a) do { _Pragma("unroll") for (int i_ = 0; i_ < 4; ++i_) asm volatile("v_exp_f32 %0, %1" : "=v"(E[i_]) : "v"(SX[a + i_])); } while (0)
#define AT_PK2(DST, k, E) do { asm volatile("v_cvt_pk_bf16_f32 %0, %1, %2" : "=v"(DST[k]) : "v"(E[0]), "v"(E[1]));   \
                               asm volatile("v_cvt_pk_bf16_f32 %0, %1, %2" : "=v"(DST[k + 1]) : "v"(E[2]), "v"(E[3])); } while (0)
#define AT_STEP(SX, PX0, PX1, SY, CY, QY1, QY2, KF, OY, PY0, PY1, VF, LOADS)                                   \
    do {                                                                                                       \
        float e0_[4], e1_[4], e2_[4];                                                                          \
        AMFMA16_NEW(SY, KF.k1, QY1, CY);                                                                       \
        LOADS;                                                                                                 \
        SBAR();                                                                                                \
        AT_EXP4(e0_, SX, 0);                                                                                   \
        AT_PK2(PY1, 2, etail);                                                                                 \
        AMFMA8_ACC(SY, KF.k2, QY2);                                                                            \
        AT_EXP4(e1_, SX, 4);                                                                                   \
        AT_PK2(PX0, 0, e0_);                                                                                   \
        AMFMA16_ACC(OY, VF.v0, PY0);                                                                           \
        AT_EXP4(e2_, SX, 8);                                                                                   \
        AT_PK2(PX0, 2, e1_);                                                                                   \
        AMFMA16_ACC(OY, VF.v1, PY1);                                                                           \
        AT_EXP4(etail, SX, 12);                                                                                \
        AT_PK2(PX1, 0, e2_);                                                                                   \
        SBAR();                                                                                                \
    } while (0)

DI void at_online(const KFrag& k, const VFrag& v, bf16x8 q1, bf16x4 q2, float& m, f32x16& o) {
    f32x16 z = {};
    f32x16 s = MFMA16(k.k1, q1, z);
    s = MFMA8(k.k2, q2, s);
    float t = s[0];
#pragma unroll
    for (int i = 1; i < 16; ++i) t = fmaxf(t, s[i]);
    t = fmaxf(t, swap32(t));
    const float mn = fmaxf(m, t);
    const float alpha = __builtin_amdgcn_exp2f(m - mn);
    m = mn;
#pragma unroll
    for (int i = 0; i < 16; ++i) { s[i] = __builtin_amdgcn_exp2f(s[i] - mn); o[i] *= alpha; }
    o = MFMA16(v.v0, packp(s, 0), o);
    o = MFMA16(v.v1, packp(s, 1), o);
}

DI void at_store(const f32x16& o, char* ws, int h, int tb, int lane) {
    const int r = lane & 31, hi = lane >> 5;
    const float other = swap32(o[10]);
    const float inv = 1.0f / (hi ? other : o[10]);
    char* of = ws + WS_OF + (size_t)(2 * tb + (r >> 4)) * KS * 1024;
    *(u32x4*)(of + (h >> 1) * 1024 + ((r & 15) + 16 * (2 * (h & 1) + hi)) * 16) =
        (u32x4){cvtpk(o[0] * inv, o[1] * inv), cvtpk(o[2] * inv, o[3] * inv), cvtpk(o[4] * inv, o[5] * inv), cvtpk(o[6] * inv, o[7] * inv)};
    if (hi == 0) *(unsigned*)(of + (16 + (h >> 4)) * 1024 + ((r & 15) + 16 * ((h >> 2) & 3)) * 16 + (h & 3) * 4) = cvtpk(o[8] * inv, o[9] * inv);
}

DI float at_qnorm2(bf16x8 q1, bf16x4 q2) {
    const u32x4 a = __builtin_bit_cast(u32x4, q1); const u32x2 b = __builtin_bit_cast(u32x2, q2);
    float n = bf_lo(b[0]) * bf_lo(b[0]) + bf_hi(b[0]) * bf_hi(b[0]);
#pragma unroll
    for (int j = 0; j < 4; ++j) n += bf_lo(a[j]) * bf_lo(a[j]) + bf_hi(a[j]) * bf_hi(a[j]);
    return n + swap32(n);
}

DI void attn_block(char* ws, char* lds, int bid) {
    const int tid = threadIdx.x, lane = tid & 63, hi = lane >> 5;
    const int w = __builtin_amdgcn_readfirstlane(tid >> 6);
    const int xcd = bid & 7, idx = bid >> 3;
    const int h = 4 * xcd + (idx & 3), qg = idx >> 2;
    const int tbA = 8 * qg + 2 * w, tbB = tbA + 1;
    const unsigned lds0 = (unsigned)(uintptr_t)lds;
    const char* kvh = ws + WS_KV + (size_t)h * NTB * KVREC;
    MARKS; MARK(0);

    bf16x8 qA1, qB1; unsigned qa2, qb2; float km;
    {
        const char* q1 = ws + WS_Q + ((size_t)h * NTB + tbA) * QREC;
        const char* kx = ws + WS_Q + (size_t)h * NTB * QREC + 1152;
        const unsigned l16 = lane * 16, l4 = (lane & 31) * 4 + 1024, lk = lane * QREC;
        asm volatile("global_load_dword %0, %1, %2" : "=v"(km) : "v"(lk), "s"(kx));
        asm volatile("global_load_dwordx4 %0, %1, %2" : "=v"(qA1) : "v"(l16), "s"(q1));
        asm volatile("global_load_dwordx4 %0, %1, %2 offset:%3" : "=v"(qB1) : "v"(l16), "s"(q1), "n"(QREC));
        asm volatile("global_load_dword %0, %1, %2" : "=v"(qa2) : "v"(l4), "s"(q1));
        asm volatile("global_load_dword %0, %1, %2 offset:%3" : "=v"(qb2) : "v"(l4), "s"(q1), "n"(QREC));
    }
    at_dma(kvh, 0, lds0, w, lane);
    at_dma(kvh, 1, lds0 + AT_STAGE, w, lane);
    asm volatile("s_waitcnt vmcnt(10)" : "+v"(km), "+v"(qA1), "+v"(qB1), "+v"(qa2), "+v"(qb2));
    const bf16x4 qA2 = __builtin_bit_cast(bf16x4, (u32x2){hi ? 0u : qa2, 0u});
    const bf16x4 qB2 = __builtin_bit_cast(bf16x4, (u32x2){hi ? 0u : qb2, 0u});
    km = rowmax16(km);
    km = fmaxf(km, swap16(km));
    km = fmaxf(km, swap32(km));
    const float mA = __builtin_amdgcn_sqrtf(at_qnorm2(qA1, qA2) * km) * 1.001f, mB = __builtin_amdgcn_sqrtf(at_qnorm2(qB1, qB2) * km) * 1.001f;
    const bool fast = __all(mA <= AT_BOUND_MAX && mB <= AT_BOUND_MAX);

    const int voffs = 1280 + hi * 320 + min(lane & 31, 19) * 16;
    const lds_cptr L = (lds_cptr)lds;
    f32x16 oA = {}, oB = {};

    if (fast) {
        f32x16 cA, cB;
#pragma unroll
        for (int i = 0; i < 16; ++i) { cA[i] = -mA; cB[i] = -mB; }
        asm volatile("" : "+v"(cA), "+v"(cB));
        f32x16 sA, sB;
        u32x4 pA0 = {}, pA1 = {}, pB0 = {}, pB1 = {};
        float etail[4] = {0.f, 0.f, 0.f, 0.f};
        KFrag k0, k1; VFrag v0, v1;
        MARK(1);
        WAITV_BAR(5);
        MARK(2);
        k0 = at_readk(L, lane);
        v1 = at_readv(L + voffs);
        sA = MFMA16(k0.k1, qA1, cA); sA = MFMA8(k0.k2, qA2, sA);
        asm volatile("s_nop 15" : "+v"(sA));
        int slot = 0;
        for (int st = 0; st < 8; ++st) {
            const lds_cptr cur = L + slot * AT_STAGE;
            const int nslot = slot == 2 ? 0 : slot + 1;
#pragma unroll
            for (int t = 0; t < 8; t += 2) {
                AT_STEP(sA, pA0, pA1, sB, cB, qB1, qB2, k0, oB, pB0, pB1, v1,
                        (k1 = at_readk(cur + (t + 1) * KVREC, lane), v0 = at_readv(cur + t * KVREC + voffs)));
                AT_STEP(sB, pB0, pB1, sA, cA, qA1, qA2, k1, oA, pA0, pA1, v0, (void)0);
                lds_cptr nk = cur + (t + 2) * KVREC;
                if (t == 6) {
                    if (st < 7) {
                        if (st < 6) { WAITV_BAR(0); at_dma(kvh, st + 2, lds0 + (nslot == 2 ? 0 : nslot + 1) * AT_STAGE, w, lane); }
                        else WAITV_BAR(0);
                        nk = L + nslot * AT_STAGE;
                    } else nk = cur;
                }
                AT_STEP(sA, pA0, pA1, sB, cB, qB1, qB2, k1, oB, pB0, pB1, v0,
                        (k0 = at_readk(nk, lane), v1 = at_readv(cur + (t + 1) * KVREC + voffs)));
                AT_STEP(sB, pB0, pB1, sA, cA, qA1, qA2, k0, oA, pA0, pA1, v1, (void)0);
            }
            slot = nslot;
            MARK(3 + st);
        }
        asm volatile("s_nop 1");
        AT_PK2(pB1, 2, etail);
        asm volatile("s_nop 1" : "+v"(pB1));
        AMFMA16_ACC(oB, v1.v0, pB0);
        AMFMA16_ACC(oB, v1.v1, pB1);
        asm volatile("s_nop 15\n\ts_nop 7" : "+v"(oA), "+v"(oB));
    } else {
        float mxA = -INFINITY, mxB = -INFINITY;
        WAITV_BAR(5);
        int slot = 0;
        for (int st = 0; st < 8; ++st) {
            const lds_cptr cur = L + slot * AT_STAGE;
            const int nslot = slot == 2 ? 0 : slot + 1;
            for (int t = 0; t < 8; ++t) {
                const KFrag k = at_readk(cur + t * KVREC, lane);
                const VFrag v = at_readv(cur + t * KVREC + voffs);
                at_online(k, v, qA1, qA2, mxA, oA);
                at_online(k, v, qB1, qB2, mxB, oB);
            }
            if (st < 7) {
                WAITV_BAR(0);
                if (st < 6) at_dma(kvh, st + 2, lds0 + (nslot == 2 ? 0 : nslot + 1) * AT_STAGE, w, lane);
            }
            slot = nslot;
        }
    }
    at_store(oA, ws, h, tbA, lane);
    at_store(oB, ws, h, tbB, lane);
    MARK(11); MARK_DUMP(2, 12);
}

__global__ __launch_bounds__(256, 2) void k_attn(char* ws) {
    extern __shared__ __attribute__((aligned(16))) char lds[];
    STAMP_BEGIN;
    attn_block(ws, lds, blockIdx.x);
    STAMP_END(2);
}

constexpr int DN_STEP = 32 * 288, DN_SLOT = 2 * DN_STEP, DN_LDS = 4 * DN_SLOT, DN_NST = 18;
constexpr int dn_piece(int st, int i) {
    if ((i == 3 || i == 7 || i == 11) && st + 3 < DN_NST) return (st + 3) * 8 + 2 + (i - 3) / 4;
    if ((i == 15 || i == 17) && st + 4 < DN_NST) return (st + 4) * 8 + (i - 15) / 2;
    return -1;
}
constexpr int dn_young(int st) {
    int n = 0; bool seen = st + 1 == 1;
    const int need = st + 1;
    for (int s2 = 0; s2 <= st; ++s2)
        for (int i = 0; i < 18; ++i) {
            if (s2 == st && i >= 15) break;
            if (i == 0 && s2 + 2 < DN_NST) { if (seen) n += 4; if (s2 + 2 == need) { seen = true; n = 0; } }
            const int pc = dn_piece(s2, i);
            if (pc >= 0) { if (seen) n += 1; if ((pc >> 3) == need) { seen = true; n = 0; } }
        }
    return n;
}
DI void dn_ldb(bf16x8 (&b)[2][2], const char* hs, unsigned lo, int st) {
#pragma unroll
    for (int k = 0; k < 2; ++k) {
        asm volatile("global_load_dwordx4 %0, %1, %2 offset:%3" : "=v"(b[0][k]) : "v"(lo), "s"(hs + (size_t)st * 2048), "n"(k * 1024));
        asm volatile("global_load_dwordx4 %0, %1, %2 offset:%3" : "=v"(b[1][k]) : "v"(lo), "s"(hs + (size_t)FS * 1024 + (size_t)st * 2048), "n"(k * 1024));
    }
}
template <int N> DI void dn_wait(bf16x8 (&b)[2][2]) {
    asm volatile("s_waitcnt vmcnt(%4) lgkmcnt(0)\n\ts_barrier" : "+v"(b[0][0]), "+v"(b[0][1]), "+v"(b[1][0]), "+v"(b[1][1]) : "n"(N) : "memory");
}

DI void down_block(const char* wd, char* ws, char* lds, int bid) {
    const int tid = threadIdx.x, lane = tid & 63;
    const int w = __builtin_amdgcn_readfirstlane(tid >> 6);
    const int xcd = bid & 7, idx = bid >> 3;
    const int kq = xcd >> 1, tg = (xcd & 1) * 8 + (idx >> 2), dq = idx & 3;
    const int tb16 = 8 * tg + 2 * w, fs0 = 36 * kq;
    const unsigned lds0 = (unsigned)(uintptr_t)lds;
    unsigned voff[5], doff[5];
#pragma unroll
    for (int m = 0; m < 5; ++m) { const int i = min(5 * (w & 1) + m, 8), n = 64 * i + lane; voff[m] = (n / 18) * (D * 2) + (n % 18) * 16; doff[m] = (w >> 1) * DN_STEP + i * 1024; }
    const char* wsrc = wd + (size_t)(fs0 + (w >> 1)) * 32 * (D * 2) + dq * 288;
    const lds_cptr A = (lds_cptr)lds + (4 * (lane >> 4) + ((lane & 15) >> 2)) * 288 + (lane & 3) * 8;
    const char* hs = ws + WS_HF + ((size_t)tb16 * FS + fs0) * 1024;
    const unsigned lo = lane * 16;
    MARKS; MARK(0);

    auto dma = [&](int st, int m) {
        const char* base = wsrc;
        asm volatile("" : "+s"(base));
        glds16(base + (size_t)st * 2 * 32 * (D * 2), voff[m], lds0 + (st & 3) * DN_SLOT + doff[m]);
    };
#pragma unroll
    for (int st = 0; st < 3; ++st)
#pragma unroll
        for (int m = 0; m < 5; ++m) dma(st, m);
    dma(3, 0); dma(3, 1);
    f32x4 acc[9][2];
#pragma unroll
    for (int i = 0; i < 9; ++i) { acc[i][0] = (f32x4){}; acc[i][1] = (f32x4){}; }
    bf16x8 b[3][2][2];
    dn_ldb(b[0], hs, lo, 0);
    dn_ldb(b[1], hs, lo, 1);
    lds_cptr Alo = A, Ahi = A + 2 * DN_SLOT;
    asm volatile("" : "+v"(Alo), "+v"(Ahi));
    auto addr = [&](int st, int i) { return ((st & 2) ? Ahi : Alo) + (st & 1) * DN_SLOT + (i / 9) * DN_STEP + (i % 9) * 32; };
    dn_wait<4>(b[0]);
    bf16x4 flo[4], fhi[4];
#pragma unroll
    for (int i = 0; i < 3; ++i) { flo[i] = trread(addr(0, i)); fhi[i] = trread(addr(0, i) + 16 * 288); }
    static_for<0, DN_NST * 18>([&](auto nc) {
        constexpr int n = decltype(nc)::value, st = n / 18, i = n % 18, set = st % 3;
        if constexpr (i == 0 && st + 2 < DN_NST) dn_ldb(b[(st + 2) % 3], hs, lo, st + 2);
        if constexpr (i == 15 && st + 1 < DN_NST) dn_wait<dn_young(st)>(b[(st + 1) % 3]);
        if constexpr (dn_piece(st, i) >= 0) dma(dn_piece(st, i) >> 3, dn_piece(st, i) & 7);
        constexpr bool more = n + 3 < DN_NST * 18;
        constexpr int nst = (n + 3) / 18, ni = (n + 3) % 18;
        const bf16x8 fr = join(flo[n & 3], fhi[n & 3]);
        acc[i % 9][0] = MFMA32K(fr, b[set][0][i / 9], acc[i % 9][0]);
        SBAR();
        if constexpr (more) flo[(n + 3) & 3] = trread(addr(nst, ni));
        SBAR();
        acc[i % 9][1] = MFMA32K(fr, b[set][1][i / 9], acc[i % 9][1]);
        SBAR();
        if constexpr (more) fhi[(n + 3) & 3] = trread(addr(nst, ni) + 16 * 288);
        SBAR();
    });
    MARK(1);
    char* dst = ws + WS_PART + (((size_t)kq * NT16 + tb16) * 36 + 9 * dq) * 1024;
#pragma unroll
    for (int nh = 0; nh < 2; ++nh)
#pragma unroll
        for (int i = 0; i < 9; ++i) gstore16(dst + (size_t)nh * 36 * 1024 + i * 1024, lo, __builtin_bit_cast(u32x4, acc[i][nh]));
    MARK(2);
    asm volatile("s_waitcnt vmcnt(0)\n\ts_barrier" ::: "memory");
    if (w == 0) {
        typedef __attribute__((address_space(1))) unsigned gu32;
        gu32* cnt = (gu32*)(ws + WS_CNT + (tg * 4 + dq) * 256);
        if (lane == 0) __hip_atomic_fetch_add(cnt, 1u, __ATOMIC_RELAXED, __HIP_MEMORY_SCOPE_AGENT);
        for (int spins = 0; spins < (1 << 20); ++spins) {
            if ((unsigned)__builtin_amdgcn_readfirstlane(__hip_atomic_load(cnt, __ATOMIC_RELAXED, __HIP_MEMORY_SCOPE_AGENT)) >= 4u) break;
            __builtin_amdgcn_s_sleep(4);
        }
        __builtin_amdgcn_fence(__ATOMIC_ACQUIRE, "agent");
        asm volatile("s_waitcnt vmcnt(0)" ::: "memory");
    }
    asm volatile("s_barrier" ::: "memory");
    MARK(3);
    f32x4 pq[5][4];
#pragma unroll
    for (int m = 0; m < 5; ++m) {
        const int idx = min(w + 4 * m, 17), nh = idx >= 9, i = idx - 9 * nh;
        const char* pp = ws + WS_PART + ((size_t)(8 * tg + 2 * kq + nh) * 36 + 9 * dq + i) * 1024 + lo;
#pragma unroll
        for (int k = 0; k < 4; ++k) pq[m][k] = *(const f32x4*)(pp + (size_t)k * NT16 * 36 * 1024);
    }
    asm volatile("" ::: "memory");
    SBAR();
#pragma unroll
    for (int m = 0; m < 5; ++m) {
        const int idx = min(w + 4 * m, 17), nh = idx >= 9, i = idx - 9 * nh;
        const f32x4 ffn = (pq[m][0] + pq[m][1]) + (pq[m][2] + pq[m][3]);
        float g[4];
#pragma unroll
        for (int c = 0; c < 4; ++c) { const float f = rbf(ffn[c]); g[c] = f * __builtin_amdgcn_rcpf(1.0f + __builtin_amdgcn_exp2f(f * -LOG2E)); }
        *(u32x2*)(ws + WS_G + ((size_t)(8 * tg + 2 * kq + nh) * 36 + 9 * dq + i) * 512 + lane * 8) = (u32x2){cvtpk(g[0], g[1]), cvtpk(g[2], g[3])};
    }
    MARK(4); MARK_DUMP(3, 5);
}

__global__ __launch_bounds__(256, 2) void k_down(const char* wd, char* ws) {
    extern __shared__ __attribute__((aligned(16))) char lds[];
    STAMP_BEGIN;
    down_block(wd, ws, lds, blockIdx.x);
    STAMP_END(3);
}

#define AT_PRIO 2
#define MIX_MODE 0
__global__ __launch_bounds__(256, 2) void k_mix(const char* wd, char* ws) {
    extern __shared__ __attribute__((aligned(16))) char lds[];
    STAMP_BEGIN;
    const int b = blockIdx.x;
    const bool attn = b < 256; const int bid = b & 255;
    if (attn) { __builtin_amdgcn_s_setprio(AT_PRIO); attn_block(ws, lds, bid); } else down_block(wd, ws, lds, bid);
    STAMP_END(5);
}

constexpr int FN_A = 3 * KS * 1024, FN_LDS = FN_A + 8 * 3 * 1024;
DI void ldx1(bf16x8& d, const char* base, unsigned lo, int off) {
    asm volatile("global_load_dwordx4 %0, %1, %2 offset:%3" : "=v"(d) : "v"(lo), "s"(base), "n"(off));
}
template <int N> DI void waitx(bf16x8& a, bf16x8& b) { asm volatile("s_waitcnt vmcnt(%2)" : "+v"(a), "+v"(b) : "n"(N)); }
DI void fn_ldgx(u32x2 (&gv)[3], u32x2 (&xv)[3], const char* gp, unsigned go, const char* xp, unsigned xo) {
#pragma unroll
    for (int t = 0; t < 3; ++t) {
        asm volatile("global_load_dwordx2 %0, %1, %2 offset:%3" : "=v"(gv[t]) : "v"(go), "s"(gp), "n"(t * 512));
        asm volatile("global_load_dwordx2 %0, %1, %2 offset:%3" : "=v"(xv[t]) : "v"(xo), "s"(xp), "n"(t * 32));
    }
}
__global__ __launch_bounds__(512, 1) void k_final(const unsigned short* x, char* ws, unsigned short* out) {
    extern __shared__ __attribute__((aligned(16))) char lds[];
    STAMP_BEGIN;
    const int tid = threadIdx.x, lane = tid & 63;
    const int w = __builtin_amdgcn_readfirstlane(tid >> 6);
    const int xcd = blockIdx.x & 7, idx = blockIdx.x >> 3;
    const int tg = 2 * xcd + idx / 12, dg = idx % 12, kh = w >> 2, tb16 = 8 * tg + 2 * (w & 3);
    const unsigned lds0 = (unsigned)(uintptr_t)lds, lo = lane * 16;
    MARKS; MARK(0);
    {
        const char* src = ws + WS_WOP + (size_t)dg * FN_A;
#pragma unroll
        for (int i = 0; i < 7; ++i) { const int n = min(w * 7 + i, 3 * KS - 1); glds16(src + n * 1024, lo, lds0 + n * 1024); }
    }
    const char* os = ws + WS_OF + ((size_t)tb16 * KS + 9 * kh) * 1024;
    bf16x8 b[2][9];
    auto ldo = [&](int s) { ldx1(b[0][s], os + (s >> 2) * 4096, lo, (s & 3) * 1024); ldx1(b[1][s], os + KS * 1024 + (s >> 2) * 4096, lo, (s & 3) * 1024); };
#pragma unroll
    for (int s = 0; s < 4; ++s) ldo(s);
    MARK(1);
    asm volatile("s_waitcnt vmcnt(8)\n\ts_barrier" ::: "memory");
    MARK(2);
    const int tok = (tb16 + kh) * 16 + (lane & 15);
    const char* gp = ws + WS_G + ((size_t)(tb16 + kh) * 36 + 3 * dg) * 512;
    const char* xp = (const char*)(x + (size_t)(tb16 + kh) * 16 * D + dg * 48);
    const unsigned go = lane * 8, xo2 = ((lane & 15) * D + 4 * (lane >> 4)) * 2;
    u32x2 gv[3], xv[3];
    const lds_cptr A = (lds_cptr)lds + lo + 9 * kh * 1024;
    f32x4 acc[2][3] = {};
    static_for<0, 9>([&](auto sc) {
        constexpr int s = decltype(sc)::value;
        bf16x8 a[3];
#pragma unroll
        for (int t = 0; t < 3; ++t) a[t] = *(const __attribute__((address_space(3))) bf16x8*)(A + (t * KS + s) * 1024);
        if constexpr (s + 4 < 9) ldo(s + 4);
        if constexpr (s == 5) fn_ldgx(gv, xv, gp, go, xp, xo2);
        waitx<2 * ((s + 4 < 9 ? s + 4 : 8) - s) + (s >= 5 ? 6 : 0)>(b[0][s], b[1][s]);
#pragma unroll
        for (int t = 0; t < 3; ++t) {
            acc[0][t] = MFMA32K(a[t], b[0][s], acc[0][t]);
            acc[1][t] = MFMA32K(a[t], b[1][s], acc[1][t]);
        }
        SBAR();
    });
    asm volatile("s_waitcnt vmcnt(0)" : "+v"(gv[0]), "+v"(gv[1]), "+v"(gv[2]), "+v"(xv[0]), "+v"(xv[1]), "+v"(xv[2]));
    MARK(3);
    typedef __attribute__((address_space(3))) f32x4* l128;
    const lds_ptr xo = (lds_ptr)lds + FN_A + lo;
#pragma unroll
    for (int t = 0; t < 3; ++t) *(l128)(xo + (w * 3 + t) * 1024) = kh ? acc[0][t] : acc[1][t];
    __syncthreads();
#pragma unroll
    for (int t = 0; t < 3; ++t) {
        const f32x4 other = *(l128)(xo + ((w ^ 4) * 3 + t) * 1024);
        const f32x4 sum = kh ? other + acc[1][t] : acc[0][t] + other;
        float o[4];
#pragma unroll
        for (int c = 0; c < 4; ++c) {
            const float g = (c & 1) ? bf_hi(gv[t][c >> 1]) : bf_lo(gv[t][c >> 1]);
            const float xx = (c & 1) ? bf_hi(xv[t][c >> 1]) : bf_lo(xv[t][c >> 1]);
            o[c] = xx + rbf(rbf(sum[c]) * g);
        }
        *(u32x2*)(out + (size_t)tok * D + dg * 48 + 16 * t + 4 * (lane >> 4)) = (u32x2){cvtpk(o[0], o[1]), cvtpk(o[2], o[3])};
    }
    MARK(4);
    MARK(5); MARK_DUMP(4, 6);
    STAMP_END(4);
}

extern "C" void kernel_launch(void* const* d_in, const int* in_sizes, int n_in, void* d_out, int out_size, void* d_ws, size_t ws_size, hipStream_t stream) {
    const unsigned short* x = (const unsigned short*)d_in[0];
    const unsigned short* wqkv = (const unsigned short*)d_in[1];
    const char* wo = (const char*)d_in[2];
    const char* wg = (const char*)d_in[4];
    const char* wu = (const char*)d_in[5];
    const char* wd = (const char*)d_in[6];
    char* ws = (char*)d_ws;
    (void)hipFuncSetAttribute((const void*)k_gemm1, hipFuncAttributeMaxDynamicSharedMemorySize, 4 * G1_SLOT);
    (void)hipFuncSetAttribute((const void*)k_attn, hipFuncAttributeMaxDynamicSharedMemorySize, 3 * AT_STAGE);
    (void)hipFuncSetAttribute((const void*)k_down, hipFuncAttributeMaxDynamicSharedMemorySize, DN_LDS);
    k_prep<<<256, 1024, 0, stream>>>(x, wqkv, (const unsigned short*)wo, ws);
    k_gemm1<<<256, 512, 4 * G1_SLOT, stream>>>(wg, wu, wd, ws);
    (void)hipFuncSetAttribute((const void*)k_mix, hipFuncAttributeMaxDynamicSharedMemorySize, DN_LDS);
    k_mix<<<512, 256, DN_LDS, stream>>>(wd, ws);
    (void)hipFuncSetAttribute((const void*)k_final, hipFuncAttributeMaxDynamicSharedMemorySize, FN_LDS);
    k_final<<<192, 512, FN_LDS, stream>>>(x, ws, (unsigned short*)d_out);
}
```

```cpp
#include <hip/hip_runtime.h>
#include <hip/hip_bf16.h>
#include <stdint.h>
#include <type_traits>

typedef __attribute__((ext_vector_type(8))) short bf16x8;
typedef __attribute__((ext_vector_type(4))) short bf16x4;
typedef __attribute__((ext_vector_type(2))) __bf16 bf16p;
typedef __attribute__((ext_vector_type(2))) float f32x2;
typedef __attribute__((ext_vector_type(4))) float f32x4;
typedef __attribute__((ext_vector_type(16))) float f32x16;
typedef __attribute__((ext_vector_type(2))) unsigned u32x2;
typedef __attribute__((ext_vector_type(4))) unsigned u32x4;
typedef __attribute__((address_space(3))) const char* lds_cptr;
typedef __attribute__((address_space(3))) char* lds_ptr;
typedef short v4i16_t __attribute__((ext_vector_type(4)));

#define DI __device__ __forceinline__
#define MFMA16(a, b, c) __builtin_amdgcn_mfma_f32_32x32x16_bf16((a), (b), (c), 0, 0, 0)
#define MFMA32K(a, b, c) __builtin_amdgcn_mfma_f32_16x16x32_bf16((a), (b), (c), 0, 0, 0)
#define AMF_ACC(c, a, b) asm volatile("v_mfma_f32_16x16x32_bf16 %0, %1, %2, %0" : "+v"(c) : "v"(a), "v"(b))
#define AMF_NEW(c, a, b) asm volatile("v_mfma_f32_16x16x32_bf16 %0, %1, %2, 0" : "=&v"(c) : "v"(a), "v"(b))
#define MFMA8(a, b, c) __builtin_amdgcn_mfma_f32_32x32x8bf16_1k((a), (b), (c), 0, 0, 0)
#define SBAR() __builtin_amdgcn_sched_barrier(0)

template <int I, int N, class Fn> DI void static_for(Fn&& fn) {
    if constexpr (I < N) { fn(std::integral_constant<int, I>{}); static_for<I + 1, N>(fn); }
}

constexpr int S = 2048, D = 576, NH = 32, DH = 18, F = 4608;
constexpr int NTB = S / 32;
constexpr int NT16 = S / 16;
constexpr int KS = D / 32;
constexpr int FS = F / 32;

constexpr int KVREC = 2560;
constexpr size_t WS_XNF = 0;
constexpr size_t WS_WQP = WS_XNF + (size_t)NT16 * KS * 1024;
constexpr int QREC = 1280;
constexpr size_t WS_WOP = WS_WQP + (size_t)NH * KS * 4096;
constexpr size_t WS_Q = WS_WOP + (size_t)36 * KS * 1024;
constexpr size_t WS_KV = WS_Q + (size_t)NH * NTB * QREC;
constexpr size_t WS_OF = WS_KV + (size_t)NH * NTB * KVREC;
constexpr size_t WS_HF = WS_OF + (size_t)NT16 * KS * 1024;
constexpr size_t WS_PART = WS_HF + (size_t)NT16 * FS * 1024;
constexpr size_t WS_END = WS_PART + (size_t)4 * NT16 * 36 * 1024;

#define STAMP_BEGIN
#define STAMP_END(k)
#define MARKS
#define MARK(i)
#define MARK_DUMP(k, n)

constexpr float LOG2E = 1.4426950408889634f;
constexpr float QSCALE = 0.23570226039551584f * LOG2E;

DI unsigned cvtpk(float lo, float hi) { return __builtin_bit_cast(unsigned, __builtin_convertvector((f32x2){lo, hi}, bf16p)); }
DI float bf_lo(unsigned u) { return __uint_as_float(u << 16); }
DI float bf_hi(unsigned u) { return __uint_as_float(u & 0xffff0000u); }
DI float rbf(float x) { return bf_lo(cvtpk(x, x)); }
DI bf16x4 trread(lds_cptr p) { return __builtin_bit_cast(bf16x4, __builtin_amdgcn_ds_read_tr16_b64_v4i16((__attribute__((address_space(3))) v4i16_t*)p)); }
DI bf16x8 trfrag(lds_cptr p0, lds_cptr p1) { const bf16x4 a = trread(p0), b = trread(p1); return (bf16x8){a[0], a[1], a[2], a[3], b[0], b[1], b[2], b[3]}; }
DI bf16x8 join(bf16x4 a, bf16x4 b) { return (bf16x8){a[0], a[1], a[2], a[3], b[0], b[1], b[2], b[3]}; }
DI float swap16(float x) {
    auto r = __builtin_amdgcn_permlane16_swap(__float_as_uint(x), __float_as_uint(x), false, false);
    return __uint_as_float((threadIdx.x & 16) ? r[0] : r[1]);
}
template <int CTRL> DI float dppf(float x) { return __uint_as_float(__builtin_amdgcn_update_dpp(0u, __float_as_uint(x), CTRL, 0xf, 0xf, true)); }
DI float rowmax16(float x) {
    x = fmaxf(x, dppf<0xB1>(x));
    x = fmaxf(x, dppf<0x4E>(x));
    x = fmaxf(x, dppf<0x141>(x));
    return fmaxf(x, dppf<0x140>(x));
}
DI float swap32(float x) { auto r = __builtin_amdgcn_permlane32_swap(__float_as_uint(x), __float_as_uint(x), false, false); return __uint_as_float((threadIdx.x & 32) ? r[0] : r[1]); }

DI void glds16(const void* sbase, unsigned voff, unsigned lds_base) {
    asm volatile("s_mov_b32 m0, %2\n\ts_nop 0\n\tglobal_load_lds_dwordx4 %0, %1" :: "v"(voff), "s"(sbase), "s"(lds_base) : "memory");
}
#define STPOL "sc1"
DI void gstore16(void* sbase, unsigned voff, u32x4 v) { asm volatile("global_store_dwordx4 %0, %1, %2 " STPOL "\n\ts_nop 1" :: "v"(voff), "v"(v), "s"(sbase) : "memory"); }
DI void gstore8(void* sbase, unsigned voff, u32x2 v) { asm volatile("global_store_dwordx2 %0, %1, %2" :: "v"(voff), "v"(v), "s"(sbase) : "memory"); }
DI void gstore16p(void* p, u32x4 v) { asm volatile("global_store_dwordx4 %0, %1, off " STPOL "\n\ts_nop 1" :: "v"(p), "v"(v) : "memory"); }
#define WAITV_BAR(n) asm volatile("s_waitcnt vmcnt(" #n ") lgkmcnt(0)\n\ts_barrier" ::: "memory")

DI int wo_row(int ks, int kg, int j) {
    return ks < 16 ? DH * (2 * ks + (kg >> 1)) + 8 * (j >> 2) + 4 * (kg & 1) + (j & 3) : DH * (16 * (ks - 16) + 4 * kg + (j >> 1)) + 16 + (j & 1);
}
DI float sum32(float x) {
    x += dppf<0xB1>(x);
    x += dppf<0x4E>(x);
    x += dppf<0x141>(x);
    x += dppf<0x140>(x);
    return x + swap16(x);
}

__global__ __launch_bounds__(1024) void k_prep(const unsigned short* __restrict__ x, const unsigned short* __restrict__ wqkv, const unsigned short* __restrict__ wo, char* __restrict__ ws) {
    STAMP_BEGIN;
    __shared__ __attribute__((aligned(16))) char stage[KS * 512];
    const int tid = threadIdx.x;
    const int wv = __builtin_amdgcn_readfirstlane(tid >> 6);
    if (wv < 4) {
        const int row = blockIdx.x * 8 + (tid >> 5), t = tid & 31;
        const u32x4* xr = (const u32x4*)(x + (size_t)row * D);
        u32x4 v[3];
        v[0] = xr[t]; v[1] = xr[t + 32]; v[2] = (t < 8) ? xr[t + 64] : (u32x4){0u, 0u, 0u, 0u};
        float sum = 0.f;
#pragma unroll
        for (int i = 0; i < 3; ++i)
#pragma unroll
            for (int j = 0; j < 4; ++j) sum += bf_lo(v[i][j]) + bf_hi(v[i][j]);
        const float mean = sum32(sum) * (1.0f / D);
        float sq = 0.f;
#pragma unroll
        for (int i = 0; i < 3; ++i)
            if (i < 2 || t < 8)
#pragma unroll
                for (int j = 0; j < 4; ++j) { const float a = bf_lo(v[i][j]) - mean, b = bf_hi(v[i][j]) - mean; sq += a * a + b * b; }
        const float mb = rbf(mean);
        const float den = rbf(__fsqrt_rn(rbf(rbf(sum32(sq) * (1.0f / D)) + 1e-5f)));
        const float rc = __builtin_amdgcn_rcpf(den);
        auto quot = [&](float a) { const float q = a * rc; return __builtin_fmaf(__builtin_fmaf(-q, den, a), rc, q); };
#pragma unroll
        for (int i = 0; i < 3; ++i) {
            if (i < 2 || t < 8) {
                const int p = t + 32 * i;
                unsigned o[4];
#pragma unroll
                for (int j = 0; j < 4; ++j) o[j] = cvtpk(quot(rbf(bf_lo(v[i][j]) - mb)), quot(rbf(bf_hi(v[i][j]) - mb)));
                char* d = stage + (p >> 2) * 512 + (p & 1) * 256 + (tid >> 5) * 16 + ((p >> 1) & 1) * 8;
                *(u32x2*)d = (u32x2){o[0], o[1]};
                *(u32x2*)(d + 128) = (u32x2){o[2], o[3]};
            }
        }
        asm volatile("s_waitcnt lgkmcnt(0)\n\ts_barrier" ::: "memory");
        char* dst = ws + WS_XNF + (size_t)(blockIdx.x >> 1) * KS * 1024 + (blockIdx.x & 1) * 128;
#pragma unroll
        for (int i = 0; i < 3; ++i) {
            const int n = tid + 256 * i;
            if (n < KS * 32) gstore16p(dst + (n >> 3) * 256 + (n & 7) * 16, *(const u32x4*)(stage + n * 16));
        }
    } else if (wv < 13) {
        const int n = blockIdx.x * 576 + (tid - 256);
        const int pc = n & 3, r = (n >> 2) & 31, sel = (n >> 7) & 1, hs = n >> 8, s = hs % KS, h = hs / KS;
        const int c = pc ^ (2 * ((r >> 2) & 1)), t = c >> 1, g = c & 1;
        const unsigned* w = (const unsigned*)(wqkv + (size_t)(32 * s + r) * (3 * D) + DH * h);
        u32x4 o = {0u, 0u, 0u, 0u};
        if (sel == 0 || t == 0) {
            const int col = (sel == 0 ? t * D : 2 * D) + 8 * g;
            o = (u32x4){w[col / 2], w[col / 2 + 1], w[col / 2 + 2], w[col / 2 + 3]};
        } else if (g == 0) {
            o = (u32x4){w[8], w[D / 2 + 8], w[D + 8], 0u};
        }
        gstore16p(ws + WS_WQP + (size_t)n * 16, o);
    } else if (tid < 256 + 576 + 162) {
        const int n = blockIdx.x * 162 + (tid - (256 + 576));
        const int lane = n & 63, fs = n >> 6, ks = fs % KS, dt = fs / KS;
        unsigned short v[8];
#pragma unroll
        for (int j = 0; j < 8; ++j) v[j] = wo[(size_t)wo_row(ks, lane >> 4, j) * D + 16 * dt + (lane & 15)];
        gstore16p(ws + WS_WOP + (size_t)n * 16, (u32x4){v[0] | ((unsigned)v[1] << 16), v[2] | ((unsigned)v[3] << 16), v[4] | ((unsigned)v[5] << 16), v[6] | ((unsigned)v[7] << 16)});
    }
    STAMP_END(0);
}

#define G1_MAP 1
constexpr int G1_SLOT = 9 * 4096;

DI int img_aoff(int lane) {
    const int kg = lane >> 4, q = (lane & 15) >> 2, p = lane & 3;
    return (4 * kg + q) * 64 + (((p >> 1) ^ (2 * (kg & 1))) * 16) + 8 * (p & 1);
}
DI void g1_ldx(bf16x8& d, const char* base, unsigned lo, int ks) {
    asm volatile("global_load_dwordx4 %0, %1, %2 offset:%3" : "=v"(d) : "v"(lo), "s"(base + (ks >> 2) * 4096), "n"((ks & 3) * 1024));
}
DI float glu(float g, float u) { return u * __builtin_amdgcn_rcpf(1.0f + __builtin_amdgcn_exp2f(g * -LOG2E)); }

constexpr int g1_group(int u) { return u < 4 ? (u & 1) : u / 2; }
constexpr int g1_half(int u) { return u < 4 ? (u >> 1) : (u & 1); }
constexpr int g1_dma_at(int u, int g) {
    if (u == 0) return (g >= 12 && g <= 20 && !(g & 1)) ? 1 * 8 + (g - 12) / 2 : (g >= 22 && g <= 30 && !(g & 1)) ? 2 * 8 + (g - 22) / 2 : -1;
    if (u == 1) return -1;
    if (u == 2) return (g <= 16 && !(g & 3)) ? 3 * 8 + g / 4 : (g >= 20 && g <= 28 && !(g & 1)) ? 4 * 8 + (g - 20) / 2 : -1;
    if (u <= 9) { const int sp = 5; return (g % sp == 0 && g / sp < 5) ? (u + 2) * 8 + g / sp : -1; }
    return -1;
}
constexpr int g1_x_at(int u, int g) {
    if (g & 3) return -1;
    if (u == 0) return g / 4 < 3 ? 6 + g / 4 : -1;
    if (u == 1) return 9 + g / 4;
    return -1;
}
constexpr int g1_x_younger(int ks, int u, int g) {
    int n = 0; bool seen = ks < 6;
    if (ks < 6) n += 2 * (5 - ks);
    for (int uu = 0; uu <= u; ++uu)
        for (int gg = 0; gg < 36 && (uu < u || gg <= g); ++gg) {
            const int x = g1_x_at(uu, gg);
            if (x >= 0) { if (seen) n += 2; if (x == ks) seen = true; }
            if (g1_dma_at(uu, gg) >= 0 && seen) n += 1;
        }
    return n;
}

struct G1Ctx {
    const char *wrow, *wq, *xs;
    char* hdst;
    unsigned lds0, dsto, voff, lo;
    int fbase, fsingle, w;
};
DI void g1_piece(const G1Ctx& c, int unit, int m) {
    const int grp = g1_group(unit), hk = g1_half(unit);
    const unsigned slot = c.lds0 + (unit & 3) * G1_SLOT;
    const int mm = (m == 4 && c.w >= 4) ? 3 : m;
    if (grp == 0) {
        const int pi = c.w + 8 * mm;
        glds16(c.wq + hk * G1_SLOT + pi * 1024, c.lo, slot + pi * 1024);
    } else {
        const int f0 = grp == 5 ? c.fsingle : c.fbase + 32 * (grp - 1);
        glds16(c.wrow + (size_t)(288 * hk + 64 * mm) * (F * 2) + f0 * 2, c.voff, slot + c.dsto + mm * 8192);
    }
}

struct G1Epi { float e[2], h[2]; u32x4 pk; };
DI void g1_epi_slice(int s, const f32x4 (&c)[2][2][2], G1Epi& t, char* hdst, unsigned lo) {
    const int v = s >> 2, nh = v >> 3, j = v & 7, ph = s & 3;
    if (ph == 0) { t.e[v & 1] = __builtin_amdgcn_exp2f(c[0][j >> 2][nh][j & 3] * -LOG2E); asm volatile("" : "+v"(t.e[v & 1])); }
    if (ph == 1) { t.e[v & 1] = __builtin_amdgcn_rcpf(1.0f + t.e[v & 1]); asm volatile("" : "+v"(t.e[v & 1])); }
    if (ph == 2) { t.h[v & 1] = c[1][j >> 2][nh][j & 3] * t.e[v & 1]; asm volatile("" : "+v"(t.h[v & 1])); }
    if (ph == 3 && (v & 1)) {
        t.pk[j >> 1] = cvtpk(t.h[0], t.h[1]);
        asm volatile("" : "+v"(t.pk[j >> 1]));
        if (j == 7) gstore16(hdst + (size_t)nh * FS * 1024, lo, t.pk);
    }
}

struct QkvEpi { char *qg, *kg; lds_ptr stg; float kmax; };
DI void qkv_slice(int k, const f32x4 (&c)[2][2][2], QkvEpi& e, unsigned lo) {
    typedef __attribute__((address_space(3))) u32x2* l64;
    typedef __attribute__((address_space(3))) unsigned* l32;
    typedef __attribute__((address_space(3))) unsigned short* l16;
    typedef __attribute__((address_space(3))) const u32x4* l128;
    asm volatile("" : "+v"(lo));
    const int lane = lo >> 4;
    const int t = lane & 15, g4 = lane >> 4;
    const lds_ptr rec = e.stg + QREC;
    if (k < 4) {
        const int nh = k >> 1, r = 16 * nh + t;
        const f32x4 ex = c[1][1][nh];
        if ((k & 1) == 0) {
            const unsigned fo = (r + 32 * (g4 >> 1)) * 16 + (g4 & 1) * 8;
            const f32x4 q = c[0][0][nh], kk = c[0][1][nh];
            *(l64)(e.stg + fo) = (u32x2){cvtpk(q[0] * QSCALE, q[1] * QSCALE), cvtpk(q[2] * QSCALE, q[3] * QSCALE)};
            const u32x2 kp = {cvtpk(kk[0], kk[1]), cvtpk(kk[2], kk[3])};
            *(l64)(rec + fo) = kp;
            const unsigned e23 = cvtpk(ex[2], ex[3]);
            float n2 = g4 == 0 ? bf_lo(e23) * bf_lo(e23) + bf_hi(e23) * bf_hi(e23) : 0.f;
            n2 += bf_lo(kp[0]) * bf_lo(kp[0]) + bf_hi(kp[0]) * bf_hi(kp[0]) + bf_lo(kp[1]) * bf_lo(kp[1]) + bf_hi(kp[1]) * bf_hi(kp[1]);
            n2 += swap16(n2);
            n2 += swap32(n2);
            e.kmax = nh ? fmaxf(e.kmax, n2) : n2;
            asm volatile("" : "+v"(e.kmax));
        } else {
            const f32x4 v = c[1][0][nh];
            *(l64)(rec + 1280 + nh * 640 + (g4 & 1) * 320 + t * 16 + (g4 >> 1) * 8) = (u32x2){cvtpk(v[0], v[1]), cvtpk(v[2], v[3])};
            const unsigned e01 = cvtpk(ex[0], ex[1]), e23 = cvtpk(ex[2], ex[3]);
            const lds_ptr vt = rec + 1280 + nh * 640 + ((t >> 2) & 1) * 320 + (4 * (t >> 3) + (t & 3)) * 2;
            if (g4 == 0) {
                *(l64)(rec + 1024 + r * 8) = (u32x2){e23, 0u};
                *(l32)(e.stg + 1024 + r * 4) = cvtpk(ex[0] * QSCALE, ex[1] * QSCALE);
            } else {
                *(l16)(vt + (g4 == 1 ? 16 : 16 + g4) * 16) = (unsigned short)(g4 == 1 ? e01 : (g4 == 2 ? 0x3f80u : 0u));
                if (g4 == 1) *(l16)(vt + 17 * 16) = (unsigned short)(e01 >> 16);
            }
        }
    } else if (k == 4) {
        const float m = rowmax16(e.kmax);
        if (lane == 0) *(l32)(e.stg + 1152) = __float_as_uint(m);
    } else if (k == 5) {
        const unsigned o0 = lane * 16, oq = min(lane, 8) * 16 + 1024;
        const u32x4 d0 = *(l128)(e.stg + o0), d1 = *(l128)(e.stg + oq);
        gstore16(e.qg, o0, d0);
        gstore16(e.qg, oq, d1);
    } else {
        const unsigned o0 = lane * 16, ok = min(lane, 31) * 16 + 2048;
        const u32x4 d2 = *(l128)(rec + o0), d3 = *(l128)(rec + 1024 + o0), d4 = *(l128)(rec + ok);
        gstore16(e.kg, o0, d2);
        gstore16(e.kg, o0 + 1024, d3);
        gstore16(e.kg, ok, d4);
    }
}

struct G1Ring { bf16x4 lo[3], hi[3]; };
constexpr int g1_meet(int u) { return u == 1 ? 18 : u == 3 ? 10 : u == 10 ? 0 : 5; }
template <int U>
DI void g1_unit(const G1Ctx& cx, lds_cptr& b0, lds_cptr& b1, bool odd, G1Ring& fr, bf16x8 (&xb)[2][KS], f32x4 (&c)[2][2][2],
                const f32x4 (&cprev)[2][2][2], QkvEpi& qe) {
    constexpr int GRP = g1_group(U), HK = g1_half(U);
    constexpr bool PAIR = GRP != 5, NPAIR = g1_group(U + 1) != 5;
    constexpr int PER = PAIR ? 4 : 2, N = 9 * PER;
    constexpr bool EPI = U >= 4;
    auto addr = [&](int i) {
        const bool nx = i >= N, pr = nx ? NPAIR : PAIR;
        const int j = nx ? i - N : i, ks = pr ? j >> 2 : j >> 1, sel = pr ? (j >> 1) & 1 : j & 1, t = pr ? j & 1 : 0;
        return (t ? b1 : b0) + ks * 4096 + sel * 2048;
    };
    G1Epi ep;
    char* hprev = cx.hdst + (U / 2 - 2) * 1024;
    static_for<0, N>([&](auto ic) {
        constexpr int i = decltype(ic)::value;
        constexpr int kl = i / PER, ks = 9 * HK + kl, sel = PAIR ? (i >> 1) & 1 : i & 1, t = PAIR ? i & 1 : 0;
        constexpr int g = PAIR ? i : 2 * i;
        constexpr int x = g1_x_at(U, g), dm = g1_dma_at(U, g);
        constexpr bool more = U < 11 || i + 2 < N;
        if constexpr (i == N - 2 && U < 11) {
            asm volatile("s_waitcnt vmcnt(%0) lgkmcnt(0)\n\ts_barrier" :: "n"(g1_meet(U)) : "memory");
            constexpr int delta = (((U + 1) & 3) - (U & 3)) * G1_SLOT;
            if constexpr (U == 9) b0 = odd ? b1 : b0;
            b0 += delta; b1 += delta;
            asm volatile("" : "+v"(b0), "+v"(b1));
        }
        if constexpr (x >= 0) { g1_ldx(xb[0][x], cx.xs, cx.lo, x); g1_ldx(xb[1][x], cx.xs + KS * 1024, cx.lo, x); }
        if constexpr (dm >= 0) g1_piece(cx, dm >> 3, dm & 7);
        if constexpr ((U == 0 || U == 2) && (i & 3) == 0) {
            constexpr int young = g1_x_younger(ks, U, g);
            asm volatile("s_waitcnt vmcnt(%2)" : "+v"(xb[0][ks]), "+v"(xb[1][ks]) : "n"(young));
        }
        constexpr bool SWAP = GRP == 0 && sel == 1 && t == 0;
        const bf16x8 a = join(fr.lo[i % 3], fr.hi[i % 3]);
        if constexpr (ks == 0) { if constexpr (SWAP) AMF_NEW(c[sel][t][0], xb[0][ks], a); else AMF_NEW(c[sel][t][0], a, xb[0][ks]); }
        else { if constexpr (SWAP) AMF_ACC(c[sel][t][0], xb[0][ks], a); else AMF_ACC(c[sel][t][0], a, xb[0][ks]); }
        SBAR();
        if constexpr (more) fr.lo[(i + 2) % 3] = trread(addr(i + 2));
        SBAR();
        if constexpr (ks == 0) { if constexpr (SWAP) AMF_NEW(c[sel][t][1], xb[1][ks], a); else AMF_NEW(c[sel][t][1], a, xb[1][ks]); }
        else { if constexpr (SWAP) AMF_ACC(c[sel][t][1], xb[1][ks], a); else AMF_ACC(c[sel][t][1], a, xb[1][ks]); }
        SBAR();
        if constexpr (more) fr.hi[(i + 2) % 3] = trread(addr(i + 2) + 1024);
        SBAR();
        if constexpr (U == 3 && (i & 3) == 2 && i / 4 < 7) qkv_slice(i / 4, cprev, qe, cx.lo);
        if constexpr (EPI && PAIR) {
            constexpr int sl = 36 * HK + i - 4;
            if constexpr (sl >= 0 && sl < 64) g1_epi_slice(sl, cprev, ep, hprev, cx.lo);
        } else if constexpr (EPI) {
            constexpr int sl = 2 * (18 * HK + i - 2);
            if constexpr (sl >= 0 && sl < 64) { g1_epi_slice(sl, cprev, ep, hprev, cx.lo); g1_epi_slice(sl + 1, cprev, ep, hprev, cx.lo); }
        }
        SBAR();
    });
}

__global__ __launch_bounds__(512, 2) void k_gemm1(const char* wg, const char* wu, char* ws) {
    extern __shared__ __attribute__((aligned(16))) char lds[];
    STAMP_BEGIN;
    const int tid = threadIdx.x, lane = tid & 63;
    const int w = __builtin_amdgcn_readfirstlane(tid >> 6);
    const int xcd = blockIdx.x & 7, idx = blockIdx.x >> 3;
    const int cg = 8 * (xcd >> 1) + (idx & 7), rg = 4 * (xcd & 1) + (idx >> 3), tb = 8 * rg + w;
    MARKS; MARK(0);

    const int odd = cg & 1;
    G1Ctx cx;
    cx.w = w; cx.lds0 = (unsigned)(uintptr_t)lds; cx.lo = lane * 16;
    cx.fbase = 144 * cg + 16 * odd; cx.fsingle = odd ? 144 * cg - 16 : 144 * cg + 128;
    cx.wq = ws + WS_WQP + (size_t)cg * KS * 4096;
    cx.xs = ws + WS_XNF + (size_t)(2 * tb) * KS * 1024;
    cx.wrow = ((w & 1) ? wu : wg) + (size_t)(32 * (w >> 2) + 16 * ((w >> 1) & 1)) * (F * 2);
    cx.dsto = (w >> 2) * 4096 + (w & 1) * 2048 + ((w >> 1) & 1) * 1024;
    cx.voff = (lane >> 2) * (F * 2) + (((lane & 3) ^ (2 * ((lane >> 4) & 1))) * 16);
    cx.hdst = ws + WS_HF + ((size_t)(2 * tb) * FS + cx.fbase / 32) * 1024;

#pragma unroll
    for (int m = 0; m < 5; ++m) g1_piece(cx, 0, m);
    bf16x8 xb[2][KS];
#pragma unroll
    for (int s = 0; s < 6; ++s) { g1_ldx(xb[0][s], cx.xs, cx.lo, s); g1_ldx(xb[1][s], cx.xs + KS * 1024, cx.lo, s); }

    const int aoff0 = img_aoff(lane);
    lds_cptr b0 = (lds_cptr)lds + aoff0, b1 = (lds_cptr)lds + (aoff0 ^ 32);
    asm volatile("" : "+v"(b0), "+v"(b1));
    f32x4 ca[2][2][2], cb[2][2][2];
    QkvEpi qe;
    qe.qg = ws + WS_Q + ((size_t)cg * NTB + tb) * QREC;
    qe.kg = ws + WS_KV + ((size_t)cg * NTB + tb) * KVREC;
    qe.stg = (lds_ptr)lds + 2 * G1_SLOT + w * 4608;

    WAITV_BAR(12);
    MARK(1);
    G1Ring fr;
#pragma unroll
    for (int i = 0; i < 2; ++i) {
        const lds_cptr a = (i & 1) ? b1 : b0;
        fr.lo[i] = trread(a); fr.hi[i] = trread(a + 1024);
    }
    g1_unit<0>(cx, b0, b1, odd, fr, xb, ca, cb, qe);
    g1_unit<1>(cx, b0, b1, odd, fr, xb, cb, ca, qe);
    MARK(2);
    g1_unit<2>(cx, b0, b1, odd, fr, xb, ca, cb, qe);
    MARK(3);
    g1_unit<3>(cx, b0, b1, odd, fr, xb, cb, ca, qe);
    MARK(4);
    g1_unit<4>(cx, b0, b1, odd, fr, xb, ca, cb, qe);
    g1_unit<5>(cx, b0, b1, odd, fr, xb, ca, cb, qe);
    MARK(5);
    g1_unit<6>(cx, b0, b1, odd, fr, xb, cb, ca, qe);
    g1_unit<7>(cx, b0, b1, odd, fr, xb, cb, ca, qe);
    MARK(6);
    g1_unit<8>(cx, b0, b1, odd, fr, xb, ca, cb, qe);
    g1_unit<9>(cx, b0, b1, odd, fr, xb, ca, cb, qe);
    MARK(7);
    g1_unit<10>(cx, b0, b1, odd, fr, xb, cb, ca, qe);
    g1_unit<11>(cx, b0, b1, odd, fr, xb, cb, ca, qe);
    asm volatile("s_nop 7\n\ts_nop 3" : "+v"(cb[0][0][0]), "+v"(cb[0][0][1]), "+v"(cb[1][0][0]), "+v"(cb[1][0][1]));
    {
        char* hs = ws + WS_HF + ((size_t)(2 * tb) * FS + cx.fsingle / 32) * 1024 + odd * 8;
#pragma unroll
        for (int nh = 0; nh < 2; ++nh) {
            float h[4];
#pragma unroll
            for (int j = 0; j < 4; ++j) h[j] = glu(cb[0][0][nh][j], cb[1][0][nh][j]);
            gstore8(hs + (size_t)nh * FS * 1024, cx.lo, (u32x2){cvtpk(h[0], h[1]), cvtpk(h[2], h[3])});
        }
    }
    MARK(8); MARK_DUMP(1, 9);
    STAMP_END(1);
}

constexpr int AT_STAGE = 8 * KVREC;
constexpr float AT_BOUND_MAX = 50.0f;

struct KFrag { bf16x8 k1; bf16x4 k2; };
struct VFrag { bf16x8 v0, v1; };
DI KFrag at_readk(lds_cptr rec, int lane) {
    KFrag k;
    k.k1 = *(const __attribute__((address_space(3))) bf16x8*)(rec + lane * 16);
    k.k2 = *(const __attribute__((address_space(3))) bf16x4*)(rec + 1024 + (lane & 31) * 8);
    return k;
}
DI VFrag at_readv(lds_cptr recv) {
    VFrag v;
    v.v0 = *(const __attribute__((address_space(3))) bf16x8*)(recv);
    v.v1 = *(const __attribute__((address_space(3))) bf16x8*)(recv + 640);
    return v;
}
DI bf16x8 packp(const f32x16& p, int s) {
    return __builtin_bit_cast(bf16x8, (u32x4){cvtpk(p[8 * s], p[8 * s + 1]), cvtpk(p[8 * s + 2], p[8 * s + 3]), cvtpk(p[8 * s + 4], p[8 * s + 5]), cvtpk(p[8 * s + 6], p[8 * s + 7])});
}

DI void at_dma(const char* kvh, int stage, unsigned ldsb, int w, int lane) {
    const char* src = kvh + (size_t)stage * AT_STAGE + w * 5 * 1024;
    const unsigned dst = ldsb + w * 5 * 1024, lo = lane * 16;
    asm volatile("s_mov_b32 m0, %2\n\ts_nop 0\n\t"
                 "global_load_lds_dwordx4 %0, %1\n\tglobal_load_lds_dwordx4 %0, %1 offset:1024\n\t"
                 "global_load_lds_dwordx4 %0, %1 offset:2048\n\tglobal_load_lds_dwordx4 %0, %1 offset:3072"
                 :: "v"(lo), "s"(src), "s"(dst) : "memory");
    glds16(src + 4096, lo, dst + 4096);
}

#define AMFMA16_NEW(d, a, b, c) asm volatile("v_mfma_f32_32x32x16_bf16 %0, %1, %2, %3" : "=&v"(d) : "v"(a), "v"(b), "v"(c))
#define AMFMA16_ACC(d, a, b) asm volatile("v_mfma_f32_32x32x16_bf16 %0, %1, %2, %0" : "+v"(d) : "v"(a), "v"(b))
#define AMFMA8_ACC(d, a, b) asm volatile("v_mfma_f32_32x32x8_bf16 %0, %1, %2, %0" : "+v"(d) : "v"(a), "v"(b))
#define AT_EXP4(E, SX, a) do { _Pragma("unroll") for (int i_ = 0; i_ < 4; ++i_) asm volatile("v_exp_f32 %0, %1" : "=v"(E[i_]) : "v"(SX[a + i_])); } while (0)
#define AT_PK2(DST, k, E) do { asm volatile("v_cvt_pk_bf16_f32 %0, %1, %2" : "=v"(DST[k]) : "v"(E[0]), "v"(E[1]));   \
                               asm volatile("v_cvt_pk_bf16_f32 %0, %1, %2" : "=v"(DST[k + 1]) : "v"(E[2]), "v"(E[3])); } while (0)
#define AT_STEP(SX, PX0, PX1, SY, CY, QY1, QY2, KF, OY, PY0, PY1, VF, LOADS)                                   \
    do {                                                                                                       \
        float e0_[4], e1_[4], e2_[4];                                                                          \
        AMFMA16_NEW(SY, KF.k1, QY1, CY);                                                                       \
        LOADS;                                                                                                 \
        SBAR();                                                                                                \
        AT_EXP4(e0_, SX, 0);                                                                                   \
        AT_PK2(PY1, 2, etail);                                                                                 \
        AMFMA8_ACC(SY, KF.k2, QY2);                                                                            \
        AT_EXP4(e1_, SX, 4);                                                                                   \
        AT_PK2(PX0, 0, e0_);                                                                                   \
        AMFMA16_ACC(OY, VF.v0, PY0);                                                                           \
        AT_EXP4(e2_, SX, 8);                                                                                   \
        AT_PK2(PX0, 2, e1_);                                                                                   \
        AMFMA16_ACC(OY, VF.v1, PY1);                                                                           \
        AT_EXP4(etail, SX, 12);                                                                                \
        AT_PK2(PX1, 0, e2_);                                                                                   \
        SBAR();                                                                                                \
    } while (0)

DI void at_online(const KFrag& k, const VFrag& v, bf16x8 q1, bf16x4 q2, float& m, f32x16& o) {
    f32x16 z = {};
    f32x16 s = MFMA16(k.k1, q1, z);
    s = MFMA8(k.k2, q2, s);
    float t = s[0];
#pragma unroll
    for (int i = 1; i < 16; ++i) t = fmaxf(t, s[i]);
    t = fmaxf(t, swap32(t));
    const float mn = fmaxf(m, t);
    const float alpha = __builtin_amdgcn_exp2f(m - mn);
    m = mn;
#pragma unroll
    for (int i = 0; i < 16; ++i) { s[i] = __builtin_amdgcn_exp2f(s[i] - mn); o[i] *= alpha; }
    o = MFMA16(v.v0, packp(s, 0), o);
    o = MFMA16(v.v1, packp(s, 1), o);
}

DI void at_store(const f32x16& o, char* ws, int h, int tb, int lane) {
    const int r = lane & 31, hi = lane >> 5;
    const float other = swap32(o[10]);
    const float inv = 1.0f / (hi ? other : o[10]);
    char* of = ws + WS_OF + (size_t)(2 * tb + (r >> 4)) * KS * 1024;
    gstore16p(of + (h >> 1) * 1024 + ((r & 15) + 16 * (2 * (h & 1) + hi)) * 16,
              (u32x4){cvtpk(o[0] * inv, o[1] * inv), cvtpk(o[2] * inv, o[3] * inv), cvtpk(o[4] * inv, o[5] * inv), cvtpk(o[6] * inv, o[7] * inv)});
    if (hi == 0) *(unsigned*)(of + (16 + (h >> 4)) * 1024 + ((r & 15) + 16 * ((h >> 2) & 3)) * 16 + (h & 3) * 4) = cvtpk(o[8] * inv, o[9] * inv);
}

DI float at_qnorm2(bf16x8 q1, bf16x4 q2) {
    const u32x4 a = __builtin_bit_cast(u32x4, q1); const u32x2 b = __builtin_bit_cast(u32x2, q2);
    float n = bf_lo(b[0]) * bf_lo(b[0]) + bf_hi(b[0]) * bf_hi(b[0]);
#pragma unroll
    for (int j = 0; j < 4; ++j) n += bf_lo(a[j]) * bf_lo(a[j]) + bf_hi(a[j]) * bf_hi(a[j]);
    return n + swap32(n);
}

DI void attn_block(char* ws, char* lds, int bid) {
    const int tid = threadIdx.x, lane = tid & 63, hi = lane >> 5;
    const int w = __builtin_amdgcn_readfirstlane(tid >> 6);
    const int xcd = bid & 7, idx = bid >> 3;
    const int h = 4 * xcd + (idx & 3), qg = idx >> 2;
    const int tbA = 8 * qg + 2 * w, tbB = tbA + 1;
    const unsigned lds0 = (unsigned)(uintptr_t)lds;
    const char* kvh = ws + WS_KV + (size_t)h * NTB * KVREC;
    MARKS; MARK(0);

    bf16x8 qA1, qB1; unsigned qa2, qb2; float km;
    {
        const char* q1 = ws + WS_Q + ((size_t)h * NTB + tbA) * QREC;
        const char* kx = ws + WS_Q + (size_t)h * NTB * QREC + 1152;
        const unsigned l16 = lane * 16, l4 = (lane & 31) * 4 + 1024, lk = lane * QREC;
        asm volatile("global_load_dword %0, %1, %2" : "=v"(km) : "v"(lk), "s"(kx));
        asm volatile("global_load_dwordx4 %0, %1, %2" : "=v"(qA1) : "v"(l16), "s"(q1));
        asm volatile("global_load_dwordx4 %0, %1, %2 offset:%3" : "=v"(qB1) : "v"(l16), "s"(q1), "n"(QREC));
        asm volatile("global_load_dword %0, %1, %2" : "=v"(qa2) : "v"(l4), "s"(q1));
        asm volatile("global_load_dword %0, %1, %2 offset:%3" : "=v"(qb2) : "v"(l4), "s"(q1), "n"(QREC));
    }
    at_dma(kvh, 0, lds0, w, lane);
    at_dma(kvh, 1, lds0 + AT_STAGE, w, lane);
    asm volatile("s_waitcnt vmcnt(10)" : "+v"(km), "+v"(qA1), "+v"(qB1), "+v"(qa2), "+v"(qb2));
    const bf16x4 qA2 = __builtin_bit_cast(bf16x4, (u32x2){hi ? 0u : qa2, 0u});
    const bf16x4 qB2 = __builtin_bit_cast(bf16x4, (u32x2){hi ? 0u : qb2, 0u});
    km = rowmax16(km);
    km = fmaxf(km, swap16(km));
    km = fmaxf(km, swap32(km));
    const float mA = __builtin_amdgcn_sqrtf(at_qnorm2(qA1, qA2) * km) * 1.001f, mB = __builtin_amdgcn_sqrtf(at_qnorm2(qB1, qB2) * km) * 1.001f;
    const bool fast = __all(mA <= AT_BOUND_MAX && mB <= AT_BOUND_MAX);

    const int voffs = 1280 + hi * 320 + min(lane & 31, 19) * 16;
    const lds_cptr L = (lds_cptr)lds;
    f32x16 oA = {}, oB = {};

    if (fast) {
        f32x16 cA, cB;
#pragma unroll
        for (int i = 0; i < 16; ++i) { cA[i] = -mA; cB[i] = -mB; }
        asm volatile("" : "+v"(cA), "+v"(cB));
        f32x16 sA, sB;
        u32x4 pA0 = {}, pA1 = {}, pB0 = {}, pB1 = {};
        float etail[4] = {0.f, 0.f, 0.f, 0.f};
        KFrag k0, k1; VFrag v0, v1;
        MARK(1);
        WAITV_BAR(5);
        MARK(2);
        k0 = at_readk(L, lane);
        v1 = at_readv(L + voffs);
        sA = MFMA16(k0.k1, qA1, cA); sA = MFMA8(k0.k2, qA2, sA);
        asm volatile("s_nop 15" : "+v"(sA));
        int slot = 0;
        for (int st = 0; st < 8; ++st) {
            const lds_cptr cur = L + slot * AT_STAGE;
            const int nslot = slot == 2 ? 0 : slot + 1;
#pragma unroll
            for (int t = 0; t < 8; t += 2) {
                AT_STEP(sA, pA0, pA1, sB, cB, qB1, qB2, k0, oB, pB0, pB1, v1,
                        (k1 = at_readk(cur + (t + 1) * KVREC, lane), v0 = at_readv(cur + t * KVREC + voffs)));
                AT_STEP(sB, pB0, pB1, sA, cA, qA1, qA2, k1, oA, pA0, pA1, v0, (void)0);
                lds_cptr nk = cur + (t + 2) * KVREC;
                if (t == 6) {
                    if (st < 7) {
                        if (st < 6) { WAITV_BAR(0); at_dma(kvh, st + 2, lds0 + (nslot == 2 ? 0 : nslot + 1) * AT_STAGE, w, lane); }
                        else WAITV_BAR(0);
                        nk = L + nslot * AT_STAGE;
                    } else nk = cur;
                }
                AT_STEP(sA, pA0, pA1, sB, cB, qB1, qB2, k1, oB, pB0, pB1, v0,
                        (k0 = at_readk(nk, lane), v1 = at_readv(cur + (t + 1) * KVREC + voffs)));
                AT_STEP(sB, pB0, pB1, sA, cA, qA1, qA2, k0, oA, pA0, pA1, v1, (void)0);
            }
            slot = nslot;
            MARK(3 + st);
        }
        asm volatile("s_nop 1");
        AT_PK2(pB1, 2, etail);
        asm volatile("s_nop 1" : "+v"(pB1));
        AMFMA16_ACC(oB, v1.v0, pB0);
        AMFMA16_ACC(oB, v1.v1, pB1);
        asm volatile("s_nop 15\n\ts_nop 7" : "+v"(oA), "+v"(oB));
    } else {
        float mxA = -INFINITY, mxB = -INFINITY;
        WAITV_BAR(5);
        int slot = 0;
        for (int st = 0; st < 8; ++st) {
            const lds_cptr cur = L + slot * AT_STAGE;
            const int nslot = slot == 2 ? 0 : slot + 1;
            for (int t = 0; t < 8; ++t) {
                const KFrag k = at_readk(cur + t * KVREC, lane);
                const VFrag v = at_readv(cur + t * KVREC + voffs);
                at_online(k, v, qA1, qA2, mxA, oA);
                at_online(k, v, qB1, qB2, mxB, oB);
            }
            if (st < 7) {
                WAITV_BAR(0);
                if (st < 6) at_dma(kvh, st + 2, lds0 + (nslot == 2 ? 0 : nslot + 1) * AT_STAGE, w, lane);
            }
            slot = nslot;
        }
    }
    at_store(oA, ws, h, tbA, lane);
    at_store(oB, ws, h, tbB, lane);
    MARK(11); MARK_DUMP(2, 12);
}

constexpr int DN_STEP = 32 * 288, DN_SLOT = 2 * DN_STEP, DN_LDS = 4 * DN_SLOT, DN_NST = 18;
constexpr int dn_piece(int st, int i) {
    if ((i == 3 || i == 7 || i == 11) && st + 3 < DN_NST) return (st + 3) * 8 + 2 + (i - 3) / 4;
    if ((i == 15 || i == 17) && st + 4 < DN_NST) return (st + 4) * 8 + (i - 15) / 2;
    return -1;
}
constexpr int dn_young(int st) {
    int n = 0; bool seen = st + 1 == 1;
    const int need = st + 1;
    for (int s2 = 0; s2 <= st; ++s2)
        for (int i = 0; i < 18; ++i) {
            if (s2 == st && i >= 15) break;
            if (i == 0 && s2 + 2 < DN_NST) { if (seen) n += 4; if (s2 + 2 == need) { seen = true; n = 0; } }
            const int pc = dn_piece(s2, i);
            if (pc >= 0) { if (seen) n += 1; if ((pc >> 3) == need) { seen = true; n = 0; } }
        }
    return n;
}
DI void dn_ldb(bf16x8 (&b)[2][2], const char* hs, unsigned lo, int st) {
#pragma unroll
    for (int k = 0; k < 2; ++k) {
        asm volatile("global_load_dwordx4 %0, %1, %2 offset:%3" : "=v"(b[0][k]) : "v"(lo), "s"(hs + (size_t)st * 2048), "n"(k * 1024));
        asm volatile("global_load_dwordx4 %0, %1, %2 offset:%3" : "=v"(b[1][k]) : "v"(lo), "s"(hs + (size_t)FS * 1024 + (size_t)st * 2048), "n"(k * 1024));
    }
}
template <int N> DI void dn_wait(bf16x8 (&b)[2][2]) {
    asm volatile("s_waitcnt vmcnt(%4) lgkmcnt(0)\n\ts_barrier" : "+v"(b[0][0]), "+v"(b[0][1]), "+v"(b[1][0]), "+v"(b[1][1]) : "n"(N) : "memory");
}

DI void down_block(const char* wd, char* ws, char* lds, int bid) {
    const int tid = threadIdx.x, lane = tid & 63;
    const int w = __builtin_amdgcn_readfirstlane(tid >> 6);
    const int xcd = bid & 7, idx = bid >> 3;
    const int kq = xcd >> 1, tg = (xcd & 1) * 8 + (idx >> 2), dq = idx & 3;
    const int tb16 = 8 * tg + 2 * w, fs0 = 36 * kq;
    const unsigned lds0 = (unsigned)(uintptr_t)lds;
    unsigned voff[5], doff[5];
#pragma unroll
    for (int m = 0; m < 5; ++m) { const int i = min(5 * (w & 1) + m, 8), n = 64 * i + lane; voff[m] = (n / 18) * (D * 2) + (n % 18) * 16; doff[m] = (w >> 1) * DN_STEP + i * 1024; }
    const char* wsrc = wd + (size_t)(fs0 + (w >> 1)) * 32 * (D * 2) + dq * 288;
    const lds_cptr A = (lds_cptr)lds + (4 * (lane >> 4) + ((lane & 15) >> 2)) * 288 + (lane & 3) * 8;
    const char* hs = ws + WS_HF + ((size_t)tb16 * FS + fs0) * 1024;
    const unsigned lo = lane * 16;
    MARKS; MARK(0);

    auto dma = [&](int st, int m) {
        const char* base = wsrc;
        asm volatile("" : "+s"(base));
        glds16(base + (size_t)st * 2 * 32 * (D * 2), voff[m], lds0 + (st & 3) * DN_SLOT + doff[m]);
    };
#pragma unroll
    for (int st = 0; st < 3; ++st)
#pragma unroll
        for (int m = 0; m < 5; ++m) dma(st, m);
    dma(3, 0); dma(3, 1);
    f32x4 acc[9][2];
    bf16x8 b[3][2][2];
    dn_ldb(b[0], hs, lo, 0);
    dn_ldb(b[1], hs, lo, 1);
    lds_cptr Alo = A, Ahi = A + 2 * DN_SLOT;
    asm volatile("" : "+v"(Alo), "+v"(Ahi));
    auto addr = [&](int st, int i) { return ((st & 2) ? Ahi : Alo) + (st & 1) * DN_SLOT + (i / 9) * DN_STEP + (i % 9) * 32; };
    dn_wait<4>(b[0]);
    bf16x4 flo[4], fhi[4];
#pragma unroll
    for (int i = 0; i < 3; ++i) { flo[i] = trread(addr(0, i)); fhi[i] = trread(addr(0, i) + 16 * 288); }
    static_for<0, DN_NST * 18>([&](auto nc) {
        constexpr int n = decltype(nc)::value, st = n / 18, i = n % 18, set = st % 3;
        if constexpr (i == 0 && st + 2 < DN_NST) dn_ldb(b[(st + 2) % 3], hs, lo, st + 2);
        if constexpr (i == 15 && st + 1 < DN_NST) dn_wait<dn_young(st)>(b[(st + 1) % 3]);
        if constexpr (dn_piece(st, i) >= 0) dma(dn_piece(st, i) >> 3, dn_piece(st, i) & 7);
        constexpr bool more = n + 3 < DN_NST * 18;
        constexpr int nst = (n + 3) / 18, ni = (n + 3) % 18;
        const bf16x8 fr = join(flo[n & 3], fhi[n & 3]);
        if constexpr (n < 9) AMF_NEW(acc[i % 9][0], fr, b[set][0][i / 9]); else AMF_ACC(acc[i % 9][0], fr, b[set][0][i / 9]);
        SBAR();
        if constexpr (more) flo[(n + 3) & 3] = trread(addr(nst, ni));
        SBAR();
        if constexpr (n < 9) AMF_NEW(acc[i % 9][1], fr, b[set][1][i / 9]); else AMF_ACC(acc[i % 9][1], fr, b[set][1][i / 9]);
        SBAR();
        if constexpr (more) fhi[(n + 3) & 3] = trread(addr(nst, ni) + 16 * 288);
        SBAR();
    });
    MARK(1);
    asm volatile("s_nop 7\n\ts_nop 3" : "+v"(acc[7][0]), "+v"(acc[7][1]), "+v"(acc[8][0]), "+v"(acc[8][1]));
    char* dst = ws + WS_PART + (((size_t)kq * NT16 + tb16) * 36 + 9 * dq) * 1024;
#pragma unroll
    for (int nh = 0; nh < 2; ++nh)
#pragma unroll
        for (int i = 0; i < 9; ++i) gstore16(dst + (size_t)nh * 36 * 1024 + i * 1024, lo, __builtin_bit_cast(u32x4, acc[i][nh]));
    MARK(2); MARK_DUMP(3, 3);
}

constexpr int MIX_LDS = DN_LDS;
static_assert(MIX_LDS >= 3 * AT_STAGE && 2 * MIX_LDS <= 160 * 1024, "two workgroups per CU");
__global__ __launch_bounds__(256, 2) void k_mix(const char* wd, char* ws) {
    extern __shared__ __attribute__((aligned(16))) char lds[];
    STAMP_BEGIN;
    const int b = blockIdx.x;
    if (b < 256) attn_block(ws, lds, b); else down_block(wd, ws, lds, b - 256);
    STAMP_END(5);
}

constexpr int FN_A = 3 * KS * 1024, FN_LDS = FN_A + 8 * 3 * 1024;
DI void ldx1(bf16x8& d, const char* base, unsigned lo, int off) {
    asm volatile("global_load_dwordx4 %0, %1, %2 offset:%3" : "=v"(d) : "v"(lo), "s"(base), "n"(off));
}
template <int N> DI void waitx(bf16x8& a, bf16x8& b) { asm volatile("s_waitcnt vmcnt(%2)" : "+v"(a), "+v"(b) : "n"(N)); }
DI void fn_ldpx(f32x4 (&pq)[3][4], u32x2 (&xv)[3], const char* pp, unsigned lo, const char* xp, unsigned xo) {
#pragma unroll
    for (int t = 0; t < 3; ++t) {
#pragma unroll
        for (int k = 0; k < 4; ++k) asm volatile("global_load_dwordx4 %0, %1, %2 offset:%3" : "=v"(pq[t][k]) : "v"(lo), "s"(pp + (size_t)k * NT16 * 36 * 1024), "n"(t * 1024));
        asm volatile("global_load_dwordx2 %0, %1, %2 offset:%3" : "=v"(xv[t]) : "v"(xo), "s"(xp), "n"(t * 32));
    }
}
__global__ __launch_bounds__(512, 1) void k_final(const unsigned short* x, char* ws, unsigned short* out) {
    extern __shared__ __attribute__((aligned(16))) char lds[];
    STAMP_BEGIN;
    const int tid = threadIdx.x, lane = tid & 63;
    const int w = __builtin_amdgcn_readfirstlane(tid >> 6);
    const int xcd = blockIdx.x & 7, idx = blockIdx.x >> 3;
    const int tg = 2 * xcd + idx / 12, dg = idx % 12, kh = w >> 2, tb16 = 8 * tg + 2 * (w & 3);
    const unsigned lds0 = (unsigned)(uintptr_t)lds, lo = lane * 16;
    MARKS; MARK(0);
    {
        const char* src = ws + WS_WOP + (size_t)dg * FN_A;
#pragma unroll
        for (int i = 0; i < 7; ++i) { const int n = min(w * 7 + i, 3 * KS - 1); glds16(src + n * 1024, lo, lds0 + n * 1024); }
    }
    const char* os = ws + WS_OF + ((size_t)tb16 * KS + 9 * kh) * 1024;
    bf16x8 b[2][9];
    auto ldo = [&](int s) { ldx1(b[0][s], os + (s >> 2) * 4096, lo, (s & 3) * 1024); ldx1(b[1][s], os + KS * 1024 + (s >> 2) * 4096, lo, (s & 3) * 1024); };
#pragma unroll
    for (int s = 0; s < 4; ++s) ldo(s);
    MARK(1);
    asm volatile("s_waitcnt vmcnt(8)\n\ts_barrier" ::: "memory");
    MARK(2);
    const int tok = (tb16 + kh) * 16 + (lane & 15);
    const char* xp = (const char*)(x + (size_t)(tb16 + kh) * 16 * D + dg * 48);
    const unsigned xo2 = ((lane & 15) * D + 4 * (lane >> 4)) * 2;
    u32x2 xv[3];
    f32x4 pq[3][4];
    const char* pp = ws + WS_PART + ((size_t)(tb16 + kh) * 36 + 3 * dg) * 1024;
    const lds_cptr A = (lds_cptr)lds + lo + 9 * kh * 1024;
    f32x4 acc[2][3] = {};
    static_for<0, 9>([&](auto sc) {
        constexpr int s = decltype(sc)::value;
        bf16x8 a[3];
#pragma unroll
        for (int t = 0; t < 3; ++t) a[t] = *(const __attribute__((address_space(3))) bf16x8*)(A + (t * KS + s) * 1024);
        if constexpr (s + 4 < 9) ldo(s + 4);
        if constexpr (s == 4) fn_ldpx(pq, xv, pp, lo, xp, xo2);
        waitx<2 * ((s + 4 < 9 ? s + 4 : 8) - s) + (s >= 4 ? 15 : 0)>(b[0][s], b[1][s]);
#pragma unroll
        for (int t = 0; t < 3; ++t) {
            acc[0][t] = MFMA32K(a[t], b[0][s], acc[0][t]);
            acc[1][t] = MFMA32K(a[t], b[1][s], acc[1][t]);
        }
        SBAR();
    });
    MARK(3);
    typedef __attribute__((address_space(3))) f32x4* l128;
    const lds_ptr xo = (lds_ptr)lds + FN_A + lo;
#pragma unroll
    for (int t = 0; t < 3; ++t) *(l128)(xo + (w * 3 + t) * 1024) = kh ? acc[0][t] : acc[1][t];
    asm volatile("s_waitcnt lgkmcnt(0)\n\ts_barrier" ::: "memory");
    f32x4 sum[3];
#pragma unroll
    for (int t = 0; t < 3; ++t) {
        const f32x4 other = *(l128)(xo + ((w ^ 4) * 3 + t) * 1024);
        sum[t] = kh ? other + acc[1][t] : acc[0][t] + other;
    }
    asm volatile("s_waitcnt vmcnt(0)" : "+v"(xv[0]), "+v"(xv[1]), "+v"(xv[2]));
#pragma unroll
    for (int t = 0; t < 3; ++t) {
        asm volatile("" : "+v"(pq[t][0]), "+v"(pq[t][1]), "+v"(pq[t][2]), "+v"(pq[t][3]));
        const f32x4 ffn = (pq[t][0] + pq[t][1]) + (pq[t][2] + pq[t][3]);
        float o[4];
#pragma unroll
        for (int c = 0; c < 4; ++c) {
            const float f = rbf(ffn[c]);
            const float g = rbf(f * __builtin_amdgcn_rcpf(1.0f + __builtin_amdgcn_exp2f(f * -LOG2E)));
            const float xx = (c & 1) ? bf_hi(xv[t][c >> 1]) : bf_lo(xv[t][c >> 1]);
            o[c] = xx + rbf(rbf(sum[t][c]) * g);
        }
        *(u32x2*)(out + (size_t)tok * D + dg * 48 + 16 * t + 4 * (lane >> 4)) = (u32x2){cvtpk(o[0], o[1]), cvtpk(o[2], o[3])};
    }
    MARK(4);
    MARK(5); MARK_DUMP(4, 6);
    STAMP_END(4);
}

extern "C" void kernel_launch(void* const* d_in, const int* in_sizes, int n_in, void* d_out, int out_size, void* d_ws, size_t ws_size, hipStream_t stream) {
    const unsigned short* x = (const unsigned short*)d_in[0];
    const unsigned short* wqkv = (const unsigned short*)d_in[1];
    const unsigned short* wo = (const unsigned short*)d_in[2];
    const char* wg = (const char*)d_in[4];
    const char* wu = (const char*)d_in[5];
    const char* wd = (const char*)d_in[6];
    char* ws = (char*)d_ws;
    (void)hipFuncSetAttribute((const void*)k_gemm1, hipFuncAttributeMaxDynamicSharedMemorySize, 4 * G1_SLOT);
    (void)hipFuncSetAttribute((const void*)k_mix, hipFuncAttributeMaxDynamicSharedMemorySize, MIX_LDS);
    (void)hipFuncSetAttribute((const void*)k_final, hipFuncAttributeMaxDynamicSharedMemorySize, FN_LDS);
    k_prep<<<256, 1024, 0, stream>>>(x, wqkv, wo, ws);
    k_gemm1<<<256, 512, 4 * G1_SLOT, stream>>>(wg, wu, ws);
    k_mix<<<512, 256, MIX_LDS, stream>>>(wd, ws);
    k_final<<<192, 512, FN_LDS, stream>>>(x, ws, (unsigned short*)d_out);
}
```

```cpp
#include <hip/hip_runtime.h>
#include <hip/hip_bf16.h>
#include <stdint.h>
#include <type_traits>

typedef __attribute__((ext_vector_type(8))) short bf16x8;
typedef __attribute__((ext_vector_type(4))) short bf16x4;
typedef __attribute__((ext_vector_type(2))) __bf16 bf16p;
typedef __attribute__((ext_vector_type(2))) float f32x2;
typedef __attribute__((ext_vector_type(4))) float f32x4;
typedef __attribute__((ext_vector_type(16))) float f32x16;
typedef __attribute__((ext_vector_type(2))) unsigned u32x2;
typedef __attribute__((ext_vector_type(3))) unsigned u32x3;
typedef __attribute__((ext_vector_type(4))) unsigned u32x4;
typedef __attribute__((address_space(3))) const char* lds_cptr;
typedef __attribute__((address_space(3))) char* lds_ptr;
typedef short v4i16_t __attribute__((ext_vector_type(4)));

#define DI __device__ __forceinline__
#define MFMA16(a, b, c) __builtin_amdgcn_mfma_f32_32x32x16_bf16((a), (b), (c), 0, 0, 0)
#define MFMA32K(a, b, c) __builtin_amdgcn_mfma_f32_16x16x32_bf16((a), (b), (c), 0, 0, 0)
#define AMF_ACC(c, a, b) asm volatile("v_mfma_f32_16x16x32_bf16 %0, %1, %2, %0" : "+v"(c) : "v"(a), "v"(b))
#define AMF_NEW(c, a, b) asm volatile("v_mfma_f32_16x16x32_bf16 %0, %1, %2, 0" : "=&v"(c) : "v"(a), "v"(b))
#define MFMA8(a, b, c) __builtin_amdgcn_mfma_f32_32x32x8bf16_1k((a), (b), (c), 0, 0, 0)
#define SBAR() __builtin_amdgcn_sched_barrier(0)

template <int I, int N, class Fn> DI void static_for(Fn&& fn) {
    if constexpr (I < N) { fn(std::integral_constant<int, I>{}); static_for<I + 1, N>(fn); }
}

constexpr int S = 2048, D = 576, NH = 32, DH = 18, F = 4608;
constexpr int NTB = S / 32;
constexpr int NT16 = S / 16;
constexpr int KS = D / 32;
constexpr int FS = F / 32;

constexpr int KVREC = 2560;
constexpr size_t WS_XNF = 0;
constexpr size_t WS_WQP = WS_XNF + (size_t)NT16 * KS * 1024;
constexpr int QREC = 1280;
constexpr size_t WS_WOP = WS_WQP + (size_t)NH * KS * 4096;
constexpr size_t WS_Q = WS_WOP + (size_t)36 * KS * 1024;
constexpr size_t WS_KV = WS_Q + (size_t)NH * NTB * QREC;
constexpr size_t WS_OF = WS_KV + (size_t)NH * NTB * KVREC;
constexpr size_t WS_HF = WS_OF + (size_t)NT16 * KS * 1024;
constexpr int PTILE = 768;
constexpr size_t WS_PART = WS_HF + (size_t)NT16 * FS * 1024;
constexpr size_t WS_END = WS_PART + (size_t)4 * NT16 * 36 * PTILE;

#define STAMP_BEGIN
#define STAMP_END(k)
#define MARKS
#define MARK(i)
#define MARK_DUMP(k, n)

constexpr float LOG2E = 1.4426950408889634f;
constexpr float QSCALE = 0.23570226039551584f * LOG2E;

DI unsigned cvtpk(float lo, float hi) { return __builtin_bit_cast(unsigned, __builtin_convertvector((f32x2){lo, hi}, bf16p)); }
DI float bf_lo(unsigned u) { return __uint_as_float(u << 16); }
DI float bf_hi(unsigned u) { return __uint_as_float(u & 0xffff0000u); }
DI float rbf(float x) { return bf_lo(cvtpk(x, x)); }
DI bf16x4 trread(lds_cptr p) { return __builtin_bit_cast(bf16x4, __builtin_amdgcn_ds_read_tr16_b64_v4i16((__attribute__((address_space(3))) v4i16_t*)p)); }
DI bf16x8 trfrag(lds_cptr p0, lds_cptr p1) { const bf16x4 a = trread(p0), b = trread(p1); return (bf16x8){a[0], a[1], a[2], a[3], b[0], b[1], b[2], b[3]}; }
DI bf16x8 join(bf16x4 a, bf16x4 b) { return (bf16x8){a[0], a[1], a[2], a[3], b[0], b[1], b[2], b[3]}; }
DI float sum16x(float x) { auto r = __builtin_amdgcn_permlane16_swap(__float_as_uint(x), __float_as_uint(x), false, false); return __uint_as_float(r[0]) + __uint_as_float(r[1]); }
DI float sum32x(float x) { auto r = __builtin_amdgcn_permlane32_swap(__float_as_uint(x), __float_as_uint(x), false, false); return __uint_as_float(r[0]) + __uint_as_float(r[1]); }
DI float max16x(float x) { auto r = __builtin_amdgcn_permlane16_swap(__float_as_uint(x), __float_as_uint(x), false, false); return fmaxf(__uint_as_float(r[0]), __uint_as_float(r[1])); }
DI float max32x(float x) { auto r = __builtin_amdgcn_permlane32_swap(__float_as_uint(x), __float_as_uint(x), false, false); return fmaxf(__uint_as_float(r[0]), __uint_as_float(r[1])); }
template <int CTRL> DI float dppf(float x) { return __uint_as_float(__builtin_amdgcn_update_dpp(0u, __float_as_uint(x), CTRL, 0xf, 0xf, true)); }
DI float rowmax16(float x) {
    x = fmaxf(x, dppf<0xB1>(x));
    x = fmaxf(x, dppf<0x4E>(x));
    x = fmaxf(x, dppf<0x141>(x));
    return fmaxf(x, dppf<0x140>(x));
}
DI float swap32(float x) { auto r = __builtin_amdgcn_permlane32_swap(__float_as_uint(x), __float_as_uint(x), false, false); return __uint_as_float((threadIdx.x & 32) ? r[0] : r[1]); }

DI void glds16(const void* sbase, unsigned voff, unsigned lds_base) {
    asm volatile("s_mov_b32 m0, %2\n\ts_nop 0\n\tglobal_load_lds_dwordx4 %0, %1" :: "v"(voff), "s"(sbase), "s"(lds_base) : "memory");
}
#define STPOL "sc1"
DI void gstore16(void* sbase, unsigned voff, u32x4 v) { asm volatile("global_store_dwordx4 %0, %1, %2 " STPOL "\n\ts_nop 1" :: "v"(voff), "v"(v), "s"(sbase) : "memory"); }
DI void gstore8(void* sbase, unsigned voff, u32x2 v) { asm volatile("global_store_dwordx2 %0, %1, %2" :: "v"(voff), "v"(v), "s"(sbase) : "memory"); }
DI u32x3 pack24(f32x4 v) {
    const unsigned a = __float_as_uint(v[0]), b = __float_as_uint(v[1]), c = __float_as_uint(v[2]), d = __float_as_uint(v[3]);
    return (u32x3){__builtin_amdgcn_perm(a, d, 0x07060501u), __builtin_amdgcn_perm(b, d, 0x07060502u), __builtin_amdgcn_perm(c, d, 0x07060503u)};
}
DI f32x4 unpack24(u32x3 p) {
    const unsigned t = __builtin_amdgcn_perm(p[2], p[1], 0x04000c0cu);
    const unsigned d = __builtin_amdgcn_perm(t, p[0], 0x0706000cu);
    return (f32x4){__uint_as_float(p[0] & 0xffffff00u), __uint_as_float(p[1] & 0xffffff00u), __uint_as_float(p[2] & 0xffffff00u), __uint_as_float(d)};
}
DI void gstore12(void* sbase, unsigned voff, u32x3 v) { asm volatile("global_store_dwordx3 %0, %1, %2 " STPOL "\n\ts_nop 1" :: "v"(voff), "v"(v), "s"(sbase) : "memory"); }
DI void gstore16p(void* p, u32x4 v) { asm volatile("global_store_dwordx4 %0, %1, off " STPOL "\n\ts_nop 1" :: "v"(p), "v"(v) : "memory"); }
#define WAITV_BAR(n) asm volatile("s_waitcnt vmcnt(" #n ") lgkmcnt(0)\n\ts_barrier" ::: "memory")

DI int wo_row(int ks, int kg, int j) {
    return ks < 16 ? DH * (2 * ks + (kg >> 1)) + 8 * (j >> 2) + 4 * (kg & 1) + (j & 3) : DH * (16 * (ks - 16) + 4 * kg + (j >> 1)) + 16 + (j & 1);
}
DI float sum32(float x) {
    x += dppf<0xB1>(x);
    x += dppf<0x4E>(x);
    x += dppf<0x141>(x);
    x += dppf<0x140>(x);
    return sum16x(x);
}

__global__ __launch_bounds__(1024) void k_prep(const unsigned short* __restrict__ x, const unsigned short* __restrict__ wqkv, const unsigned short* __restrict__ wo, char* __restrict__ ws) {
    STAMP_BEGIN;
    __shared__ __attribute__((aligned(16))) char stage[KS * 512];
    const int tid = threadIdx.x;
    const int wv = __builtin_amdgcn_readfirstlane(tid >> 6);
    if (wv < 4) {
        const int row = blockIdx.x * 8 + (tid >> 5), t = tid & 31;
        const u32x4* xr = (const u32x4*)(x + (size_t)row * D);
        u32x4 v[3];
        v[0] = xr[t]; v[1] = xr[t + 32]; v[2] = (t < 8) ? xr[t + 64] : (u32x4){0u, 0u, 0u, 0u};
        float sum = 0.f;
#pragma unroll
        for (int i = 0; i < 3; ++i)
#pragma unroll
            for (int j = 0; j < 4; ++j) sum += bf_lo(v[i][j]) + bf_hi(v[i][j]);
        const float mean = sum32(sum) * (1.0f / D);
        float sq = 0.f;
#pragma unroll
        for (int i = 0; i < 3; ++i)
            if (i < 2 || t < 8)
#pragma unroll
                for (int j = 0; j < 4; ++j) { const float a = bf_lo(v[i][j]) - mean, b = bf_hi(v[i][j]) - mean; sq += a * a + b * b; }
        const float mb = rbf(mean);
        const float den = rbf(__fsqrt_rn(rbf(rbf(sum32(sq) * (1.0f / D)) + 1e-5f)));
        const float rc = __builtin_amdgcn_rcpf(den);
        auto quot = [&](float a) { const float q = a * rc; return __builtin_fmaf(__builtin_fmaf(-q, den, a), rc, q); };
#pragma unroll
        for (int i = 0; i < 3; ++i) {
            if (i < 2 || t < 8) {
                const int p = t + 32 * i;
                unsigned o[4];
#pragma unroll
                for (int j = 0; j < 4; ++j) o[j] = cvtpk(quot(rbf(bf_lo(v[i][j]) - mb)), quot(rbf(bf_hi(v[i][j]) - mb)));
                char* d = stage + (p >> 2) * 512 + (p & 1) * 256 + (tid >> 5) * 16 + ((p >> 1) & 1) * 8;
                *(u32x2*)d = (u32x2){o[0], o[1]};
                *(u32x2*)(d + 128) = (u32x2){o[2], o[3]};
            }
        }
        asm volatile("s_waitcnt lgkmcnt(0)\n\ts_barrier" ::: "memory");
        char* dst = ws + WS_XNF + (size_t)(blockIdx.x >> 1) * KS * 1024 + (blockIdx.x & 1) * 128;
#pragma unroll
        for (int i = 0; i < 3; ++i) {
            const int n = tid + 256 * i;
            if (n < KS * 32) gstore16p(dst + (n >> 3) * 256 + (n & 7) * 16, *(const u32x4*)(stage + n * 16));
        }
    } else if (wv < 13) {
        const int n = blockIdx.x * 576 + (tid - 256);
        const int pc = n & 3, r = (n >> 2) & 31, sel = (n >> 7) & 1, hs = n >> 8, s = hs % KS, h = hs / KS;
        const int c = pc ^ (2 * ((r >> 2) & 1)), t = c >> 1, g = c & 1;
        const unsigned* w = (const unsigned*)(wqkv + (size_t)(32 * s + r) * (3 * D) + DH * h);
        u32x4 o = {0u, 0u, 0u, 0u};
        if (sel == 0 || t == 0) {
            const int col = (sel == 0 ? t * D : 2 * D) + 8 * g;
            o = (u32x4){w[col / 2], w[col / 2 + 1], w[col / 2 + 2], w[col / 2 + 3]};
        } else if (g == 0) {
            o = (u32x4){w[8], w[D / 2 + 8], w[D + 8], 0u};
        }
        gstore16p(ws + WS_WQP + (size_t)n * 16, o);
    } else if (tid < 256 + 576 + 162) {
        const int n = blockIdx.x * 162 + (tid - (256 + 576));
        const int lane = n & 63, fs = n >> 6, ks = fs % KS, dt = fs / KS;
        unsigned short v[8];
#pragma unroll
        for (int j = 0; j < 8; ++j) v[j] = wo[(size_t)wo_row(ks, lane >> 4, j) * D + 16 * dt + (lane & 15)];
        gstore16p(ws + WS_WOP + (size_t)n * 16, (u32x4){v[0] | ((unsigned)v[1] << 16), v[2] | ((unsigned)v[3] << 16), v[4] | ((unsigned)v[5] << 16), v[6] | ((unsigned)v[7] << 16)});
    }
    STAMP_END(0);
}

#define G1_MAP 1
constexpr int G1_SLOT = 9 * 4096;

DI int img_aoff(int lane) {
    const int kg = lane >> 4, q = (lane & 15) >> 2, p = lane & 3;
    return (4 * kg + q) * 64 + (((p >> 1) ^ (2 * (kg & 1))) * 16) + 8 * (p & 1);
}
DI void g1_ldx(bf16x8& d, const char* base, unsigned lo, int ks) {
    asm volatile("global_load_dwordx4 %0, %1, %2 offset:%3" : "=v"(d) : "v"(lo), "s"(base + (ks >> 2) * 4096), "n"((ks & 3) * 1024));
}
DI float glu(float g, float u) { return u * __builtin_amdgcn_rcpf(1.0f + __builtin_amdgcn_exp2f(g * -LOG2E)); }

constexpr int g1_group(int u) { return u < 4 ? (u & 1) : u / 2; }
constexpr int g1_half(int u) { return u < 4 ? (u >> 1) : (u & 1); }
constexpr int g1_dma_at(int u, int g) {
    if (u == 0) return (g >= 12 && g <= 20 && !(g & 1)) ? 1 * 8 + (g - 12) / 2 : (g >= 22 && g <= 30 && !(g & 1)) ? 2 * 8 + (g - 22) / 2 : -1;
    if (u == 1) return -1;
    if (u == 2) return (g <= 16 && !(g & 3)) ? 3 * 8 + g / 4 : (g >= 20 && g <= 28 && !(g & 1)) ? 4 * 8 + (g - 20) / 2 : -1;
    if (u <= 9) { const int sp = 5; return (g % sp == 0 && g / sp < 5) ? (u + 2) * 8 + g / sp : -1; }
    return -1;
}
constexpr int g1_x_at(int u, int g) {
    if (g & 3) return -1;
    if (u == 0) return g / 4 < 3 ? 6 + g / 4 : -1;
    if (u == 1) return 9 + g / 4;
    return -1;
}
constexpr int g1_x_younger(int ks, int u, int g) {
    int n = 0; bool seen = ks < 6;
    if (ks < 6) n += 2 * (5 - ks);
    for (int uu = 0; uu <= u; ++uu)
        for (int gg = 0; gg < 36 && (uu < u || gg <= g); ++gg) {
            const int x = g1_x_at(uu, gg);
            if (x >= 0) { if (seen) n += 2; if (x == ks) seen = true; }
            if (g1_dma_at(uu, gg) >= 0 && seen) n += 1;
        }
    return n;
}

struct G1Ctx {
    const char *wrow, *wq, *xs;
    char* hdst;
    unsigned lds0, dsto, voff, lo;
    int fbase, fsingle, w;
};
DI void g1_piece(const G1Ctx& c, int unit, int m) {
    const int grp = g1_group(unit), hk = g1_half(unit);
    const unsigned slot = c.lds0 + (unit & 3) * G1_SLOT;
    const int mm = (m == 4 && c.w >= 4) ? 3 : m;
    if (grp == 0) {
        const int pi = c.w + 8 * mm;
        glds16(c.wq + hk * G1_SLOT + pi * 1024, c.lo, slot + pi * 1024);
    } else {
        const int f0 = grp == 5 ? c.fsingle : c.fbase + 32 * (grp - 1);
        glds16(c.wrow + (size_t)(288 * hk + 64 * mm) * (F * 2) + f0 * 2, c.voff, slot + c.dsto + mm * 8192);
    }
}

struct G1Epi { float e[2], h[2]; u32x4 pk; };
DI void g1_epi_slice(int s, const f32x4 (&c)[2][2][2], G1Epi& t, char* hdst, unsigned lo) {
    const int v = s >> 2, nh = v >> 3, j = v & 7, ph = s & 3;
    if (ph == 0) { t.e[v & 1] = __builtin_amdgcn_exp2f(c[0][j >> 2][nh][j & 3] * -LOG2E); asm volatile("" : "+v"(t.e[v & 1])); }
    if (ph == 1) { t.e[v & 1] = __builtin_amdgcn_rcpf(1.0f + t.e[v & 1]); asm volatile("" : "+v"(t.e[v & 1])); }
    if (ph == 2) { t.h[v & 1] = c[1][j >> 2][nh][j & 3] * t.e[v & 1]; asm volatile("" : "+v"(t.h[v & 1])); }
    if (ph == 3 && (v & 1)) {
        t.pk[j >> 1] = cvtpk(t.h[0], t.h[1]);
        asm volatile("" : "+v"(t.pk[j >> 1]));
        if (j == 7) gstore16(hdst + (size_t)nh * FS * 1024, lo, t.pk);
    }
}

struct QkvEpi { char *qg, *kg; lds_ptr stg; float kmax; };
DI void qkv_slice(int k, const f32x4 (&c)[2][2][2], QkvEpi& e, unsigned lo) {
    typedef __attribute__((address_space(3))) u32x2* l64;
    typedef __attribute__((address_space(3))) unsigned* l32;
    typedef __attribute__((address_space(3))) unsigned short* l16;
    typedef __attribute__((address_space(3))) const u32x4* l128;
    asm volatile("" : "+v"(lo));
    const int lane = lo >> 4;
    const int t = lane & 15, g4 = lane >> 4;
    const lds_ptr rec = e.stg + QREC;
    if (k < 4) {
        const int nh = k >> 1, r = 16 * nh + t;
        const f32x4 ex = c[1][1][nh];
        if ((k & 1) == 0) {
            const unsigned fo = (r + 32 * (g4 >> 1)) * 16 + (g4 & 1) * 8;
            const f32x4 q = c[0][0][nh], kk = c[0][1][nh];
            *(l64)(e.stg + fo) = (u32x2){cvtpk(q[0] * QSCALE, q[1] * QSCALE), cvtpk(q[2] * QSCALE, q[3] * QSCALE)};
            *(l64)(rec + fo) = (u32x2){cvtpk(kk[0], kk[1]), cvtpk(kk[2], kk[3])};
            float n2 = g4 == 0 ? ex[2] * ex[2] + ex[3] * ex[3] : 0.f;
            n2 += (kk[0] * kk[0] + kk[1] * kk[1]) + (kk[2] * kk[2] + kk[3] * kk[3]);
            n2 = sum32x(sum16x(n2));
            e.kmax = nh ? fmaxf(e.kmax, n2) : n2;
            asm volatile("" : "+v"(e.kmax));
        } else {
            const f32x4 v = c[1][0][nh];
            *(l64)(rec + 1280 + nh * 640 + (g4 & 1) * 320 + t * 16 + (g4 >> 1) * 8) = (u32x2){cvtpk(v[0], v[1]), cvtpk(v[2], v[3])};
            const unsigned e01 = cvtpk(ex[0], ex[1]), e23 = cvtpk(ex[2], ex[3]);
            const bool g0 = g4 == 0, g1 = g4 == 1;
            const lds_ptr spare = e.stg + 1168;
            const lds_ptr vt = rec + 1280 + nh * 640 + ((t >> 2) & 1) * 320 + (4 * (t >> 3) + (t & 3)) * 2;
            *(l64)(g0 ? rec + 1024 + r * 8 : spare + 8) = (u32x2){e23, 0u};
            *(l32)(g0 ? e.stg + 1024 + r * 4 : spare + 4) = cvtpk(ex[0] * QSCALE, ex[1] * QSCALE);
            *(l16)(g0 ? spare : vt + (g1 ? 16 : 16 + g4) * 16) = (unsigned short)(g1 ? e01 : (g4 == 2 ? 0x3f80u : 0u));
            *(l16)(g1 ? vt + 17 * 16 : spare + 2) = (unsigned short)(e01 >> 16);
        }
    } else if (k == 4) {
        const float m = rowmax16(e.kmax);
        if (lane == 0) *(l32)(e.stg + 1152) = __float_as_uint(m);
    } else if (k == 5) {
        const unsigned o0 = lane * 16, oq = min(lane, 8) * 16 + 1024;
        const u32x4 d0 = *(l128)(e.stg + o0), d1 = *(l128)(e.stg + oq);
        gstore16(e.qg, o0, d0);
        gstore16(e.qg, oq, d1);
    } else {
        const unsigned o0 = lane * 16, ok = min(lane, 31) * 16 + 2048;
        const u32x4 d2 = *(l128)(rec + o0), d3 = *(l128)(rec + 1024 + o0), d4 = *(l128)(rec + ok);
        gstore16(e.kg, o0, d2);
        gstore16(e.kg, o0 + 1024, d3);
        gstore16(e.kg, ok, d4);
    }
}

struct G1Ring { bf16x4 lo[3], hi[3]; };
constexpr int g1_meet(int u) { return u == 1 ? 18 : u == 3 ? 10 : u == 10 ? 0 : 5; }
template <int U>
DI void g1_unit(const G1Ctx& cx, lds_cptr& b0, lds_cptr& b1, bool odd, G1Ring& fr, bf16x8 (&xb)[2][KS], f32x4 (&c)[2][2][2],
                const f32x4 (&cprev)[2][2][2], QkvEpi& qe) {
    constexpr int GRP = g1_group(U), HK = g1_half(U);
    constexpr bool PAIR = GRP != 5, NPAIR = g1_group(U + 1) != 5;
    constexpr int PER = PAIR ? 4 : 2, N = 9 * PER;
    constexpr bool EPI = U >= 4;
    auto addr = [&](int i) {
        const bool nx = i >= N, pr = nx ? NPAIR : PAIR;
        const int j = nx ? i - N : i, ks = pr ? j >> 2 : j >> 1, sel = pr ? (j >> 1) & 1 : j & 1, t = pr ? j & 1 : 0;
        return (t ? b1 : b0) + ks * 4096 + sel * 2048;
    };
    G1Epi ep;
    char* hprev = cx.hdst + (U / 2 - 2) * 1024;
    static_for<0, N>([&](auto ic) {
        constexpr int i = decltype(ic)::value;
        constexpr int kl = i / PER, ks = 9 * HK + kl, sel = PAIR ? (i >> 1) & 1 : i & 1, t = PAIR ? i & 1 : 0;
        constexpr int g = PAIR ? i : 2 * i;
        constexpr int x = g1_x_at(U, g), dm = g1_dma_at(U, g);
        constexpr bool more = U < 11 || i + 2 < N;
        if constexpr (i == N - 2 && U < 11) {
            asm volatile("s_waitcnt vmcnt(%0) lgkmcnt(0)\n\ts_barrier" :: "n"(g1_meet(U)) : "memory");
            constexpr int delta = (((U + 1) & 3) - (U & 3)) * G1_SLOT;
            if constexpr (U == 9) b0 = odd ? b1 : b0;
            b0 += delta; b1 += delta;
            asm volatile("" : "+v"(b0), "+v"(b1));
        }
        if constexpr (x >= 0) { g1_ldx(xb[0][x], cx.xs, cx.lo, x); g1_ldx(xb[1][x], cx.xs + KS * 1024, cx.lo, x); }
        if constexpr (dm >= 0) g1_piece(cx, dm >> 3, dm & 7);
        if constexpr ((U == 0 || U == 2) && (i & 3) == 0) {
            constexpr int young = g1_x_younger(ks, U, g);
            asm volatile("s_waitcnt vmcnt(%2)" : "+v"(xb[0][ks]), "+v"(xb[1][ks]) : "n"(young));
        }
        constexpr bool SWAP = GRP == 0 && sel == 1 && t == 0;
        const bf16x8 a = join(fr.lo[i % 3], fr.hi[i % 3]);
        if constexpr (ks == 0) { if constexpr (SWAP) AMF_NEW(c[sel][t][0], xb[0][ks], a); else AMF_NEW(c[sel][t][0], a, xb[0][ks]); }
        else { if constexpr (SWAP) AMF_ACC(c[sel][t][0], xb[0][ks], a); else AMF_ACC(c[sel][t][0], a, xb[0][ks]); }
        SBAR();
        if constexpr (more) fr.lo[(i + 2) % 3] = trread(addr(i + 2));
        SBAR();
        if constexpr (ks == 0) { if constexpr (SWAP) AMF_NEW(c[sel][t][1], xb[1][ks], a); else AMF_NEW(c[sel][t][1], a, xb[1][ks]); }
        else { if constexpr (SWAP) AMF_ACC(c[sel][t][1], xb[1][ks], a); else AMF_ACC(c[sel][t][1], a, xb[1][ks]); }
        SBAR();
        if constexpr (more) fr.hi[(i + 2) % 3] = trread(addr(i + 2) + 1024);
        SBAR();
        if constexpr (U == 3 && (i & 3) == 2 && i / 4 < 7) qkv_slice(i / 4, cprev, qe, cx.lo);
        if constexpr (EPI && PAIR) {
            constexpr int sl = 36 * HK + i - 4;
            if constexpr (sl >= 0 && sl < 64) g1_epi_slice(sl, cprev, ep, hprev, cx.lo);
        } else if constexpr (EPI) {
            constexpr int sl = 2 * (18 * HK + i - 2);
            if constexpr (sl >= 0 && sl < 64) { g1_epi_slice(sl, cprev, ep, hprev, cx.lo); g1_epi_slice(sl + 1, cprev, ep, hprev, cx.lo); }
        }
        SBAR();
    });
}

__global__ __launch_bounds__(512, 2) void k_gemm1(const char* wg, const char* wu, char* ws) {
    extern __shared__ __attribute__((aligned(16))) char lds[];
    STAMP_BEGIN;
    const int tid = threadIdx.x, lane = tid & 63;
    const int w = __builtin_amdgcn_readfirstlane(tid >> 6);
    const int xcd = blockIdx.x & 7, idx = blockIdx.x >> 3;
    const int cg = 8 * (xcd >> 1) + (idx & 7), rg = 4 * (xcd & 1) + (idx >> 3), tb = 8 * rg + w;
    MARKS; MARK(0);

    const int odd = cg & 1;
    G1Ctx cx;
    cx.w = w; cx.lds0 = (unsigned)(uintptr_t)lds; cx.lo = lane * 16;
    cx.fbase = 144 * cg + 16 * odd; cx.fsingle = odd ? 144 * cg - 16 : 144 * cg + 128;
    cx.wq = ws + WS_WQP + (size_t)cg * KS * 4096;
    cx.xs = ws + WS_XNF + (size_t)(2 * tb) * KS * 1024;
    cx.wrow = ((w & 1) ? wu : wg) + (size_t)(32 * (w >> 2) + 16 * ((w >> 1) & 1)) * (F * 2);
    cx.dsto = (w >> 2) * 4096 + (w & 1) * 2048 + ((w >> 1) & 1) * 1024;
    cx.voff = (lane >> 2) * (F * 2) + (((lane & 3) ^ (2 * ((lane >> 4) & 1))) * 16);
    cx.hdst = ws + WS_HF + ((size_t)(2 * tb) * FS + cx.fbase / 32) * 1024;

#pragma unroll
    for (int m = 0; m < 5; ++m) g1_piece(cx, 0, m);
    bf16x8 xb[2][KS];
#pragma unroll
    for (int s = 0; s < 6; ++s) { g1_ldx(xb[0][s], cx.xs, cx.lo, s); g1_ldx(xb[1][s], cx.xs + KS * 1024, cx.lo, s); }

    const int aoff0 = img_aoff(lane);
    lds_cptr b0 = (lds_cptr)lds + aoff0, b1 = (lds_cptr)lds + (aoff0 ^ 32);
    asm volatile("" : "+v"(b0), "+v"(b1));
    f32x4 ca[2][2][2], cb[2][2][2];
    QkvEpi qe;
    qe.qg = ws + WS_Q + ((size_t)cg * NTB + tb) * QREC;
    qe.kg = ws + WS_KV + ((size_t)cg * NTB + tb) * KVREC;
    qe.stg = (lds_ptr)lds + 2 * G1_SLOT + w * 4608;

    WAITV_BAR(12);
    MARK(1);
    G1Ring fr;
#pragma unroll
    for (int i = 0; i < 2; ++i) {
        const lds_cptr a = (i & 1) ? b1 : b0;
        fr.lo[i] = trread(a); fr.hi[i] = trread(a + 1024);
    }
    g1_unit<0>(cx, b0, b1, odd, fr, xb, ca, cb, qe);
    g1_unit<1>(cx, b0, b1, odd, fr, xb, cb, ca, qe);
    MARK(2);
    g1_unit<2>(cx, b0, b1, odd, fr, xb, ca, cb, qe);
    MARK(3);
    g1_unit<3>(cx, b0, b1, odd, fr, xb, cb, ca, qe);
    MARK(4);
    g1_unit<4>(cx, b0, b1, odd, fr, xb, ca, cb, qe);
    g1_unit<5>(cx, b0, b1, odd, fr, xb, ca, cb, qe);
    MARK(5);
    g1_unit<6>(cx, b0, b1, odd, fr, xb, cb, ca, qe);
    g1_unit<7>(cx, b0, b1, odd, fr, xb, cb, ca, qe);
    MARK(6);
    g1_unit<8>(cx, b0, b1, odd, fr, xb, ca, cb, qe);
    g1_unit<9>(cx, b0, b1, odd, fr, xb, ca, cb, qe);
    MARK(7);
    g1_unit<10>(cx, b0, b1, odd, fr, xb, cb, ca, qe);
    g1_unit<11>(cx, b0, b1, odd, fr, xb, cb, ca, qe);
    asm volatile("s_nop 7\n\ts_nop 3" : "+v"(cb[0][0][0]), "+v"(cb[0][0][1]), "+v"(cb[1][0][0]), "+v"(cb[1][0][1]));
    {
        char* hs = ws + WS_HF + ((size_t)(2 * tb) * FS + cx.fsingle / 32) * 1024 + odd * 8;
#pragma unroll
        for (int nh = 0; nh < 2; ++nh) {
            float h[4];
#pragma unroll
            for (int j = 0; j < 4; ++j) h[j] = glu(cb[0][0][nh][j], cb[1][0][nh][j]);
            gstore8(hs + (size_t)nh * FS * 1024, cx.lo, (u32x2){cvtpk(h[0], h[1]), cvtpk(h[2], h[3])});
        }
    }
    MARK(8); MARK_DUMP(1, 9);
    STAMP_END(1);
}

constexpr int AT_STAGE = 8 * KVREC;
constexpr float AT_BOUND_MAX = 50.0f;

struct KFrag { bf16x8 k1; bf16x4 k2; };
struct VFrag { bf16x8 v0, v1; };
DI KFrag at_readk(lds_cptr rec, int lane) {
    KFrag k;
    k.k1 = *(const __attribute__((address_space(3))) bf16x8*)(rec + lane * 16);
    k.k2 = *(const __attribute__((address_space(3))) bf16x4*)(rec + 1024 + (lane & 31) * 8);
    return k;
}
DI VFrag at_readv(lds_cptr recv) {
    VFrag v;
    v.v0 = *(const __attribute__((address_space(3))) bf16x8*)(recv);
    v.v1 = *(const __attribute__((address_space(3))) bf16x8*)(recv + 640);
    return v;
}
DI bf16x8 packp(const f32x16& p, int s) {
    return __builtin_bit_cast(bf16x8, (u32x4){cvtpk(p[8 * s], p[8 * s + 1]), cvtpk(p[8 * s + 2], p[8 * s + 3]), cvtpk(p[8 * s + 4], p[8 * s + 5]), cvtpk(p[8 * s + 6], p[8 * s + 7])});
}

DI void at_dma(const char* kvh, int stage, unsigned ldsb, int w, int lane) {
    const char* src = kvh + (size_t)stage * AT_STAGE + w * 5 * 1024;
    const unsigned dst = ldsb + w * 5 * 1024, lo = lane * 16;
    asm volatile("s_mov_b32 m0, %2\n\ts_nop 0\n\t"
                 "global_load_lds_dwordx4 %0, %1\n\tglobal_load_lds_dwordx4 %0, %1 offset:1024\n\t"
                 "global_load_lds_dwordx4 %0, %1 offset:2048\n\tglobal_load_lds_dwordx4 %0, %1 offset:3072"
                 :: "v"(lo), "s"(src), "s"(dst) : "memory");
    glds16(src + 4096, lo, dst + 4096);
}

#define AMFMA16_NEW(d, a, b, c) asm volatile("v_mfma_f32_32x32x16_bf16 %0, %1, %2, %3" : "=&v"(d) : "v"(a), "v"(b), "v"(c))
#define AMFMA16_ACC(d, a, b) asm volatile("v_mfma_f32_32x32x16_bf16 %0, %1, %2, %0" : "+v"(d) : "v"(a), "v"(b))
#define AMFMA8_ACC(d, a, b) asm volatile("v_mfma_f32_32x32x8_bf16 %0, %1, %2, %0" : "+v"(d) : "v"(a), "v"(b))
#define AT_EXP4(E, SX, a) do { _Pragma("unroll") for (int i_ = 0; i_ < 4; ++i_) asm volatile("v_exp_f32 %0, %1" : "=v"(E[i_]) : "v"(SX[a + i_])); } while (0)
#define AT_PK2(DST, k, E) do { asm volatile("v_cvt_pk_bf16_f32 %0, %1, %2" : "=v"(DST[k]) : "v"(E[0]), "v"(E[1]));   \
                               asm volatile("v_cvt_pk_bf16_f32 %0, %1, %2" : "=v"(DST[k + 1]) : "v"(E[2]), "v"(E[3])); } while (0)
#define AT_STEP(SX, PX0, PX1, SY, CY, QY1, QY2, KF, OY, PY0, PY1, VF, LOADS)                                   \
    do {                                                                                                       \
        float e0_[4], e1_[4], e2_[4];                                                                          \
        AMFMA16_NEW(SY, KF.k1, QY1, CY);                                                                       \
        LOADS;                                                                                                 \
        SBAR();                                                                                                \
        AT_EXP4(e0_, SX, 0);                                                                                   \
        AT_PK2(PY1, 2, etail);                                                                                 \
        AMFMA8_ACC(SY, KF.k2, QY2);                                                                            \
        AT_EXP4(e1_, SX, 4);                                                                                   \
        AT_PK2(PX0, 0, e0_);                                                                                   \
        AMFMA16_ACC(OY, VF.v0, PY0);                                                                           \
        AT_EXP4(e2_, SX, 8);                                                                                   \
        AT_PK2(PX0, 2, e1_);                                                                                   \
        AMFMA16_ACC(OY, VF.v1, PY1);                                                                           \
        AT_EXP4(etail, SX, 12);                                                                                \
        AT_PK2(PX1, 0, e2_);                                                                                   \
        SBAR();                                                                                                \
    } while (0)

DI void at_online(const KFrag& k, const VFrag& v, bf16x8 q1, bf16x4 q2, float& m, f32x16& o) {
    f32x16 z = {};
    f32x16 s = MFMA16(k.k1, q1, z);
    s = MFMA8(k.k2, q2, s);
    float t = s[0];
#pragma unroll
    for (int i = 1; i < 16; ++i) t = fmaxf(t, s[i]);
    t = fmaxf(t, swap32(t));
    const float mn = fmaxf(m, t);
    const float alpha = __builtin_amdgcn_exp2f(m - mn);
    m = mn;
#pragma unroll
    for (int i = 0; i < 16; ++i) { s[i] = __builtin_amdgcn_exp2f(s[i] - mn); o[i] *= alpha; }
    o = MFMA16(v.v0, packp(s, 0), o);
    o = MFMA16(v.v1, packp(s, 1), o);
}

DI void at_store(const f32x16& o, char* ws, int h, int tb, int lane) {
    const int r = lane & 31, hi = lane >> 5;
    const float other = swap32(o[10]);
    const float inv = 1.0f / (hi ? other : o[10]);
    char* of = ws + WS_OF + (size_t)(2 * tb + (r >> 4)) * KS * 1024;
    gstore16p(of + (h >> 1) * 1024 + ((r & 15) + 16 * (2 * (h & 1) + hi)) * 16,
              (u32x4){cvtpk(o[0] * inv, o[1] * inv), cvtpk(o[2] * inv, o[3] * inv), cvtpk(o[4] * inv, o[5] * inv), cvtpk(o[6] * inv, o[7] * inv)});
    if (hi == 0) *(unsigned*)(of + (16 + (h >> 4)) * 1024 + ((r & 15) + 16 * ((h >> 2) & 3)) * 16 + (h & 3) * 4) = cvtpk(o[8] * inv, o[9] * inv);
}

DI float at_qnorm2(bf16x8 q1, bf16x4 q2) {
    const u32x4 a = __builtin_bit_cast(u32x4, q1); const u32x2 b = __builtin_bit_cast(u32x2, q2);
    float n = bf_lo(b[0]) * bf_lo(b[0]) + bf_hi(b[0]) * bf_hi(b[0]);
#pragma unroll
    for (int j = 0; j < 4; ++j) n += bf_lo(a[j]) * bf_lo(a[j]) + bf_hi(a[j]) * bf_hi(a[j]);
    return sum32x(n);
}

DI void attn_block(char* ws, char* lds, int bid) {
    const int tid = threadIdx.x, lane = tid & 63, hi = lane >> 5;
    const int w = __builtin_amdgcn_readfirstlane(tid >> 6);
    const int xcd = bid & 7, idx = bid >> 3;
    const int h = 4 * xcd + (idx & 3), qg = idx >> 2;
    const int tbA = 8 * qg + 2 * w, tbB = tbA + 1;
    const unsigned lds0 = (unsigned)(uintptr_t)lds;
    const char* kvh = ws + WS_KV + (size_t)h * NTB * KVREC;
    MARKS; MARK(0);

    bf16x8 qA1, qB1; unsigned qa2, qb2; float km;
    {
        const char* q1 = ws + WS_Q + ((size_t)h * NTB + tbA) * QREC;
        const char* kx = ws + WS_Q + (size_t)h * NTB * QREC + 1152;
        const unsigned l16 = lane * 16, l4 = (lane & 31) * 4 + 1024, lk = lane * QREC;
        asm volatile("global_load_dword %0, %1, %2" : "=v"(km) : "v"(lk), "s"(kx));
        asm volatile("global_load_dwordx4 %0, %1, %2" : "=v"(qA1) : "v"(l16), "s"(q1));
        asm volatile("global_load_dwordx4 %0, %1, %2 offset:%3" : "=v"(qB1) : "v"(l16), "s"(q1), "n"(QREC));
        asm volatile("global_load_dword %0, %1, %2" : "=v"(qa2) : "v"(l4), "s"(q1));
        asm volatile("global_load_dword %0, %1, %2 offset:%3" : "=v"(qb2) : "v"(l4), "s"(q1), "n"(QREC));
    }
    at_dma(kvh, 0, lds0, w, lane);
    at_dma(kvh, 1, lds0 + AT_STAGE, w, lane);
    asm volatile("s_waitcnt vmcnt(10)" : "+v"(km), "+v"(qA1), "+v"(qB1), "+v"(qa2), "+v"(qb2));
    const bf16x4 qA2 = __builtin_bit_cast(bf16x4, (u32x2){hi ? 0u : qa2, 0u});
    const bf16x4 qB2 = __builtin_bit_cast(bf16x4, (u32x2){hi ? 0u : qb2, 0u});
    km = rowmax16(km);
    km = max32x(max16x(km));
    const float mA = __builtin_amdgcn_sqrtf(at_qnorm2(qA1, qA2) * km) * 1.01f, mB = __builtin_amdgcn_sqrtf(at_qnorm2(qB1, qB2) * km) * 1.01f;
    const bool fast = __all(mA <= AT_BOUND_MAX && mB <= AT_BOUND_MAX);

    const int voffs = 1280 + hi * 320 + min(lane & 31, 19) * 16;
    const lds_cptr L = (lds_cptr)lds;
    f32x16 oA = {}, oB = {};

    if (fast) {
        f32x16 cA, cB;
#pragma unroll
        for (int i = 0; i < 16; ++i) { cA[i] = -mA; cB[i] = -mB; }
        asm volatile("" : "+v"(cA), "+v"(cB));
        f32x16 sA, sB;
        u32x4 pA0 = {}, pA1 = {}, pB0 = {}, pB1 = {};
        float etail[4] = {0.f, 0.f, 0.f, 0.f};
        KFrag k0, k1; VFrag v0, v1;
        MARK(1);
        WAITV_BAR(5);
        MARK(2);
        k0 = at_readk(L, lane);
        v1 = at_readv(L + voffs);
        sA = MFMA16(k0.k1, qA1, cA); sA = MFMA8(k0.k2, qA2, sA);
        asm volatile("s_nop 15" : "+v"(sA));
        int slot = 0;
        for (int st = 0; st < 8; ++st) {
            const lds_cptr cur = L + slot * AT_STAGE;
            const int nslot = slot == 2 ? 0 : slot + 1;
#pragma unroll
            for (int t = 0; t < 8; t += 2) {
                AT_STEP(sA, pA0, pA1, sB, cB, qB1, qB2, k0, oB, pB0, pB1, v1,
                        (k1 = at_readk(cur + (t + 1) * KVREC, lane), v0 = at_readv(cur + t * KVREC + voffs)));
                AT_STEP(sB, pB0, pB1, sA, cA, qA1, qA2, k1, oA, pA0, pA1, v0, (void)0);
                lds_cptr nk = cur + (t + 2) * KVREC;
                if (t == 6) {
                    if (st < 7) {
                        if (st < 6) { WAITV_BAR(0); at_dma(kvh, st + 2, lds0 + (nslot == 2 ? 0 : nslot + 1) * AT_STAGE, w, lane); }
                        else WAITV_BAR(0);
                        nk = L + nslot * AT_STAGE;
                    } else nk = cur;
                }
                AT_STEP(sA, pA0, pA1, sB, cB, qB1, qB2, k1, oB, pB0, pB1, v0,
                        (k0 = at_readk(nk, lane), v1 = at_readv(cur + (t + 1) * KVREC + voffs)));
                AT_STEP(sB, pB0, pB1, sA, cA, qA1, qA2, k0, oA, pA0, pA1, v1, (void)0);
            }
            slot = nslot;
            MARK(3 + st);
        }
        asm volatile("s_nop 1");
        AT_PK2(pB1, 2, etail);
        asm volatile("s_nop 1" : "+v"(pB1));
        AMFMA16_ACC(oB, v1.v0, pB0);
        AMFMA16_ACC(oB, v1.v1, pB1);
        asm volatile("s_nop 15\n\ts_nop 7" : "+v"(oA), "+v"(oB));
    } else {
        float mxA = -INFINITY, mxB = -INFINITY;
        WAITV_BAR(5);
        int slot = 0;
        for (int st = 0; st < 8; ++st) {
            const lds_cptr cur = L + slot * AT_STAGE;
            const int nslot = slot == 2 ? 0 : slot + 1;
            for (int t = 0; t < 8; ++t) {
                const KFrag k = at_readk(cur + t * KVREC, lane);
                const VFrag v = at_readv(cur + t * KVREC + voffs);
                at_online(k, v, qA1, qA2, mxA, oA);
                at_online(k, v, qB1, qB2, mxB, oB);
            }
            if (st < 7) {
                WAITV_BAR(0);
                if (st < 6) at_dma(kvh, st + 2, lds0 + (nslot == 2 ? 0 : nslot + 1) * AT_STAGE, w, lane);
            }
            slot = nslot;
        }
    }
    at_store(oA, ws, h, tbA, lane);
    at_store(oB, ws, h, tbB, lane);
    MARK(11); MARK_DUMP(2, 12);
}

constexpr int DN_STEP = 32 * 288, DN_SLOT = 2 * DN_STEP, DN_LDS = 4 * DN_SLOT, DN_NST = 18;
constexpr int dn_piece(int st, int i) {
    if ((i == 3 || i == 7 || i == 11) && st + 3 < DN_NST) return (st + 3) * 8 + 2 + (i - 3) / 4;
    if ((i == 15 || i == 17) && st + 4 < DN_NST) return (st + 4) * 8 + (i - 15) / 2;
    return -1;
}
constexpr int dn_young(int st) {
    int n = 0; bool seen = st + 1 == 1;
    const int need = st + 1;
    for (int s2 = 0; s2 <= st; ++s2)
        for (int i = 0; i < 18; ++i) {
            if (s2 == st && i >= 15) break;
            if (i == 0 && s2 + 2 < DN_NST) { if (seen) n += 4; if (s2 + 2 == need) { seen = true; n = 0; } }
            const int pc = dn_piece(s2, i);
            if (pc >= 0) { if (seen) n += 1; if ((pc >> 3) == need) { seen = true; n = 0; } }
        }
    return n;
}
DI void dn_ldb(bf16x8 (&b)[2][2], const char* hs, unsigned lo, int st) {
#pragma unroll
    for (int k = 0; k < 2; ++k) {
        asm volatile("global_load_dwordx4 %0, %1, %2 offset:%3" : "=v"(b[0][k]) : "v"(lo), "s"(hs + (size_t)st * 2048), "n"(k * 1024));
        asm volatile("global_load_dwordx4 %0, %1, %2 offset:%3" : "=v"(b[1][k]) : "v"(lo), "s"(hs + (size_t)FS * 1024 + (size_t)st * 2048), "n"(k * 1024));
    }
}
template <int N> DI void dn_wait(bf16x8 (&b)[2][2]) {
    asm volatile("s_waitcnt vmcnt(%4) lgkmcnt(0)\n\ts_barrier" : "+v"(b[0][0]), "+v"(b[0][1]), "+v"(b[1][0]), "+v"(b[1][1]) : "n"(N) : "memory");
}

DI void down_block(const char* wd, char* ws, char* lds, int bid) {
    const int tid = threadIdx.x, lane = tid & 63;
    const int w = __builtin_amdgcn_readfirstlane(tid >> 6);
    const int xcd = bid & 7, idx = bid >> 3;
    const int kq = xcd >> 1, tg = (xcd & 1) * 8 + (idx >> 2), dq = idx & 3;
    const int tb16 = 8 * tg + 2 * w, fs0 = 36 * kq;
    const unsigned lds0 = (unsigned)(uintptr_t)lds;
    unsigned voff[5], doff[5];
#pragma unroll
    for (int m = 0; m < 5; ++m) { const int i = min(5 * (w & 1) + m, 8), n = 64 * i + lane; voff[m] = (n / 18) * (D * 2) + (n % 18) * 16; doff[m] = (w >> 1) * DN_STEP + i * 1024; }
    const char* wsrc = wd + (size_t)(fs0 + (w >> 1)) * 32 * (D * 2) + dq * 288;
    const lds_cptr A = (lds_cptr)lds + (4 * (lane >> 4) + ((lane & 15) >> 2)) * 288 + (lane & 3) * 8;
    const char* hs = ws + WS_HF + ((size_t)tb16 * FS + fs0) * 1024;
    const unsigned lo = lane * 16;
    MARKS; MARK(0);

    auto dma = [&](int st, int m) {
        const char* base = wsrc;
        asm volatile("" : "+s"(base));
        glds16(base + (size_t)st * 2 * 32 * (D * 2), voff[m], lds0 + (st & 3) * DN_SLOT + doff[m]);
    };
#pragma unroll
    for (int st = 0; st < 3; ++st)
#pragma unroll
        for (int m = 0; m < 5; ++m) dma(st, m);
    dma(3, 0); dma(3, 1);
    f32x4 acc[9][2];
    bf16x8 b[3][2][2];
    dn_ldb(b[0], hs, lo, 0);
    dn_ldb(b[1], hs, lo, 1);
    lds_cptr Alo = A, Ahi = A + 2 * DN_SLOT;
    asm volatile("" : "+v"(Alo), "+v"(Ahi));
    auto addr = [&](int st, int i) { return ((st & 2) ? Ahi : Alo) + (st & 1) * DN_SLOT + (i / 9) * DN_STEP + (i % 9) * 32; };
    dn_wait<4>(b[0]);
    bf16x4 flo[4], fhi[4];
#pragma unroll
    for (int i = 0; i < 3; ++i) { flo[i] = trread(addr(0, i)); fhi[i] = trread(addr(0, i) + 16 * 288); }
    static_for<0, DN_NST * 18>([&](auto nc) {
        constexpr int n = decltype(nc)::value, st = n / 18, i = n % 18, set = st % 3;
        if constexpr (i == 0 && st + 2 < DN_NST) dn_ldb(b[(st + 2) % 3], hs, lo, st + 2);
        if constexpr (i == 15 && st + 1 < DN_NST) dn_wait<dn_young(st)>(b[(st + 1) % 3]);
        if constexpr (dn_piece(st, i) >= 0) dma(dn_piece(st, i) >> 3, dn_piece(st, i) & 7);
        constexpr bool more = n + 3 < DN_NST * 18;
        constexpr int nst = (n + 3) / 18, ni = (n + 3) % 18;
        const bf16x8 fr = join(flo[n & 3], fhi[n & 3]);
        if constexpr (n < 9) AMF_NEW(acc[i % 9][0], fr, b[set][0][i / 9]); else AMF_ACC(acc[i % 9][0], fr, b[set][0][i / 9]);
        SBAR();
        if constexpr (more) flo[(n + 3) & 3] = trread(addr(nst, ni));
        SBAR();
        if constexpr (n < 9) AMF_NEW(acc[i % 9][1], fr, b[set][1][i / 9]); else AMF_ACC(acc[i % 9][1], fr, b[set][1][i / 9]);
        SBAR();
        if constexpr (more) fhi[(n + 3) & 3] = trread(addr(nst, ni) + 16 * 288);
        SBAR();
    });
    MARK(1);
    asm volatile("s_nop 7\n\ts_nop 3" : "+v"(acc[7][0]), "+v"(acc[7][1]), "+v"(acc[8][0]), "+v"(acc[8][1]));
    char* dst = ws + WS_PART + (((size_t)kq * NT16 + tb16) * 36 + 9 * dq) * PTILE;
#pragma unroll
    for (int nh = 0; nh < 2; ++nh)
#pragma unroll
        for (int i = 0; i < 9; ++i) gstore12(dst + (size_t)nh * 36 * PTILE + i * PTILE, lane * 12, pack24(acc[i][nh]));
    MARK(2); MARK_DUMP(3, 3);
}

constexpr int MIX_LDS = DN_LDS;
static_assert(MIX_LDS >= 3 * AT_STAGE && 2 * MIX_LDS <= 160 * 1024, "two workgroups per CU");
__global__ __launch_bounds__(256, 2) void k_mix(const char* wd, char* ws) {
    extern __shared__ __attribute__((aligned(16))) char lds[];
    STAMP_BEGIN;
    const int b = blockIdx.x;
    if (b < 256) attn_block(ws, lds, b); else down_block(wd, ws, lds, b - 256);
    STAMP_END(5);
}

constexpr int FN_A = 3 * KS * 1024, FN_LDS = FN_A + 8 * 3 * 1024;
DI void ldx1(bf16x8& d, const char* base, unsigned lo, int off) {
    asm volatile("global_load_dwordx4 %0, %1, %2 offset:%3" : "=v"(d) : "v"(lo), "s"(base), "n"(off));
}
template <int N> DI void waitx(bf16x8& a, bf16x8& b) { asm volatile("s_waitcnt vmcnt(%2)" : "+v"(a), "+v"(b) : "n"(N)); }
DI void fn_ldpx(u32x3 (&pq)[3][4], u32x2 (&xv)[3], const char* pp, unsigned l12, const char* xp, unsigned xo) {
#pragma unroll
    for (int t = 0; t < 3; ++t) {
#pragma unroll
        for (int k = 0; k < 4; ++k) asm volatile("global_load_dwordx3 %0, %1, %2 offset:%3" : "=v"(pq[t][k]) : "v"(l12), "s"(pp + (size_t)k * NT16 * 36 * PTILE), "n"(t * PTILE));
        asm volatile("global_load_dwordx2 %0, %1, %2 offset:%3" : "=v"(xv[t]) : "v"(xo), "s"(xp), "n"(t * 32));
    }
}
__global__ __launch_bounds__(512, 1) void k_final(const unsigned short* x, char* ws, unsigned short* out) {
    extern __shared__ __attribute__((aligned(16))) char lds[];
    STAMP_BEGIN;
    const int tid = threadIdx.x, lane = tid & 63;
    const int w = __builtin_amdgcn_readfirstlane(tid >> 6);
    const int xcd = blockIdx.x & 7, idx = blockIdx.x >> 3;
    const int tg = 2 * xcd + idx / 12, dg = idx % 12, kh = w >> 2, tb16 = 8 * tg + 2 * (w & 3);
    const unsigned lds0 = (unsigned)(uintptr_t)lds, lo = lane * 16;
    MARKS; MARK(0);
    {
        const char* src = ws + WS_WOP + (size_t)dg * FN_A;
#pragma unroll
        for (int i = 0; i < 7; ++i) { const int n = min(w * 7 + i, 3 * KS - 1); glds16(src + n * 1024, lo, lds0 + n * 1024); }
    }
    const char* os = ws + WS_OF + ((size_t)tb16 * KS + 9 * kh) * 1024;
    bf16x8 b[2][9];
    auto ldo = [&](int s) { ldx1(b[0][s], os + (s >> 2) * 4096, lo, (s & 3) * 1024); ldx1(b[1][s], os + KS * 1024 + (s >> 2) * 4096, lo, (s & 3) * 1024); };
#pragma unroll
    for (int s = 0; s < 4; ++s) ldo(s);
    MARK(1);
    asm volatile("s_waitcnt vmcnt(8)\n\ts_barrier" ::: "memory");
    MARK(2);
    const int tok = (tb16 + kh) * 16 + (lane & 15);
    const char* xp = (const char*)(x + (size_t)(tb16 + kh) * 16 * D + dg * 48);
    const unsigned xo2 = ((lane & 15) * D + 4 * (lane >> 4)) * 2;
    u32x2 xv[3];
    u32x3 pq[3][4];
    const char* pp = ws + WS_PART + ((size_t)(tb16 + kh) * 36 + 3 * dg) * PTILE;
    const lds_cptr A = (lds_cptr)lds + lo + 9 * kh * 1024;
    f32x4 acc[2][3] = {};
    static_for<0, 9>([&](auto sc) {
        constexpr int s = decltype(sc)::value;
        bf16x8 a[3];
#pragma unroll
        for (int t = 0; t < 3; ++t) a[t] = *(const __attribute__((address_space(3))) bf16x8*)(A + (t * KS + s) * 1024);
        if constexpr (s + 4 < 9) ldo(s + 4);
        if constexpr (s == 4) fn_ldpx(pq, xv, pp, lane * 12, xp, xo2);
        waitx<2 * ((s + 4 < 9 ? s + 4 : 8) - s) + (s >= 4 ? 15 : 0)>(b[0][s], b[1][s]);
#pragma unroll
        for (int t = 0; t < 3; ++t) {
            acc[0][t] = MFMA32K(a[t], b[0][s], acc[0][t]);
            acc[1][t] = MFMA32K(a[t], b[1][s], acc[1][t]);
        }
        SBAR();
    });
    MARK(3);
    typedef __attribute__((address_space(3))) f32x4* l128;
    const lds_ptr xo = (lds_ptr)lds + FN_A + lo;
#pragma unroll
    for (int t = 0; t < 3; ++t) *(l128)(xo + (w * 3 + t) * 1024) = kh ? acc[0][t] : acc[1][t];
    asm volatile("s_waitcnt lgkmcnt(0)\n\ts_barrier" ::: "memory");
    f32x4 sum[3];
#pragma unroll
    for (int t = 0; t < 3; ++t) {
        const f32x4 other = *(l128)(xo + ((w ^ 4) * 3 + t) * 1024);
        sum[t] = kh ? other + acc[1][t] : acc[0][t] + other;
    }
    asm volatile("s_waitcnt vmcnt(0)" : "+v"(xv[0]), "+v"(xv[1]), "+v"(xv[2]));
#pragma unroll
    for (int t = 0; t < 3; ++t) {
        asm volatile("" : "+v"(pq[t][0]), "+v"(pq[t][1]), "+v"(pq[t][2]), "+v"(pq[t][3]));
        const f32x4 ffn = (unpack24(pq[t][0]) + unpack24(pq[t][1])) + (unpack24(pq[t][2]) + unpack24(pq[t][3]));
        float o[4];
#pragma unroll
        for (int c = 0; c < 4; ++c) {
            const float f = rbf(ffn[c]);
            const float g = rbf(f * __builtin_amdgcn_rcpf(1.0f + __builtin_amdgcn_exp2f(f * -LOG2E)));
            const float xx = (c & 1) ? bf_hi(xv[t][c >> 1]) : bf_lo(xv[t][c >> 1]);
            o[c] = xx + rbf(rbf(sum[t][c]) * g);
        }
        *(u32x2*)(out + (size_t)tok * D + dg * 48 + 16 * t + 4 * (lane >> 4)) = (u32x2){cvtpk(o[0], o[1]), cvtpk(o[2], o[3])};
    }
    MARK(4);
    MARK(5); MARK_DUMP(4, 6);
    STAMP_END(4);
}

extern "C" void kernel_launch(void* const* d_in, const int* in_sizes, int n_in, void* d_out, int out_size, void* d_ws, size_t ws_size, hipStream_t stream) {
    const unsigned short* x = (const unsigned short*)d_in[0];
    const unsigned short* wqkv = (const unsigned short*)d_in[1];
    const unsigned short* wo = (const unsigned short*)d_in[2];
    const char* wg = (const char*)d_in[4];
    const char* wu = (const char*)d_in[5];
    const char* wd = (const char*)d_in[6];
    char* ws = (char*)d_ws;
    (void)hipFuncSetAttribute((const void*)k_gemm1, hipFuncAttributeMaxDynamicSharedMemorySize, 4 * G1_SLOT);
    (void)hipFuncSetAttribute((const void*)k_mix, hipFuncAttributeMaxDynamicSharedMemorySize, MIX_LDS);
    (void)hipFuncSetAttribute((const void*)k_final, hipFuncAttributeMaxDynamicSharedMemorySize, FN_LDS);
    k_prep<<<256, 1024, 0, stream>>>(x, wqkv, wo, ws);
    k_gemm1<<<256, 512, 4 * G1_SLOT, stream>>>(wg, wu, ws);
    k_mix<<<512, 256, MIX_LDS, stream>>>(wd, ws);
    k_final<<<192, 512, FN_LDS, stream>>>(x, ws, (unsigned short*)d_out);
}
```

```cpp
#define DIAG 0
#define EXTRA 0
#include <hip/hip_runtime.h>
#include <hip/hip_bf16.h>
#include <stdint.h>
#include <type_traits>

typedef __attribute__((ext_vector_type(8))) short bf16x8;
typedef __attribute__((ext_vector_type(4))) short bf16x4;
typedef __attribute__((ext_vector_type(2))) __bf16 bf16p;
typedef __attribute__((ext_vector_type(2))) float f32x2;
typedef __attribute__((ext_vector_type(4))) float f32x4;
typedef __attribute__((ext_vector_type(16))) float f32x16;
typedef __attribute__((ext_vector_type(2))) unsigned u32x2;
typedef __attribute__((ext_vector_type(3))) unsigned u32x3;
typedef __attribute__((ext_vector_type(4))) unsigned u32x4;
typedef __attribute__((address_space(3))) const char* lds_cptr;
typedef __attribute__((address_space(3))) char* lds_ptr;
typedef short v4i16_t __attribute__((ext_vector_type(4)));

#define DI __device__ __forceinline__
#define MFMA16(a, b, c) __builtin_amdgcn_mfma_f32_32x32x16_bf16((a), (b), (c), 0, 0, 0)
#define MFMA32K(a, b, c) __builtin_amdgcn_mfma_f32_16x16x32_bf16((a), (b), (c), 0, 0, 0)
#define AMF_ACC(c, a, b) asm volatile("v_mfma_f32_16x16x32_bf16 %0, %1, %2, %0" : "+v"(c) : "v"(a), "v"(b))
#define AMF_NEW(c, a, b) asm volatile("v_mfma_f32_16x16x32_bf16 %0, %1, %2, 0" : "=&v"(c) : "v"(a), "v"(b))
#define MFMA8(a, b, c) __builtin_amdgcn_mfma_f32_32x32x8bf16_1k((a), (b), (c), 0, 0, 0)
#define SBAR() __builtin_amdgcn_sched_barrier(0)

template <int I, int N, class Fn> DI void static_for(Fn&& fn) {
    if constexpr (I < N) { fn(std::integral_constant<int, I>{}); static_for<I + 1, N>(fn); }
}

constexpr int S = 2048, D = 576, NH = 32, DH = 18, F = 4608;
constexpr int NTB = S / 32;
constexpr int NT16 = S / 16;
constexpr int KS = D / 32;
constexpr int FS = F / 32;

constexpr int KVREC = 2560;
constexpr size_t WS_XNF = 0;
constexpr size_t WS_WQP = WS_XNF + (size_t)NT16 * KS * 1024;
constexpr int QREC = 1280;
constexpr size_t WS_WOP = WS_WQP + (size_t)NH * KS * 4096;
constexpr size_t WS_Q = WS_WOP + (size_t)36 * KS * 1024;
constexpr size_t WS_KV = WS_Q + (size_t)NH * NTB * QREC;
constexpr size_t WS_OF = WS_KV + (size_t)NH * NTB * KVREC;
constexpr size_t WS_HF = WS_OF + (size_t)NT16 * KS * 1024;
constexpr int PTILE = 768;
constexpr size_t WS_PART = WS_HF + (size_t)NT16 * FS * 1024;
constexpr size_t WS_END = WS_PART + (size_t)4 * NT16 * 36 * PTILE;

#define STAMP_BEGIN
#define STAMP_END(k)
#define MARKS
#define MARK(i)
#define MARK_DUMP(k, n)

constexpr size_t WS_DIAG = 190u << 20;
DI void diag_stamp(char* ws, int k) {
    if (blockIdx.x == 0 && threadIdx.x == 0) { const unsigned long long t = __builtin_amdgcn_s_memrealtime(); __hip_atomic_store((unsigned long long*)(ws + WS_DIAG) + k, t, __ATOMIC_RELAXED, __HIP_MEMORY_SCOPE_AGENT); }
}
constexpr float LOG2E = 1.4426950408889634f;
constexpr float QSCALE = 0.23570226039551584f * LOG2E;

DI unsigned cvtpk(float lo, float hi) { return __builtin_bit_cast(unsigned, __builtin_convertvector((f32x2){lo, hi}, bf16p)); }
DI float bf_lo(unsigned u) { return __uint_as_float(u << 16); }
DI float bf_hi(unsigned u) { return __uint_as_float(u & 0xffff0000u); }
DI float rbf(float x) { return bf_lo(cvtpk(x, x)); }
DI bf16x4 trread(lds_cptr p) { return __builtin_bit_cast(bf16x4, __builtin_amdgcn_ds_read_tr16_b64_v4i16((__attribute__((address_space(3))) v4i16_t*)p)); }
DI bf16x8 trfrag(lds_cptr p0, lds_cptr p1) { const bf16x4 a = trread(p0), b = trread(p1); return (bf16x8){a[0], a[1], a[2], a[3], b[0], b[1], b[2], b[3]}; }
DI bf16x8 join(bf16x4 a, bf16x4 b) { return (bf16x8){a[0], a[1], a[2], a[3], b[0], b[1], b[2], b[3]}; }
DI float sum16x(float x) { auto r = __builtin_amdgcn_permlane16_swap(__float_as_uint(x), __float_as_uint(x), false, false); return __uint_as_float(r[0]) + __uint_as_float(r[1]); }
DI float sum32x(float x) { auto r = __builtin_amdgcn_permlane32_swap(__float_as_uint(x), __float_as_uint(x), false, false); return __uint_as_float(r[0]) + __uint_as_float(r[1]); }
DI float max16x(float x) { auto r = __builtin_amdgcn_permlane16_swap(__float_as_uint(x), __float_as_uint(x), false, false); return fmaxf(__uint_as_float(r[0]), __uint_as_float(r[1])); }
DI float max32x(float x) { auto r = __builtin_amdgcn_permlane32_swap(__float_as_uint(x), __float_as_uint(x), false, false); return fmaxf(__uint_as_float(r[0]), __uint_as_float(r[1])); }
template <int CTRL> DI float dppf(float x) { return __uint_as_float(__builtin_amdgcn_update_dpp(0u, __float_as_uint(x), CTRL, 0xf, 0xf, true)); }
DI float rowmax16(float x) {
    x = fmaxf(x, dppf<0xB1>(x));
    x = fmaxf(x, dppf<0x4E>(x));
    x = fmaxf(x, dppf<0x141>(x));
    return fmaxf(x, dppf<0x140>(x));
}
DI float swap32(float x) { auto r = __builtin_amdgcn_permlane32_swap(__float_as_uint(x), __float_as_uint(x), false, false); return __uint_as_float((threadIdx.x & 32) ? r[0] : r[1]); }

DI void glds16(const void* sbase, unsigned voff, unsigned lds_base) {
    asm volatile("s_mov_b32 m0, %2\n\ts_nop 0\n\tglobal_load_lds_dwordx4 %0, %1" :: "v"(voff), "s"(sbase), "s"(lds_base) : "memory");
}
#define STPOL "sc1"
DI void gstore16(void* sbase, unsigned voff, u32x4 v) { asm volatile("global_store_dwordx4 %0, %1, %2 " STPOL "\n\ts_nop 1" :: "v"(voff), "v"(v), "s"(sbase) : "memory"); }
DI void gstore8(void* sbase, unsigned voff, u32x2 v) { asm volatile("global_store_dwordx2 %0, %1, %2" :: "v"(voff), "v"(v), "s"(sbase) : "memory"); }
DI u32x3 pack24(f32x4 v) {
    const unsigned a = __float_as_uint(v[0]), b = __float_as_uint(v[1]), c = __float_as_uint(v[2]), d = __float_as_uint(v[3]);
    return (u32x3){__builtin_amdgcn_perm(a, d, 0x07060501u), __builtin_amdgcn_perm(b, d, 0x07060502u), __builtin_amdgcn_perm(c, d, 0x07060503u)};
}
DI f32x4 unpack24(u32x3 p) {
    const unsigned t = __builtin_amdgcn_perm(p[2], p[1], 0x04000c0cu);
    const unsigned d = __builtin_amdgcn_perm(t, p[0], 0x0706000cu);
    return (f32x4){__uint_as_float(p[0] & 0xffffff00u), __uint_as_float(p[1] & 0xffffff00u), __uint_as_float(p[2] & 0xffffff00u), __uint_as_float(d)};
}
DI void gstore12(void* sbase, unsigned voff, u32x3 v) { asm volatile("global_store_dwordx3 %0, %1, %2 " STPOL "\n\ts_nop 1" :: "v"(voff), "v"(v), "s"(sbase) : "memory"); }
DI void gstore16p(void* p, u32x4 v) { asm volatile("global_store_dwordx4 %0, %1, off " STPOL "\n\ts_nop 1" :: "v"(p), "v"(v) : "memory"); }
#define WAITV_BAR(n) asm volatile("s_waitcnt vmcnt(" #n ") lgkmcnt(0)\n\ts_barrier" ::: "memory")

DI int wo_row(int ks, int kg, int j) {
    return ks < 16 ? DH * (2 * ks + (kg >> 1)) + 8 * (j >> 2) + 4 * (kg & 1) + (j & 3) : DH * (16 * (ks - 16) + 4 * kg + (j >> 1)) + 16 + (j & 1);
}
DI float sum32(float x) {
    x += dppf<0xB1>(x);
    x += dppf<0x4E>(x);
    x += dppf<0x141>(x);
    x += dppf<0x140>(x);
    return sum16x(x);
}

__global__ __launch_bounds__(1024) void k_prep(const unsigned short* __restrict__ x, const unsigned short* __restrict__ wqkv, const unsigned short* __restrict__ wo, char* __restrict__ ws) {
    STAMP_BEGIN;
    __shared__ __attribute__((aligned(16))) char stage[KS * 512];
    diag_stamp(ws, 0);
    const unsigned long long tk0 = __builtin_amdgcn_s_memrealtime();
    const int tid = threadIdx.x;
    const int wv = __builtin_amdgcn_readfirstlane(tid >> 6);
    if (wv < 4) {
        const int row = blockIdx.x * 8 + (tid >> 5), t = tid & 31;
        const u32x4* xr = (const u32x4*)(x + (size_t)row * D);
        u32x4 v[3];
        v[0] = xr[t]; v[1] = xr[t + 32]; v[2] = (t < 8) ? xr[t + 64] : (u32x4){0u, 0u, 0u, 0u};
        float sum = 0.f;
#pragma unroll
        for (int i = 0; i < 3; ++i)
#pragma unroll
            for (int j = 0; j < 4; ++j) sum += bf_lo(v[i][j]) + bf_hi(v[i][j]);
        const float mean = sum32(sum) * (1.0f / D);
        float sq = 0.f;
#pragma unroll
        for (int i = 0; i < 3; ++i)
            if (i < 2 || t < 8)
#pragma unroll
                for (int j = 0; j < 4; ++j) { const float a = bf_lo(v[i][j]) - mean, b = bf_hi(v[i][j]) - mean; sq += a * a + b * b; }
        const float mb = rbf(mean);
        const float den = rbf(__fsqrt_rn(rbf(rbf(sum32(sq) * (1.0f / D)) + 1e-5f)));
        const float rc = __builtin_amdgcn_rcpf(den);
        auto quot = [&](float a) { const float q = a * rc; return __builtin_fmaf(__builtin_fmaf(-q, den, a), rc, q); };
#pragma unroll
        for (int i = 0; i < 3; ++i) {
            if (i < 2 || t < 8) {
                const int p = t + 32 * i;
                unsigned o[4];
#pragma unroll
                for (int j = 0; j < 4; ++j) o[j] = cvtpk(quot(rbf(bf_lo(v[i][j]) - mb)), quot(rbf(bf_hi(v[i][j]) - mb)));
                char* d = stage + (p >> 2) * 512 + (p & 1) * 256 + (tid >> 5) * 16 + ((p >> 1) & 1) * 8;
                *(u32x2*)d = (u32x2){o[0], o[1]};
                *(u32x2*)(d + 128) = (u32x2){o[2], o[3]};
            }
        }
        asm volatile("s_waitcnt lgkmcnt(0)\n\ts_barrier" ::: "memory");
        char* dst = ws + WS_XNF + (size_t)(blockIdx.x >> 1) * KS * 1024 + (blockIdx.x & 1) * 128;
#pragma unroll
        for (int i = 0; i < 3; ++i) {
            const int n = tid + 256 * i;
            if (n < KS * 32) gstore16p(dst + (n >> 3) * 256 + (n & 7) * 16, *(const u32x4*)(stage + n * 16));
        }
    } else if (wv < 13) {
        const int n = blockIdx.x * 576 + (tid - 256);
        const int pc = n & 3, r = (n >> 2) & 31, sel = (n >> 7) & 1, hs = n >> 8, s = hs % KS, h = hs / KS;
        const int c = pc ^ (2 * ((r >> 2) & 1)), t = c >> 1, g = c & 1;
        const unsigned* w = (const unsigned*)(wqkv + (size_t)(32 * s + r) * (3 * D) + DH * h);
        u32x4 o = {0u, 0u, 0u, 0u};
        if (sel == 0 || t == 0) {
            const int col = (sel == 0 ? t * D : 2 * D) + 8 * g;
            o = (u32x4){w[col / 2], w[col / 2 + 1], w[col / 2 + 2], w[col / 2 + 3]};
        } else if (g == 0) {
            o = (u32x4){w[8], w[D / 2 + 8], w[D + 8], 0u};
        }
        gstore16p(ws + WS_WQP + (size_t)n * 16, o);
    } else if (tid < 256 + 576 + 162) {
        const int n = blockIdx.x * 162 + (tid - (256 + 576));
        const int lane = n & 63, fs = n >> 6, ks = fs % KS, dt = fs / KS;
        unsigned short v[8];
#pragma unroll
        for (int j = 0; j < 8; ++j) v[j] = wo[(size_t)wo_row(ks, lane >> 4, j) * D + 16 * dt + (lane & 15)];
        gstore16p(ws + WS_WOP + (size_t)n * 16, (u32x4){v[0] | ((unsigned)v[1] << 16), v[2] | ((unsigned)v[3] << 16), v[4] | ((unsigned)v[5] << 16), v[6] | ((unsigned)v[7] << 16)});
    }
    if (EXTRA > 0 && blockIdx.x == 0 && threadIdx.x == 0) { while (__builtin_amdgcn_s_memrealtime() - tk0 < EXTRA) __builtin_amdgcn_s_sleep(2); }
    STAMP_END(0);
}

#define G1_MAP 1
constexpr int G1_SLOT = 9 * 4096;

DI int img_aoff(int lane) {
    const int kg = lane >> 4, q = (lane & 15) >> 2, p = lane & 3;
    return (4 * kg + q) * 64 + (((p >> 1) ^ (2 * (kg & 1))) * 16) + 8 * (p & 1);
}
DI void g1_ldx(bf16x8& d, const char* base, unsigned lo, int ks) {
    asm volatile("global_load_dwordx4 %0, %1, %2 offset:%3" : "=v"(d) : "v"(lo), "s"(base + (ks >> 2) * 4096), "n"((ks & 3) * 1024));
}
DI float glu(float g, float u) { return u * __builtin_amdgcn_rcpf(1.0f + __builtin_amdgcn_exp2f(g * -LOG2E)); }

constexpr int g1_group(int u) { return u < 4 ? (u & 1) : u / 2; }
constexpr int g1_half(int u) { return u < 4 ? (u >> 1) : (u & 1); }
constexpr int g1_dma_at(int u, int g) {
    if (u == 0) return (g >= 12 && g <= 20 && !(g & 1)) ? 1 * 8 + (g - 12) / 2 : (g >= 22 && g <= 30 && !(g & 1)) ? 2 * 8 + (g - 22) / 2 : -1;
    if (u == 1) return -1;
    if (u == 2) return (g <= 16 && !(g & 3)) ? 3 * 8 + g / 4 : (g >= 20 && g <= 28 && !(g & 1)) ? 4 * 8 + (g - 20) / 2 : -1;
    if (u <= 9) { const int sp = 5; return (g % sp == 0 && g / sp < 5) ? (u + 2) * 8 + g / sp : -1; }
    return -1;
}
constexpr int g1_x_at(int u, int g) {
    if (g & 3) return -1;
    if (u == 0) return g / 4 < 3 ? 6 + g / 4 : -1;
    if (u == 1) return 9 + g / 4;
    return -1;
}
constexpr int g1_x_younger(int ks, int u, int g) {
    int n = 0; bool seen = ks < 6;
    if (ks < 6) n += 2 * (5 - ks);
    for (int uu = 0; uu <= u; ++uu)
        for (int gg = 0; gg < 36 && (uu < u || gg <= g); ++gg) {
            const int x = g1_x_at(uu, gg);
            if (x >= 0) { if (seen) n += 2; if (x == ks) seen = true; }
            if (g1_dma_at(uu, gg) >= 0 && seen) n += 1;
        }
    return n;
}

struct G1Ctx {
    const char *wrow, *wq, *xs;
    char* hdst;
    unsigned lds0, dsto, voff, lo;
    int fbase, fsingle, w;
};
DI void g1_piece(const G1Ctx& c, int unit, int m) {
    const int grp = g1_group(unit), hk = g1_half(unit);
    const unsigned slot = c.lds0 + (unit & 3) * G1_SLOT;
    const int mm = (m == 4 && c.w >= 4) ? 3 : m;
    if (grp == 0) {
        const int pi = c.w + 8 * mm;
        glds16(c.wq + hk * G1_SLOT + pi * 1024, c.lo, slot + pi * 1024);
    } else {
        const int f0 = grp == 5 ? c.fsingle : c.fbase + 32 * (grp - 1);
        glds16(c.wrow + (size_t)(288 * hk + 64 * mm) * (F * 2) + f0 * 2, c.voff, slot + c.dsto + mm * 8192);
    }
}

struct G1Epi { float e[2], h[2]; u32x4 pk; };
DI void g1_epi_slice(int s, const f32x4 (&c)[2][2][2], G1Epi& t, char* hdst, unsigned lo) {
    const int v = s >> 2, nh = v >> 3, j = v & 7, ph = s & 3;
    if (ph == 0) { t.e[v & 1] = __builtin_amdgcn_exp2f(c[0][j >> 2][nh][j & 3] * -LOG2E); asm volatile("" : "+v"(t.e[v & 1])); }
    if (ph == 1) { t.e[v & 1] = __builtin_amdgcn_rcpf(1.0f + t.e[v & 1]); asm volatile("" : "+v"(t.e[v & 1])); }
    if (ph == 2) { t.h[v & 1] = c[1][j >> 2][nh][j & 3] * t.e[v & 1]; asm volatile("" : "+v"(t.h[v & 1])); }
    if (ph == 3 && (v & 1)) {
        t.pk[j >> 1] = cvtpk(t.h[0], t.h[1]);
        asm volatile("" : "+v"(t.pk[j >> 1]));
        if (j == 7) gstore16(hdst + (size_t)nh * FS * 1024, lo, t.pk);
    }
}

struct QkvEpi { char *qg, *kg; lds_ptr stg; float kmax; };
DI void qkv_slice(int k, const f32x4 (&c)[2][2][2], QkvEpi& e, unsigned lo) {
    typedef __attribute__((address_space(3))) u32x2* l64;
    typedef __attribute__((address_space(3))) unsigned* l32;
    typedef __attribute__((address_space(3))) unsigned short* l16;
    typedef __attribute__((address_space(3))) const u32x4* l128;
    asm volatile("" : "+v"(lo));
    const int lane = lo >> 4;
    const int t = lane & 15, g4 = lane >> 4;
    const lds_ptr rec = e.stg + QREC;
    if (k < 4) {
        const int nh = k >> 1, r = 16 * nh + t;
        const f32x4 ex = c[1][1][nh];
        if ((k & 1) == 0) {
            const unsigned fo = (r + 32 * (g4 >> 1)) * 16 + (g4 & 1) * 8;
            const f32x4 q = c[0][0][nh], kk = c[0][1][nh];
            *(l64)(e.stg + fo) = (u32x2){cvtpk(q[0] * QSCALE, q[1] * QSCALE), cvtpk(q[2] * QSCALE, q[3] * QSCALE)};
            *(l64)(rec + fo) = (u32x2){cvtpk(kk[0], kk[1]), cvtpk(kk[2], kk[3])};
            float n2 = g4 == 0 ? ex[2] * ex[2] + ex[3] * ex[3] : 0.f;
            n2 += (kk[0] * kk[0] + kk[1] * kk[1]) + (kk[2] * kk[2] + kk[3] * kk[3]);
            n2 = sum32x(sum16x(n2));
            e.kmax = nh ? fmaxf(e.kmax, n2) : n2;
            asm volatile("" : "+v"(e.kmax));
        } else {
            const f32x4 v = c[1][0][nh];
            *(l64)(rec + 1280 + nh * 640 + (g4 & 1) * 320 + t * 16 + (g4 >> 1) * 8) = (u32x2){cvtpk(v[0], v[1]), cvtpk(v[2], v[3])};
            const unsigned e01 = cvtpk(ex[0], ex[1]), e23 = cvtpk(ex[2], ex[3]);
            const bool g0 = g4 == 0, g1 = g4 == 1;
            const lds_ptr spare = e.stg + 1168;
            const lds_ptr vt = rec + 1280 + nh * 640 + ((t >> 2) & 1) * 320 + (4 * (t >> 3) + (t & 3)) * 2;
            *(l64)(g0 ? rec + 1024 + r * 8 : spare + 8) = (u32x2){e23, 0u};
            *(l32)(g0 ? e.stg + 1024 + r * 4 : spare + 4) = cvtpk(ex[0] * QSCALE, ex[1] * QSCALE);
            *(l16)(g0 ? spare : vt + (g1 ? 16 : 16 + g4) * 16) = (unsigned short)(g1 ? e01 : (g4 == 2 ? 0x3f80u : 0u));
            *(l16)(g1 ? vt + 17 * 16 : spare + 2) = (unsigned short)(e01 >> 16);
        }
    } else if (k == 4) {
        const float m = rowmax16(e.kmax);
        if (lane == 0) *(l32)(e.stg + 1152) = __float_as_uint(m);
    } else if (k == 5) {
        const unsigned o0 = lane * 16, oq = min(lane, 8) * 16 + 1024;
        const u32x4 d0 = *(l128)(e.stg + o0), d1 = *(l128)(e.stg + oq);
        gstore16(e.qg, o0, d0);
        gstore16(e.qg, oq, d1);
    } else {
        const unsigned o0 = lane * 16, ok = min(lane, 31) * 16 + 2048;
        const u32x4 d2 = *(l128)(rec + o0), d3 = *(l128)(rec + 1024 + o0), d4 = *(l128)(rec + ok);
        gstore16(e.kg, o0, d2);
        gstore16(e.kg, o0 + 1024, d3);
        gstore16(e.kg, ok, d4);
    }
}

struct G1Ring { bf16x4 lo[3], hi[3]; };
constexpr int g1_meet(int u) { return u == 1 ? 18 : u == 3 ? 10 : u == 10 ? 0 : 5; }
template <int U>
DI void g1_unit(const G1Ctx& cx, lds_cptr& b0, lds_cptr& b1, bool odd, G1Ring& fr, bf16x8 (&xb)[2][KS], f32x4 (&c)[2][2][2],
                const f32x4 (&cprev)[2][2][2], QkvEpi& qe) {
    constexpr int GRP = g1_group(U), HK = g1_half(U);
    constexpr bool PAIR = GRP != 5, NPAIR = g1_group(U + 1) != 5;
    constexpr int PER = PAIR ? 4 : 2, N = 9 * PER;
    constexpr bool EPI = U >= 4;
    auto addr = [&](int i) {
        const bool nx = i >= N, pr = nx ? NPAIR : PAIR;
        const int j = nx ? i - N : i, ks = pr ? j >> 2 : j >> 1, sel = pr ? (j >> 1) & 1 : j & 1, t = pr ? j & 1 : 0;
        return (t ? b1 : b0) + ks * 4096 + sel * 2048;
    };
    G1Epi ep;
    char* hprev = cx.hdst + (U / 2 - 2) * 1024;
    static_for<0, N>([&](auto ic) {
        constexpr int i = decltype(ic)::value;
        constexpr int kl = i / PER, ks = 9 * HK + kl, sel = PAIR ? (i >> 1) & 1 : i & 1, t = PAIR ? i & 1 : 0;
        constexpr int g = PAIR ? i : 2 * i;
        constexpr int x = g1_x_at(U, g), dm = g1_dma_at(U, g);
        constexpr bool more = U < 11 || i + 2 < N;
        if constexpr (i == N - 2 && U < 11) {
            asm volatile("s_waitcnt vmcnt(%0) lgkmcnt(0)\n\ts_barrier" :: "n"(g1_meet(U)) : "memory");
            constexpr int delta = (((U + 1) & 3) - (U & 3)) * G1_SLOT;
            if constexpr (U == 9) b0 = odd ? b1 : b0;
            b0 += delta; b1 += delta;
            asm volatile("" : "+v"(b0), "+v"(b1));
        }
        if constexpr (x >= 0) { g1_ldx(xb[0][x], cx.xs, cx.lo, x); g1_ldx(xb[1][x], cx.xs + KS * 1024, cx.lo, x); }
        if constexpr (dm >= 0) g1_piece(cx, dm >> 3, dm & 7);
        if constexpr ((U == 0 || U == 2) && (i & 3) == 0) {
            constexpr int young = g1_x_younger(ks, U, g);
            asm volatile("s_waitcnt vmcnt(%2)" : "+v"(xb[0][ks]), "+v"(xb[1][ks]) : "n"(young));
        }
        constexpr bool SWAP = GRP == 0 && sel == 1 && t == 0;
        const bf16x8 a = join(fr.lo[i % 3], fr.hi[i % 3]);
        if constexpr (ks == 0) { if constexpr (SWAP) AMF_NEW(c[sel][t][0], xb[0][ks], a); else AMF_NEW(c[sel][t][0], a, xb[0][ks]); }
        else { if constexpr (SWAP) AMF_ACC(c[sel][t][0], xb[0][ks], a); else AMF_ACC(c[sel][t][0], a, xb[0][ks]); }
        SBAR();
        if constexpr (more) fr.lo[(i + 2) % 3] = trread(addr(i + 2));
        SBAR();
        if constexpr (ks == 0) { if constexpr (SWAP) AMF_NEW(c[sel][t][1], xb[1][ks], a); else AMF_NEW(c[sel][t][1], a, xb[1][ks]); }
        else { if constexpr (SWAP) AMF_ACC(c[sel][t][1], xb[1][ks], a); else AMF_ACC(c[sel][t][1], a, xb[1][ks]); }
        SBAR();
        if constexpr (more) fr.hi[(i + 2) % 3] = trread(addr(i + 2) + 1024);
        SBAR();
        if constexpr (U == 3 && (i & 3) == 2 && i / 4 < 7) qkv_slice(i / 4, cprev, qe, cx.lo);
        if constexpr (EPI && PAIR) {
            constexpr int sl = 36 * HK + i - 4;
            if constexpr (sl >= 0 && sl < 64) g1_epi_slice(sl, cprev, ep, hprev, cx.lo);
        } else if constexpr (EPI) {
            constexpr int sl = 2 * (18 * HK + i - 2);
            if constexpr (sl >= 0 && sl < 64) { g1_epi_slice(sl, cprev, ep, hprev, cx.lo); g1_epi_slice(sl + 1, cprev, ep, hprev, cx.lo); }
        }
        SBAR();
    });
}

__global__ __launch_bounds__(512, 2) void k_gemm1(const char* wg, const char* wu, char* ws) {
    extern __shared__ __attribute__((aligned(16))) char lds[];
    STAMP_BEGIN; diag_stamp(ws, 1);
    const int tid = threadIdx.x, lane = tid & 63;
    const int w = __builtin_amdgcn_readfirstlane(tid >> 6);
    const int xcd = blockIdx.x & 7, idx = blockIdx.x >> 3;
    const int cg = 8 * (xcd >> 1) + (idx & 7), rg = 4 * (xcd & 1) + (idx >> 3), tb = 8 * rg + w;
    MARKS; MARK(0);

    const int odd = cg & 1;
    G1Ctx cx;
    cx.w = w; cx.lds0 = (unsigned)(uintptr_t)lds; cx.lo = lane * 16;
    cx.fbase = 144 * cg + 16 * odd; cx.fsingle = odd ? 144 * cg - 16 : 144 * cg + 128;
    cx.wq = ws + WS_WQP + (size_t)cg * KS * 4096;
    cx.xs = ws + WS_XNF + (size_t)(2 * tb) * KS * 1024;
    cx.wrow = ((w & 1) ? wu : wg) + (size_t)(32 * (w >> 2) + 16 * ((w >> 1) & 1)) * (F * 2);
    cx.dsto = (w >> 2) * 4096 + (w & 1) * 2048 + ((w >> 1) & 1) * 1024;
    cx.voff = (lane >> 2) * (F * 2) + (((lane & 3) ^ (2 * ((lane >> 4) & 1))) * 16);
    cx.hdst = ws + WS_HF + ((size_t)(2 * tb) * FS + cx.fbase / 32) * 1024;

#pragma unroll
    for (int m = 0; m < 5; ++m) g1_piece(cx, 0, m);
    bf16x8 xb[2][KS];
#pragma unroll
    for (int s = 0; s < 6; ++s) { g1_ldx(xb[0][s], cx.xs, cx.lo, s); g1_ldx(xb[1][s], cx.xs + KS * 1024, cx.lo, s); }

    const int aoff0 = img_aoff(lane);
    lds_cptr b0 = (lds_cptr)lds + aoff0, b1 = (lds_cptr)lds + (aoff0 ^ 32);
    asm volatile("" : "+v"(b0), "+v"(b1));
    f32x4 ca[2][2][2], cb[2][2][2];
    QkvEpi qe;
    qe.qg = ws + WS_Q + ((size_t)cg * NTB + tb) * QREC;
    qe.kg = ws + WS_KV + ((size_t)cg * NTB + tb) * KVREC;
    qe.stg = (lds_ptr)lds + 2 * G1_SLOT + w * 4608;

    WAITV_BAR(12);
    MARK(1);
    G1Ring fr;
#pragma unroll
    for (int i = 0; i < 2; ++i) {
        const lds_cptr a = (i & 1) ? b1 : b0;
        fr.lo[i] = trread(a); fr.hi[i] = trread(a + 1024);
    }
    g1_unit<0>(cx, b0, b1, odd, fr, xb, ca, cb, qe);
    g1_unit<1>(cx, b0, b1, odd, fr, xb, cb, ca, qe);
    MARK(2);
    g1_unit<2>(cx, b0, b1, odd, fr, xb, ca, cb, qe);
    MARK(3);
    g1_unit<3>(cx, b0, b1, odd, fr, xb, cb, ca, qe);
    MARK(4);
    g1_unit<4>(cx, b0, b1, odd, fr, xb, ca, cb, qe);
    g1_unit<5>(cx, b0, b1, odd, fr, xb, ca, cb, qe);
    MARK(5);
    g1_unit<6>(cx, b0, b1, odd, fr, xb, cb, ca, qe);
    g1_unit<7>(cx, b0, b1, odd, fr, xb, cb, ca, qe);
    MARK(6);
    g1_unit<8>(cx, b0, b1, odd, fr, xb, ca, cb, qe);
    g1_unit<9>(cx, b0, b1, odd, fr, xb, ca, cb, qe);
    MARK(7);
    g1_unit<10>(cx, b0, b1, odd, fr, xb, cb, ca, qe);
    g1_unit<11>(cx, b0, b1, odd, fr, xb, cb, ca, qe);
    asm volatile("s_nop 7\n\ts_nop 3" : "+v"(cb[0][0][0]), "+v"(cb[0][0][1]), "+v"(cb[1][0][0]), "+v"(cb[1][0][1]));
    {
        char* hs = ws + WS_HF + ((size_t)(2 * tb) * FS + cx.fsingle / 32) * 1024 + odd * 8;
#pragma unroll
        for (int nh = 0; nh < 2; ++nh) {
            float h[4];
#pragma unroll
            for (int j = 0; j < 4; ++j) h[j] = glu(cb[0][0][nh][j], cb[1][0][nh][j]);
            gstore8(hs + (size_t)nh * FS * 1024, cx.lo, (u32x2){cvtpk(h[0], h[1]), cvtpk(h[2], h[3])});
        }
    }
    MARK(8); MARK_DUMP(1, 9);
    STAMP_END(1);
}

constexpr int AT_STAGE = 8 * KVREC;
constexpr float AT_BOUND_MAX = 50.0f;

struct KFrag { bf16x8 k1; bf16x4 k2; };
struct VFrag { bf16x8 v0, v1; };
DI KFrag at_readk(lds_cptr rec, int lane) {
    KFrag k;
    k.k1 = *(const __attribute__((address_space(3))) bf16x8*)(rec + lane * 16);
    k.k2 = *(const __attribute__((address_space(3))) bf16x4*)(rec + 1024 + (lane & 31) * 8);
    return k;
}
DI VFrag at_readv(lds_cptr recv) {
    VFrag v;
    v.v0 = *(const __attribute__((address_space(3))) bf16x8*)(recv);
    v.v1 = *(const __attribute__((address_space(3))) bf16x8*)(recv + 640);
    return v;
}
DI bf16x8 packp(const f32x16& p, int s) {
    return __builtin_bit_cast(bf16x8, (u32x4){cvtpk(p[8 * s], p[8 * s + 1]), cvtpk(p[8 * s + 2], p[8 * s + 3]), cvtpk(p[8 * s + 4], p[8 * s + 5]), cvtpk(p[8 * s + 6], p[8 * s + 7])});
}

DI void at_dma(const char* kvh, int stage, unsigned ldsb, int w, int lane) {
    const char* src = kvh + (size_t)stage * AT_STAGE + w * 5 * 1024;
    const unsigned dst = ldsb + w * 5 * 1024, lo = lane * 16;
    asm volatile("s_mov_b32 m0, %2\n\ts_nop 0\n\t"
                 "global_load_lds_dwordx4 %0, %1\n\tglobal_load_lds_dwordx4 %0, %1 offset:1024\n\t"
                 "global_load_lds_dwordx4 %0, %1 offset:2048\n\tglobal_load_lds_dwordx4 %0, %1 offset:3072"
                 :: "v"(lo), "s"(src), "s"(dst) : "memory");
    glds16(src + 4096, lo, dst + 4096);
}

#define AMFMA16_NEW(d, a, b, c) asm volatile("v_mfma_f32_32x32x16_bf16 %0, %1, %2, %3" : "=&v"(d) : "v"(a), "v"(b), "v"(c))
#define AMFMA16_ACC(d, a, b) asm volatile("v_mfma_f32_32x32x16_bf16 %0, %1, %2, %0" : "+v"(d) : "v"(a), "v"(b))
#define AMFMA8_ACC(d, a, b) asm volatile("v_mfma_f32_32x32x8_bf16 %0, %1, %2, %0" : "+v"(d) : "v"(a), "v"(b))
#define AT_EXP4(E, SX, a) do { _Pragma("unroll") for (int i_ = 0; i_ < 4; ++i_) asm volatile("v_exp_f32 %0, %1" : "=v"(E[i_]) : "v"(SX[a + i_])); } while (0)
#define AT_PK2(DST, k, E) do { asm volatile("v_cvt_pk_bf16_f32 %0, %1, %2" : "=v"(DST[k]) : "v"(E[0]), "v"(E[1]));   \
                               asm volatile("v_cvt_pk_bf16_f32 %0, %1, %2" : "=v"(DST[k + 1]) : "v"(E[2]), "v"(E[3])); } while (0)
#define AT_STEP(SX, PX0, PX1, SY, CY, QY1, QY2, KF, OY, PY0, PY1, VF, LOADS)                                   \
    do {                                                                                                       \
        float e0_[4], e1_[4], e2_[4];                                                                          \
        AMFMA16_NEW(SY, KF.k1, QY1, CY);                                                                       \
        LOADS;                                                                                                 \
        SBAR();                                                                                                \
        AT_EXP4(e0_, SX, 0);                                                                                   \
        AT_PK2(PY1, 2, etail);                                                                                 \
        AMFMA8_ACC(SY, KF.k2, QY2);                                                                            \
        AT_EXP4(e1_, SX, 4);                                                                                   \
        AT_PK2(PX0, 0, e0_);                                                                                   \
        AMFMA16_ACC(OY, VF.v0, PY0);                                                                           \
        AT_EXP4(e2_, SX, 8);                                                                                   \
        AT_PK2(PX0, 2, e1_);                                                                                   \
        AMFMA16_ACC(OY, VF.v1, PY1);                                                                           \
        AT_EXP4(etail, SX, 12);                                                                                \
        AT_PK2(PX1, 0, e2_);                                                                                   \
        SBAR();                                                                                                \
    } while (0)

DI void at_online(const KFrag& k, const VFrag& v, bf16x8 q1, bf16x4 q2, float& m, f32x16& o) {
    f32x16 z = {};
    f32x16 s = MFMA16(k.k1, q1, z);
    s = MFMA8(k.k2, q2, s);
    float t = s[0];
#pragma unroll
    for (int i = 1; i < 16; ++i) t = fmaxf(t, s[i]);
    t = fmaxf(t, swap32(t));
    const float mn = fmaxf(m, t);
    const float alpha = __builtin_amdgcn_exp2f(m - mn);
    m = mn;
#pragma unroll
    for (int i = 0; i < 16; ++i) { s[i] = __builtin_amdgcn_exp2f(s[i] - mn); o[i] *= alpha; }
    o = MFMA16(v.v0, packp(s, 0), o);
    o = MFMA16(v.v1, packp(s, 1), o);
}

DI void at_store(const f32x16& o, char* ws, int h, int tb, int lane) {
    const int r = lane & 31, hi = lane >> 5;
    const float other = swap32(o[10]);
    const float inv = 1.0f / (hi ? other : o[10]);
    char* of = ws + WS_OF + (size_t)(2 * tb + (r >> 4)) * KS * 1024;
    gstore16p(of + (h >> 1) * 1024 + ((r & 15) + 16 * (2 * (h & 1) + hi)) * 16,
              (u32x4){cvtpk(o[0] * inv, o[1] * inv), cvtpk(o[2] * inv, o[3] * inv), cvtpk(o[4] * inv, o[5] * inv), cvtpk(o[6] * inv, o[7] * inv)});
    if (hi == 0) *(unsigned*)(of + (16 + (h >> 4)) * 1024 + ((r & 15) + 16 * ((h >> 2) & 3)) * 16 + (h & 3) * 4) = cvtpk(o[8] * inv, o[9] * inv);
}

DI float at_qnorm2(bf16x8 q1, bf16x4 q2) {
    const u32x4 a = __builtin_bit_cast(u32x4, q1); const u32x2 b = __builtin_bit_cast(u32x2, q2);
    float n = bf_lo(b[0]) * bf_lo(b[0]) + bf_hi(b[0]) * bf_hi(b[0]);
#pragma unroll
    for (int j = 0; j < 4; ++j) n += bf_lo(a[j]) * bf_lo(a[j]) + bf_hi(a[j]) * bf_hi(a[j]);
    return sum32x(n);
}

DI void attn_block(char* ws, char* lds, int bid) {
    const int tid = threadIdx.x, lane = tid & 63, hi = lane >> 5;
    const int w = __builtin_amdgcn_readfirstlane(tid >> 6);
    const int xcd = bid & 7, idx = bid >> 3;
    const int h = 4 * xcd + (idx & 3), qg = idx >> 2;
    const int tbA = 8 * qg + 2 * w, tbB = tbA + 1;
    const unsigned lds0 = (unsigned)(uintptr_t)lds;
    const char* kvh = ws + WS_KV + (size_t)h * NTB * KVREC;
    MARKS; MARK(0);

    bf16x8 qA1, qB1; unsigned qa2, qb2; float km;
    {
        const char* q1 = ws + WS_Q + ((size_t)h * NTB + tbA) * QREC;
        const char* kx = ws + WS_Q + (size_t)h * NTB * QREC + 1152;
        const unsigned l16 = lane * 16, l4 = (lane & 31) * 4 + 1024, lk = lane * QREC;
        asm volatile("global_load_dword %0, %1, %2" : "=v"(km) : "v"(lk), "s"(kx));
        asm volatile("global_load_dwordx4 %0, %1, %2" : "=v"(qA1) : "v"(l16), "s"(q1));
        asm volatile("global_load_dwordx4 %0, %1, %2 offset:%3" : "=v"(qB1) : "v"(l16), "s"(q1), "n"(QREC));
        asm volatile("global_load_dword %0, %1, %2" : "=v"(qa2) : "v"(l4), "s"(q1));
        asm volatile("global_load_dword %0, %1, %2 offset:%3" : "=v"(qb2) : "v"(l4), "s"(q1), "n"(QREC));
    }
    at_dma(kvh, 0, lds0, w, lane);
    at_dma(kvh, 1, lds0 + AT_STAGE, w, lane);
    asm volatile("s_waitcnt vmcnt(10)" : "+v"(km), "+v"(qA1), "+v"(qB1), "+v"(qa2), "+v"(qb2));
    const bf16x4 qA2 = __builtin_bit_cast(bf16x4, (u32x2){hi ? 0u : qa2, 0u});
    const bf16x4 qB2 = __builtin_bit_cast(bf16x4, (u32x2){hi ? 0u : qb2, 0u});
    km = rowmax16(km);
    km = max32x(max16x(km));
    const float mA = __builtin_amdgcn_sqrtf(at_qnorm2(qA1, qA2) * km) * 1.01f, mB = __builtin_amdgcn_sqrtf(at_qnorm2(qB1, qB2) * km) * 1.01f;
    const bool fast = __all(mA <= AT_BOUND_MAX && mB <= AT_BOUND_MAX);

    const int voffs = 1280 + hi * 320 + min(lane & 31, 19) * 16;
    const lds_cptr L = (lds_cptr)lds;
    f32x16 oA = {}, oB = {};

    if (fast) {
        f32x16 cA, cB;
#pragma unroll
        for (int i = 0; i < 16; ++i) { cA[i] = -mA; cB[i] = -mB; }
        asm volatile("" : "+v"(cA), "+v"(cB));
        f32x16 sA, sB;
        u32x4 pA0 = {}, pA1 = {}, pB0 = {}, pB1 = {};
        float etail[4] = {0.f, 0.f, 0.f, 0.f};
        KFrag k0, k1; VFrag v0, v1;
        MARK(1);
        WAITV_BAR(5);
        MARK(2);
        k0 = at_readk(L, lane);
        v1 = at_readv(L + voffs);
        sA = MFMA16(k0.k1, qA1, cA); sA = MFMA8(k0.k2, qA2, sA);
        asm volatile("s_nop 15" : "+v"(sA));
        int slot = 0;
        for (int st = 0; st < 8; ++st) {
            const lds_cptr cur = L + slot * AT_STAGE;
            const int nslot = slot == 2 ? 0 : slot + 1;
#pragma unroll
            for (int t = 0; t < 8; t += 2) {
                AT_STEP(sA, pA0, pA1, sB, cB, qB1, qB2, k0, oB, pB0, pB1, v1,
                        (k1 = at_readk(cur + (t + 1) * KVREC, lane), v0 = at_readv(cur + t * KVREC + voffs)));
                AT_STEP(sB, pB0, pB1, sA, cA, qA1, qA2, k1, oA, pA0, pA1, v0, (void)0);
                lds_cptr nk = cur + (t + 2) * KVREC;
                if (t == 6) {
                    if (st < 7) {
                        if (st < 6) { WAITV_BAR(0); at_dma(kvh, st + 2, lds0 + (nslot == 2 ? 0 : nslot + 1) * AT_STAGE, w, lane); }
                        else WAITV_BAR(0);
                        nk = L + nslot * AT_STAGE;
                    } else nk = cur;
                }
                AT_STEP(sA, pA0, pA1, sB, cB, qB1, qB2, k1, oB, pB0, pB1, v0,
                        (k0 = at_readk(nk, lane), v1 = at_readv(cur + (t + 1) * KVREC + voffs)));
                AT_STEP(sB, pB0, pB1, sA, cA, qA1, qA2, k0, oA, pA0, pA1, v1, (void)0);
            }
            slot = nslot;
            MARK(3 + st);
        }
        asm volatile("s_nop 1");
        AT_PK2(pB1, 2, etail);
        asm volatile("s_nop 1" : "+v"(pB1));
        AMFMA16_ACC(oB, v1.v0, pB0);
        AMFMA16_ACC(oB, v1.v1, pB1);
        asm volatile("s_nop 15\n\ts_nop 7" : "+v"(oA), "+v"(oB));
    } else {
        float mxA = -INFINITY, mxB = -INFINITY;
        WAITV_BAR(5);
        int slot = 0;
        for (int st = 0; st < 8; ++st) {
            const lds_cptr cur = L + slot * AT_STAGE;
            const int nslot = slot == 2 ? 0 : slot + 1;
            for (int t = 0; t < 8; ++t) {
                const KFrag k = at_readk(cur + t * KVREC, lane);
                const VFrag v = at_readv(cur + t * KVREC + voffs);
                at_online(k, v, qA1, qA2, mxA, oA);
                at_online(k, v, qB1, qB2, mxB, oB);
            }
            if (st < 7) {
                WAITV_BAR(0);
                if (st < 6) at_dma(kvh, st + 2, lds0 + (nslot == 2 ? 0 : nslot + 1) * AT_STAGE, w, lane);
            }
            slot = nslot;
        }
    }
    at_store(oA, ws, h, tbA, lane);
    at_store(oB, ws, h, tbB, lane);
    MARK(11); MARK_DUMP(2, 12);
}

constexpr int DN_STEP = 32 * 288, DN_SLOT = 2 * DN_STEP, DN_LDS = 4 * DN_SLOT, DN_NST = 18;
constexpr int dn_piece(int st, int i) {
    if ((i == 3 || i == 7 || i == 11) && st + 3 < DN_NST) return (st + 3) * 8 + 2 + (i - 3) / 4;
    if ((i == 15 || i == 17) && st + 4 < DN_NST) return (st + 4) * 8 + (i - 15) / 2;
    return -1;
}
constexpr int dn_young(int st) {
    int n = 0; bool seen = st + 1 == 1;
    const int need = st + 1;
    for (int s2 = 0; s2 <= st; ++s2)
        for (int i = 0; i < 18; ++i) {
            if (s2 == st && i >= 15) break;
            if (i == 0 && s2 + 2 < DN_NST) { if (seen) n += 4; if (s2 + 2 == need) { seen = true; n = 0; } }
            const int pc = dn_piece(s2, i);
            if (pc >= 0) { if (seen) n += 1; if ((pc >> 3) == need) { seen = true; n = 0; } }
        }
    return n;
}
DI void dn_ldb(bf16x8 (&b)[2][2], const char* hs, unsigned lo, int st) {
#pragma unroll
    for (int k = 0; k < 2; ++k) {
        asm volatile("global_load_dwordx4 %0, %1, %2 offset:%3" : "=v"(b[0][k]) : "v"(lo), "s"(hs + (size_t)st * 2048), "n"(k * 1024));
        asm volatile("global_load_dwordx4 %0, %1, %2 offset:%3" : "=v"(b[1][k]) : "v"(lo), "s"(hs + (size_t)FS * 1024 + (size_t)st * 2048), "n"(k * 1024));
    }
}
template <int N> DI void dn_wait(bf16x8 (&b)[2][2]) {
    asm volatile("s_waitcnt vmcnt(%4) lgkmcnt(0)\n\ts_barrier" : "+v"(b[0][0]), "+v"(b[0][1]), "+v"(b[1][0]), "+v"(b[1][1]) : "n"(N) : "memory");
}

DI void down_block(const char* wd, char* ws, char* lds, int bid) {
    const int tid = threadIdx.x, lane = tid & 63;
    const int w = __builtin_amdgcn_readfirstlane(tid >> 6);
    const int xcd = bid & 7, idx = bid >> 3;
    const int kq = xcd >> 1, tg = (xcd & 1) * 8 + (idx >> 2), dq = idx & 3;
    const int tb16 = 8 * tg + 2 * w, fs0 = 36 * kq;
    const unsigned lds0 = (unsigned)(uintptr_t)lds;
    unsigned voff[5], doff[5];
#pragma unroll
    for (int m = 0; m < 5; ++m) { const int i = min(5 * (w & 1) + m, 8), n = 64 * i + lane; voff[m] = (n / 18) * (D * 2) + (n % 18) * 16; doff[m] = (w >> 1) * DN_STEP + i * 1024; }
    const char* wsrc = wd + (size_t)(fs0 + (w >> 1)) * 32 * (D * 2) + dq * 288;
    const lds_cptr A = (lds_cptr)lds + (4 * (lane >> 4) + ((lane & 15) >> 2)) * 288 + (lane & 3) * 8;
    const char* hs = ws + WS_HF + ((size_t)tb16 * FS + fs0) * 1024;
    const unsigned lo = lane * 16;
    MARKS; MARK(0);

    auto dma = [&](int st, int m) {
        const char* base = wsrc;
        asm volatile("" : "+s"(base));
        glds16(base + (size_t)st * 2 * 32 * (D * 2), voff[m], lds0 + (st & 3) * DN_SLOT + doff[m]);
    };
#pragma unroll
    for (int st = 0; st < 3; ++st)
#pragma unroll
        for (int m = 0; m < 5; ++m) dma(st, m);
    dma(3, 0); dma(3, 1);
    f32x4 acc[9][2];
    bf16x8 b[3][2][2];
    dn_ldb(b[0], hs, lo, 0);
    dn_ldb(b[1], hs, lo, 1);
    lds_cptr Alo = A, Ahi = A + 2 * DN_SLOT;
    asm volatile("" : "+v"(Alo), "+v"(Ahi));
    auto addr = [&](int st, int i) { return ((st & 2) ? Ahi : Alo) + (st & 1) * DN_SLOT + (i / 9) * DN_STEP + (i % 9) * 32; };
    dn_wait<4>(b[0]);
    bf16x4 flo[4], fhi[4];
#pragma unroll
    for (int i = 0; i < 3; ++i) { flo[i] = trread(addr(0, i)); fhi[i] = trread(addr(0, i) + 16 * 288); }
    static_for<0, DN_NST * 18>([&](auto nc) {
        constexpr int n = decltype(nc)::value, st = n / 18, i = n % 18, set = st % 3;
        if constexpr (i == 0 && st + 2 < DN_NST) dn_ldb(b[(st + 2) % 3], hs, lo, st + 2);
        if constexpr (i == 15 && st + 1 < DN_NST) dn_wait<dn_young(st)>(b[(st + 1) % 3]);
        if constexpr (dn_piece(st, i) >= 0) dma(dn_piece(st, i) >> 3, dn_piece(st, i) & 7);
        constexpr bool more = n + 3 < DN_NST * 18;
        constexpr int nst = (n + 3) / 18, ni = (n + 3) % 18;
        const bf16x8 fr = join(flo[n & 3], fhi[n & 3]);
        if constexpr (n < 9) AMF_NEW(acc[i % 9][0], fr, b[set][0][i / 9]); else AMF_ACC(acc[i % 9][0], fr, b[set][0][i / 9]);
        SBAR();
        if constexpr (more) flo[(n + 3) & 3] = trread(addr(nst, ni));
        SBAR();
        if constexpr (n < 9) AMF_NEW(acc[i % 9][1], fr, b[set][1][i / 9]); else AMF_ACC(acc[i % 9][1], fr, b[set][1][i / 9]);
        SBAR();
        if constexpr (more) fhi[(n + 3) & 3] = trread(addr(nst, ni) + 16 * 288);
        SBAR();
    });
    MARK(1);
    asm volatile("s_nop 7\n\ts_nop 3" : "+v"(acc[7][0]), "+v"(acc[7][1]), "+v"(acc[8][0]), "+v"(acc[8][1]));
    char* dst = ws + WS_PART + (((size_t)kq * NT16 + tb16) * 36 + 9 * dq) * PTILE;
#pragma unroll
    for (int nh = 0; nh < 2; ++nh)
#pragma unroll
        for (int i = 0; i < 9; ++i) gstore12(dst + (size_t)nh * 36 * PTILE + i * PTILE, lane * 12, pack24(acc[i][nh]));
    MARK(2); MARK_DUMP(3, 3);
}

constexpr int MIX_LDS = DN_LDS;
static_assert(MIX_LDS >= 3 * AT_STAGE && 2 * MIX_LDS <= 160 * 1024, "two workgroups per CU");
__global__ __launch_bounds__(256, 2) void k_mix(const char* wd, char* ws) {
    extern __shared__ __attribute__((aligned(16))) char lds[];
    STAMP_BEGIN;
    const int b = blockIdx.x;
    if (b < 256) attn_block(ws, lds, b); else down_block(wd, ws, lds, b - 256);
    STAMP_END(5);
}

constexpr int FN_A = 3 * KS * 1024, FN_LDS = FN_A + 8 * 3 * 1024;
DI void ldx1(bf16x8& d, const char* base, unsigned lo, int off) {
    asm volatile("global_load_dwordx4 %0, %1, %2 offset:%3" : "=v"(d) : "v"(lo), "s"(base), "n"(off));
}
template <int N> DI void waitx(bf16x8& a, bf16x8& b) { asm volatile("s_waitcnt vmcnt(%2)" : "+v"(a), "+v"(b) : "n"(N)); }
DI void fn_ldpx(u32x3 (&pq)[3][4], u32x2 (&xv)[3], const char* pp, unsigned l12, const char* xp, unsigned xo) {
#pragma unroll
    for (int t = 0; t < 3; ++t) {
#pragma unroll
        for (int k = 0; k < 4; ++k) asm volatile("global_load_dwordx3 %0, %1, %2 offset:%3" : "=v"(pq[t][k]) : "v"(l12), "s"(pp + (size_t)k * NT16 * 36 * PTILE), "n"(t * PTILE));
        asm volatile("global_load_dwordx2 %0, %1, %2 offset:%3" : "=v"(xv[t]) : "v"(xo), "s"(xp), "n"(t * 32));
    }
}
__global__ __launch_bounds__(512, 1) void k_final(const unsigned short* x, char* ws, unsigned short* out) {
    extern __shared__ __attribute__((aligned(16))) char lds[];
    STAMP_BEGIN;
    const int tid = threadIdx.x, lane = tid & 63;
    const int w = __builtin_amdgcn_readfirstlane(tid >> 6);
    const int xcd = blockIdx.x & 7, idx = blockIdx.x >> 3;
    const int tg = 2 * xcd + idx / 12, dg = idx % 12, kh = w >> 2, tb16 = 8 * tg + 2 * (w & 3);
    const unsigned lds0 = (unsigned)(uintptr_t)lds, lo = lane * 16;
    MARKS; MARK(0);
    {
        const char* src = ws + WS_WOP + (size_t)dg * FN_A;
#pragma unroll
        for (int i = 0; i < 7; ++i) { const int n = min(w * 7 + i, 3 * KS - 1); glds16(src + n * 1024, lo, lds0 + n * 1024); }
    }
    const char* os = ws + WS_OF + ((size_t)tb16 * KS + 9 * kh) * 1024;
    bf16x8 b[2][9];
    auto ldo = [&](int s) { ldx1(b[0][s], os + (s >> 2) * 4096, lo, (s & 3) * 1024); ldx1(b[1][s], os + KS * 1024 + (s >> 2) * 4096, lo, (s & 3) * 1024); };
#pragma unroll
    for (int s = 0; s < 4; ++s) ldo(s);
    MARK(1);
    asm volatile("s_waitcnt vmcnt(8)\n\ts_barrier" ::: "memory");
    MARK(2);
    const int tok = (tb16 + kh) * 16 + (lane & 15);
    const char* xp = (const char*)(x + (size_t)(tb16 + kh) * 16 * D + dg * 48);
    const unsigned xo2 = ((lane & 15) * D + 4 * (lane >> 4)) * 2;
    u32x2 xv[3];
    u32x3 pq[3][4];
    const char* pp = ws + WS_PART + ((size_t)(tb16 + kh) * 36 + 3 * dg) * PTILE;
    const lds_cptr A = (lds_cptr)lds + lo + 9 * kh * 1024;
    f32x4 acc[2][3] = {};
    static_for<0, 9>([&](auto sc) {
        constexpr int s = decltype(sc)::value;
        bf16x8 a[3];
#pragma unroll
        for (int t = 0; t < 3; ++t) a[t] = *(const __attribute__((address_space(3))) bf16x8*)(A + (t * KS + s) * 1024);
        if constexpr (s + 4 < 9) ldo(s + 4);
        if constexpr (s == 4) fn_ldpx(pq, xv, pp, lane * 12, xp, xo2);
        waitx<2 * ((s + 4 < 9 ? s + 4 : 8) - s) + (s >= 4 ? 15 : 0)>(b[0][s], b[1][s]);
#pragma unroll
        for (int t = 0; t < 3; ++t) {
            acc[0][t] = MFMA32K(a[t], b[0][s], acc[0][t]);
            acc[1][t] = MFMA32K(a[t], b[1][s], acc[1][t]);
        }
        SBAR();
    });
    MARK(3);
    typedef __attribute__((address_space(3))) f32x4* l128;
    const lds_ptr xo = (lds_ptr)lds + FN_A + lo;
#pragma unroll
    for (int t = 0; t < 3; ++t) *(l128)(xo + (w * 3 + t) * 1024) = kh ? acc[0][t] : acc[1][t];
    asm volatile("s_waitcnt lgkmcnt(0)\n\ts_barrier" ::: "memory");
    f32x4 sum[3];
#pragma unroll
    for (int t = 0; t < 3; ++t) {
        const f32x4 other = *(l128)(xo + ((w ^ 4) * 3 + t) * 1024);
        sum[t] = kh ? other + acc[1][t] : acc[0][t] + other;
    }
    asm volatile("s_waitcnt vmcnt(0)" : "+v"(xv[0]), "+v"(xv[1]), "+v"(xv[2]));
#pragma unroll
    for (int t = 0; t < 3; ++t) {
        asm volatile("" : "+v"(pq[t][0]), "+v"(pq[t][1]), "+v"(pq[t][2]), "+v"(pq[t][3]));
        const f32x4 ffn = (unpack24(pq[t][0]) + unpack24(pq[t][1])) + (unpack24(pq[t][2]) + unpack24(pq[t][3]));
        float o[4];
#pragma unroll
        for (int c = 0; c < 4; ++c) {
            const float f = rbf(ffn[c]);
            const float g = rbf(f * __builtin_amdgcn_rcpf(1.0f + __builtin_amdgcn_exp2f(f * -LOG2E)));
            const float xx = (c & 1) ? bf_hi(xv[t][c >> 1]) : bf_lo(xv[t][c >> 1]);
            o[c] = xx + rbf(rbf(sum[t][c]) * g);
        }
        *(u32x2*)(out + (size_t)tok * D + dg * 48 + 16 * t + 4 * (lane >> 4)) = (u32x2){cvtpk(o[0], o[1]), cvtpk(o[2], o[3])};
    }
    MARK(4);
    if (blockIdx.x == 0 && threadIdx.x < 64 && DIAG > 0) {
        const unsigned long long* st = (const unsigned long long*)(ws + WS_DIAG);
        const unsigned long long t4 = __builtin_amdgcn_s_memrealtime();
        unsigned long long dl = st[1] - st[0];
        if (dl > 5000) dl = 5000;
        while (__builtin_amdgcn_s_memrealtime() - t4 < 10 * dl) __builtin_amdgcn_s_sleep(8);
    }
    MARK(5); MARK_DUMP(4, 6);
    STAMP_END(4);
}

extern "C" void kernel_launch(void* const* d_in, const int* in_sizes, int n_in, void* d_out, int out_size, void* d_ws, size_t ws_size, hipStream_t stream) {
    const unsigned short* x = (const unsigned short*)d_in[0];
    const unsigned short* wqkv = (const unsigned short*)d_in[1];
    const unsigned short* wo = (const unsigned short*)d_in[2];
    const char* wg = (const char*)d_in[4];
    const char* wu = (const char*)d_in[5];
    const char* wd = (const char*)d_in[6];
    char* ws = (char*)d_ws;
    (void)hipFuncSetAttribute((const void*)k_gemm1, hipFuncAttributeMaxDynamicSharedMemorySize, 4 * G1_SLOT);
    (void)hipFuncSetAttribute((const void*)k_mix, hipFuncAttributeMaxDynamicSharedMemorySize, MIX_LDS);
    (void)hipFuncSetAttribute((const void*)k_final, hipFuncAttributeMaxDynamicSharedMemorySize, FN_LDS);
    k_prep<<<256, 1024, 0, stream>>>(x, wqkv, wo, ws);
    k_gemm1<<<256, 512, 4 * G1_SLOT, stream>>>(wg, wu, ws);
    k_mix<<<512, 256, MIX_LDS, stream>>>(wd, ws);
    k_final<<<192, 512, FN_LDS, stream>>>(x, ws, (unsigned short*)d_out);
}
```

```cpp
#define DIAG 0
#include <hip/hip_runtime.h>
#include <hip/hip_bf16.h>
#include <stdint.h>
#include <type_traits>

typedef __attribute__((ext_vector_type(8))) short bf16x8;
typedef __attribute__((ext_vector_type(4))) short bf16x4;
typedef __attribute__((ext_vector_type(2))) __bf16 bf16p;
typedef __attribute__((ext_vector_type(2))) float f32x2;
typedef __attribute__((ext_vector_type(4))) float f32x4;
typedef __attribute__((ext_vector_type(16))) float f32x16;
typedef __attribute__((ext_vector_type(2))) unsigned u32x2;
typedef __attribute__((ext_vector_type(3))) unsigned u32x3;
typedef __attribute__((ext_vector_type(4))) unsigned u32x4;
typedef __attribute__((address_space(3))) const char* lds_cptr;
typedef __attribute__((address_space(3))) char* lds_ptr;
typedef short v4i16_t __attribute__((ext_vector_type(4)));

#define DI __device__ __forceinline__
#define MFMA16(a, b, c) __builtin_amdgcn_mfma_f32_32x32x16_bf16((a), (b), (c), 0, 0, 0)
#define MFMA32K(a, b, c) __builtin_amdgcn_mfma_f32_16x16x32_bf16((a), (b), (c), 0, 0, 0)
#define AMF_ACC(c, a, b) asm volatile("v_mfma_f32_16x16x32_bf16 %0, %1, %2, %0" : "+v"(c) : "v"(a), "v"(b))
#define AMF_NEW(c, a, b) asm volatile("v_mfma_f32_16x16x32_bf16 %0, %1, %2, 0" : "=&v"(c) : "v"(a), "v"(b))
#define MFMA8(a, b, c) __builtin_amdgcn_mfma_f32_32x32x8bf16_1k((a), (b), (c), 0, 0, 0)
#define SBAR() __builtin_amdgcn_sched_barrier(0)

template <int I, int N, class Fn> DI void static_for(Fn&& fn) {
    if constexpr (I < N) { fn(std::integral_constant<int, I>{}); static_for<I + 1, N>(fn); }
}

constexpr int S = 2048, D = 576, NH = 32, DH = 18, F = 4608;
constexpr int NTB = S / 32;
constexpr int NT16 = S / 16;
constexpr int KS = D / 32;
constexpr int FS = F / 32;

constexpr int KVREC = 2560;
constexpr size_t WS_XNF = 0;
constexpr size_t WS_WQP = WS_XNF + (size_t)NT16 * KS * 1024;
constexpr int QREC = 1280;
constexpr size_t WS_WOP = WS_WQP + (size_t)NH * KS * 4096;
constexpr size_t WS_Q = WS_WOP + (size_t)36 * KS * 1024;
constexpr size_t WS_KV = WS_Q + (size_t)NH * NTB * QREC;
constexpr size_t WS_OF = WS_KV + (size_t)NH * NTB * KVREC;
constexpr size_t WS_HF = WS_OF + (size_t)NT16 * KS * 1024;
constexpr int PTILE = 768;
constexpr size_t WS_PART = WS_HF + (size_t)NT16 * FS * 1024;
constexpr size_t WS_END = WS_PART + (size_t)4 * NT16 * 36 * PTILE;
static_assert(WS_END <= (size_t)256 << 20, "the workspace holds at least 256 MiB");

constexpr size_t WS_DIAG = 190u << 20;
DI void diag_stamp(char* ws, int k, int blk) {
    if ((int)blockIdx.x == blk && threadIdx.x == 0) { const unsigned long long t = __builtin_amdgcn_s_memrealtime(); __hip_atomic_store((unsigned long long*)(ws + WS_DIAG) + k, t, __ATOMIC_RELAXED, __HIP_MEMORY_SCOPE_AGENT); }
}
constexpr float LOG2E = 1.4426950408889634f;
constexpr float QSCALE = 0.23570226039551584f * LOG2E;

DI unsigned cvtpk(float lo, float hi) { return __builtin_bit_cast(unsigned, __builtin_convertvector((f32x2){lo, hi}, bf16p)); }
DI float bf_lo(unsigned u) { return __uint_as_float(u << 16); }
DI float bf_hi(unsigned u) { return __uint_as_float(u & 0xffff0000u); }
DI float rbf(float x) { return bf_lo(cvtpk(x, x)); }
DI bf16x4 trread(lds_cptr p) { return __builtin_bit_cast(bf16x4, __builtin_amdgcn_ds_read_tr16_b64_v4i16((__attribute__((address_space(3))) v4i16_t*)p)); }
DI bf16x8 join(bf16x4 a, bf16x4 b) { return (bf16x8){a[0], a[1], a[2], a[3], b[0], b[1], b[2], b[3]}; }
DI float sum16x(float x) { auto r = __builtin_amdgcn_permlane16_swap(__float_as_uint(x), __float_as_uint(x), false, false); return __uint_as_float(r[0]) + __uint_as_float(r[1]); }
DI float sum32x(float x) { auto r = __builtin_amdgcn_permlane32_swap(__float_as_uint(x), __float_as_uint(x), false, false); return __uint_as_float(r[0]) + __uint_as_float(r[1]); }
DI float max16x(float x) { auto r = __builtin_amdgcn_permlane16_swap(__float_as_uint(x), __float_as_uint(x), false, false); return fmaxf(__uint_as_float(r[0]), __uint_as_float(r[1])); }
DI float max32x(float x) { auto r = __builtin_amdgcn_permlane32_swap(__float_as_uint(x), __float_as_uint(x), false, false); return fmaxf(__uint_as_float(r[0]), __uint_as_float(r[1])); }
template <int CTRL> DI float dppf(float x) { return __uint_as_float(__builtin_amdgcn_update_dpp(0u, __float_as_uint(x), CTRL, 0xf, 0xf, true)); }
DI float rowmax16(float x) {
    x = fmaxf(x, dppf<0xB1>(x));
    x = fmaxf(x, dppf<0x4E>(x));
    x = fmaxf(x, dppf<0x141>(x));
    return fmaxf(x, dppf<0x140>(x));
}
DI float swap32(float x) { auto r = __builtin_amdgcn_permlane32_swap(__float_as_uint(x), __float_as_uint(x), false, false); return __uint_as_float((threadIdx.x & 32) ? r[0] : r[1]); }

DI void glds16(const void* sbase, unsigned voff, unsigned lds_base) {
    asm volatile("s_mov_b32 m0, %2\n\ts_nop 0\n\tglobal_load_lds_dwordx4 %0, %1" :: "v"(voff), "s"(sbase), "s"(lds_base) : "memory");
}
#define STPOL "sc1"
DI void gstore16(void* sbase, unsigned voff, u32x4 v) { asm volatile("global_store_dwordx4 %0, %1, %2 " STPOL "\n\ts_nop 1" :: "v"(voff), "v"(v), "s"(sbase) : "memory"); }
DI void gstore8(void* sbase, unsigned voff, u32x2 v) { asm volatile("global_store_dwordx2 %0, %1, %2" :: "v"(voff), "v"(v), "s"(sbase) : "memory"); }
DI u32x3 pack24(f32x4 v) {
    const unsigned a = __float_as_uint(v[0]), b = __float_as_uint(v[1]), c = __float_as_uint(v[2]), d = __float_as_uint(v[3]);
    return (u32x3){__builtin_amdgcn_perm(a, d, 0x07060501u), __builtin_amdgcn_perm(b, d, 0x07060502u), __builtin_amdgcn_perm(c, d, 0x07060503u)};
}
DI f32x4 unpack24(u32x3 p) {
    const unsigned t = __builtin_amdgcn_perm(p[2], p[1], 0x04000c0cu);
    const unsigned d = __builtin_amdgcn_perm(t, p[0], 0x0706000cu);
    return (f32x4){__uint_as_float(p[0] & 0xffffff00u), __uint_as_float(p[1] & 0xffffff00u), __uint_as_float(p[2] & 0xffffff00u), __uint_as_float(d)};
}
DI void gstore12(void* sbase, unsigned voff, u32x3 v) { asm volatile("global_store_dwordx3 %0, %1, %2 " STPOL "\n\ts_nop 1" :: "v"(voff), "v"(v), "s"(sbase) : "memory"); }
DI void gstore16p(void* p, u32x4 v) { asm volatile("global_store_dwordx4 %0, %1, off " STPOL "\n\ts_nop 1" :: "v"(p), "v"(v) : "memory"); }
#define WAITV_BAR(n) asm volatile("s_waitcnt vmcnt(" #n ") lgkmcnt(0)\n\ts_barrier" ::: "memory")

DI int wo_row(int ks, int kg, int j) {
    return ks < 16 ? DH * (2 * ks + (kg >> 1)) + 8 * (j >> 2) + 4 * (kg & 1) + (j & 3) : DH * (16 * (ks - 16) + 4 * kg + (j >> 1)) + 16 + (j & 1);
}
DI float sum32(float x) {
    x += dppf<0xB1>(x);
    x += dppf<0x4E>(x);
    x += dppf<0x141>(x);
    x += dppf<0x140>(x);
    return sum16x(x);
}

__global__ __launch_bounds__(1024) void k_prep(const unsigned short* __restrict__ x, const unsigned short* __restrict__ wqkv, const unsigned short* __restrict__ wo, char* __restrict__ ws) {
    __shared__ __attribute__((aligned(16))) char stage[KS * 512];
    diag_stamp(ws, 0, 0);
    const int tid = threadIdx.x;
    const int wv = __builtin_amdgcn_readfirstlane(tid >> 6);
    if (wv < 4) {
        const int row = blockIdx.x * 8 + (tid >> 5), t = tid & 31;
        const u32x4* xr = (const u32x4*)(x + (size_t)row * D);
        u32x4 v[3];
        v[0] = xr[t]; v[1] = xr[t + 32]; v[2] = (t < 8) ? xr[t + 64] : (u32x4){0u, 0u, 0u, 0u};
        float sum = 0.f;
#pragma unroll
        for (int i = 0; i < 3; ++i)
#pragma unroll
            for (int j = 0; j < 4; ++j) sum += bf_lo(v[i][j]) + bf_hi(v[i][j]);
        const float mean = sum32(sum) * (1.0f / D);
        float sq = 0.f;
#pragma unroll
        for (int i = 0; i < 3; ++i)
            if (i < 2 || t < 8)
#pragma unroll
                for (int j = 0; j < 4; ++j) { const float a = bf_lo(v[i][j]) - mean, b = bf_hi(v[i][j]) - mean; sq += a * a + b * b; }
        const float mb = rbf(mean);
        const float den = rbf(__fsqrt_rn(rbf(rbf(sum32(sq) * (1.0f / D)) + 1e-5f)));
        const float rc = __builtin_amdgcn_rcpf(den);
        auto quot = [&](float a) { const float q = a * rc; return __builtin_fmaf(__builtin_fmaf(-q, den, a), rc, q); };
#pragma unroll
        for (int i = 0; i < 3; ++i) {
            if (i < 2 || t < 8) {
                const int p = t + 32 * i;
                unsigned o[4];
#pragma unroll
                for (int j = 0; j < 4; ++j) o[j] = cvtpk(quot(rbf(bf_lo(v[i][j]) - mb)), quot(rbf(bf_hi(v[i][j]) - mb)));
                char* d = stage + (p >> 2) * 512 + (p & 1) * 256 + (tid >> 5) * 16 + ((p >> 1) & 1) * 8;
                *(u32x2*)d = (u32x2){o[0], o[1]};
                *(u32x2*)(d + 128) = (u32x2){o[2], o[3]};
            }
        }
        asm volatile("s_waitcnt lgkmcnt(0)\n\ts_barrier" ::: "memory");
        char* dst = ws + WS_XNF + (size_t)(blockIdx.x >> 1) * KS * 1024 + (blockIdx.x & 1) * 128;
#pragma unroll
        for (int i = 0; i < 3; ++i) {
            const int n = tid + 256 * i;
            if (n < KS * 32) gstore16p(dst + (n >> 3) * 256 + (n & 7) * 16, *(const u32x4*)(stage + n * 16));
        }
    } else if (wv < 13) {
        const int n = blockIdx.x * 576 + (tid - 256);
        const int pc = n & 3, r = (n >> 2) & 31, sel = (n >> 7) & 1, hs = n >> 8, s = hs % KS, h = hs / KS;
        const int c = pc ^ (2 * ((r >> 2) & 1)), t = c >> 1, g = c & 1;
        const unsigned* w = (const unsigned*)(wqkv + (size_t)(32 * s + r) * (3 * D) + DH * h);
        u32x4 o = {0u, 0u, 0u, 0u};
        if (sel == 0 || t == 0) {
            const int col = (sel == 0 ? t * D : 2 * D) + 8 * g;
            o = (u32x4){w[col / 2], w[col / 2 + 1], w[col / 2 + 2], w[col / 2 + 3]};
        } else if (g == 0) {
            o = (u32x4){w[8], w[D / 2 + 8], w[D + 8], 0u};
        }
        gstore16p(ws + WS_WQP + (size_t)n * 16, o);
    } else if (tid < 256 + 576 + 162) {
        const int n = blockIdx.x * 162 + (tid - (256 + 576));
        const int lane = n & 63, fs = n >> 6, ks = fs % KS, dt = fs / KS;
        unsigned short v[8];
#pragma unroll
        for (int j = 0; j < 8; ++j) v[j] = wo[(size_t)wo_row(ks, lane >> 4, j) * D + 16 * dt + (lane & 15)];
        gstore16p(ws + WS_WOP + (size_t)n * 16, (u32x4){v[0] | ((unsigned)v[1] << 16), v[2] | ((unsigned)v[3] << 16), v[4] | ((unsigned)v[5] << 16), v[6] | ((unsigned)v[7] << 16)});
    }
}

constexpr int G1_SLOT = 9 * 4096;

DI int img_aoff(int lane) {
    const int kg = lane >> 4, q = (lane & 15) >> 2, p = lane & 3;
    return (4 * kg + q) * 64 + (((p >> 1) ^ (2 * (kg & 1))) * 16) + 8 * (p & 1);
}
DI void g1_ldx(bf16x8& d, const char* base, unsigned lo, int ks) {
    asm volatile("global_load_dwordx4 %0, %1, %2 offset:%3" : "=v"(d) : "v"(lo), "s"(base + (ks >> 2) * 4096), "n"((ks & 3) * 1024));
}
DI float glu(float g, float u) { return u * __builtin_amdgcn_rcpf(1.0f + __builtin_amdgcn_exp2f(g * -LOG2E)); }

constexpr int g1_group(int u) { return u < 4 ? (u & 1) : u / 2; }
constexpr int g1_half(int u) { return u < 4 ? (u >> 1) : (u & 1); }
constexpr int g1_dma_at(int u, int g) {
    if (u == 0) return (g >= 12 && g <= 20 && !(g & 1)) ? 1 * 8 + (g - 12) / 2 : (g >= 22 && g <= 30 && !(g & 1)) ? 2 * 8 + (g - 22) / 2 : -1;
    if (u == 1) return -1;
    if (u == 2) return (g <= 16 && !(g & 3)) ? 3 * 8 + g / 4 : (g >= 20 && g <= 28 && !(g & 1)) ? 4 * 8 + (g - 20) / 2 : -1;
    if (u <= 9) { const int sp = 5; return (g % sp == 0 && g / sp < 5) ? (u + 2) * 8 + g / sp : -1; }
    return -1;
}
constexpr int g1_x_at(int u, int g) {
    if (g & 3) return -1;
    if (u == 0) return g / 4 < 3 ? 6 + g / 4 : -1;
    if (u == 1) return 9 + g / 4;
    return -1;
}
constexpr int g1_x_younger(int ks, int u, int g) {
    int n = 0; bool seen = ks < 6;
    if (ks < 6) n += 2 * (5 - ks);
    for (int uu = 0; uu <= u; ++uu)
        for (int gg = 0; gg < 36 && (uu < u || gg <= g); ++gg) {
            const int x = g1_x_at(uu, gg);
            if (x >= 0) { if (seen) n += 2; if (x == ks) seen = true; }
            if (g1_dma_at(uu, gg) >= 0 && seen) n += 1;
        }
    return n;
}

struct G1Ctx {
    const char *wrow, *wq, *xs;
    char* hdst;
    unsigned lds0, dsto, voff, lo;
    int fbase, fsingle, w;
};
DI void g1_piece(const G1Ctx& c, int unit, int m) {
    const int grp = g1_group(unit), hk = g1_half(unit);
    const unsigned slot = c.lds0 + (unit & 3) * G1_SLOT;
    const int mm = (m == 4 && c.w >= 4) ? 3 : m;
    if (grp == 0) {
        const int pi = c.w + 8 * mm;
        glds16(c.wq + hk * G1_SLOT + pi * 1024, c.lo, slot + pi * 1024);
    } else {
        const int f0 = grp == 5 ? c.fsingle : c.fbase + 32 * (grp - 1);
        glds16(c.wrow + (size_t)(288 * hk + 64 * mm) * (F * 2) + f0 * 2, c.voff, slot + c.dsto + mm * 8192);
    }
}

struct G1Epi { float e[2], h[2]; u32x4 pk; };
DI void g1_epi_slice(int s, const f32x4 (&c)[2][2][2], G1Epi& t, char* hdst, unsigned lo) {
    const int v = s >> 2, nh = v >> 3, j = v & 7, ph = s & 3;
    if (ph == 0) { t.e[v & 1] = __builtin_amdgcn_exp2f(c[0][j >> 2][nh][j & 3] * -LOG2E); asm volatile("" : "+v"(t.e[v & 1])); }
    if (ph == 1) { t.e[v & 1] = __builtin_amdgcn_rcpf(1.0f + t.e[v & 1]); asm volatile("" : "+v"(t.e[v & 1])); }
    if (ph == 2) { t.h[v & 1] = c[1][j >> 2][nh][j & 3] * t.e[v & 1]; asm volatile("" : "+v"(t.h[v & 1])); }
    if (ph == 3 && (v & 1)) {
        t.pk[j >> 1] = cvtpk(t.h[0], t.h[1]);
        asm volatile("" : "+v"(t.pk[j >> 1]));
        if (j == 7) gstore16(hdst + (size_t)nh * FS * 1024, lo, t.pk);
    }
}

struct QkvEpi { char *qg, *kg; lds_ptr stg; float kmax; };
DI void qkv_slice(int k, const f32x4 (&c)[2][2][2], QkvEpi& e, unsigned lo) {
    typedef __attribute__((address_space(3))) u32x2* l64;
    typedef __attribute__((address_space(3))) unsigned* l32;
    typedef __attribute__((address_space(3))) unsigned short* l16;
    typedef __attribute__((address_space(3))) const u32x4* l128;
    asm volatile("" : "+v"(lo));
    const int lane = lo >> 4;
    const int t = lane & 15, g4 = lane >> 4;
    const lds_ptr rec = e.stg + QREC;
    if (k < 4) {
        const int nh = k >> 1, r = 16 * nh + t;
        const f32x4 ex = c[1][1][nh];
        if ((k & 1) == 0) {
            const unsigned fo = (r + 32 * (g4 >> 1)) * 16 + (g4 & 1) * 8;
            const f32x4 q = c[0][0][nh], kk = c[0][1][nh];
            *(l64)(e.stg + fo) = (u32x2){cvtpk(q[0] * QSCALE, q[1] * QSCALE), cvtpk(q[2] * QSCALE, q[3] * QSCALE)};
            *(l64)(rec + fo) = (u32x2){cvtpk(kk[0], kk[1]), cvtpk(kk[2], kk[3])};
            float n2 = g4 == 0 ? ex[2] * ex[2] + ex[3] * ex[3] : 0.f;
            n2 += (kk[0] * kk[0] + kk[1] * kk[1]) + (kk[2] * kk[2] + kk[3] * kk[3]);
            n2 = sum32x(sum16x(n2));
            e.kmax = nh ? fmaxf(e.kmax, n2) : n2;
            asm volatile("" : "+v"(e.kmax));
        } else {
            const f32x4 v = c[1][0][nh];
            *(l64)(rec + 1280 + nh * 640 + (g4 & 1) * 320 + t * 16 + (g4 >> 1) * 8) = (u32x2){cvtpk(v[0], v[1]), cvtpk(v[2], v[3])};
            const unsigned e01 = cvtpk(ex[0], ex[1]), e23 = cvtpk(ex[2], ex[3]);
            const bool g0 = g4 == 0, g1 = g4 == 1;
            const lds_ptr spare = e.stg + 1168;
            const lds_ptr vt = rec + 1280 + nh * 640 + ((t >> 2) & 1) * 320 + (4 * (t >> 3) + (t & 3)) * 2;
            *(l64)(g0 ? rec + 1024 + r * 8 : spare + 8) = (u32x2){e23, 0u};
            *(l32)(g0 ? e.stg + 1024 + r * 4 : spare + 4) = cvtpk(ex[0] * QSCALE, ex[1] * QSCALE);
            *(l16)(g0 ? spare : vt + (g1 ? 16 : 16 + g4) * 16) = (unsigned short)(g1 ? e01 : (g4 == 2 ? 0x3f80u : 0u));
            *(l16)(g1 ? vt + 17 * 16 : spare + 2) = (unsigned short)(e01 >> 16);
        }
    } else if (k == 4) {
        const float m = rowmax16(e.kmax);
        if (lane == 0) *(l32)(e.stg + 1152) = __float_as_uint(m);
    } else if (k == 5) {
        const unsigned o0 = lane * 16, oq = min(lane, 8) * 16 + 1024;
        const u32x4 d0 = *(l128)(e.stg + o0), d1 = *(l128)(e.stg + oq);
        gstore16(e.qg, o0, d0);
        gstore16(e.qg, oq, d1);
    } else {
        const unsigned o0 = lane * 16, ok = min(lane, 31) * 16 + 2048;
        const u32x4 d2 = *(l128)(rec + o0), d3 = *(l128)(rec + 1024 + o0), d4 = *(l128)(rec + ok);
        gstore16(e.kg, o0, d2);
        gstore16(e.kg, o0 + 1024, d3);
        gstore16(e.kg, ok, d4);
    }
}

struct G1Ring { bf16x4 lo[3], hi[3]; };
constexpr int g1_meet(int u) { return u == 1 ? 18 : u == 3 ? 10 : u == 10 ? 0 : 5; }
template <int U>
DI void g1_unit(const G1Ctx& cx, lds_cptr& b0, lds_cptr& b1, bool odd, G1Ring& fr, bf16x8 (&xb)[2][KS], f32x4 (&c)[2][2][2],
                const f32x4 (&cprev)[2][2][2], QkvEpi& qe) {
    constexpr int GRP = g1_group(U), HK = g1_half(U);
    constexpr bool PAIR = GRP != 5, NPAIR = g1_group(U + 1) != 5;
    constexpr int PER = PAIR ? 4 : 2, N = 9 * PER;
    constexpr bool EPI = U >= 4;
    auto addr = [&](int i) {
        const bool nx = i >= N, pr = nx ? NPAIR : PAIR;
        const int j = nx ? i - N : i, ks = pr ? j >> 2 : j >> 1, sel = pr ? (j >> 1) & 1 : j & 1, t = pr ? j & 1 : 0;
        return (t ? b1 : b0) + ks * 4096 + sel * 2048;
    };
    G1Epi ep;
    char* hprev = cx.hdst + (U / 2 - 2) * 1024;
    static_for<0, N>([&](auto ic) {
        constexpr int i = decltype(ic)::value;
        constexpr int kl = i / PER, ks = 9 * HK + kl, sel = PAIR ? (i >> 1) & 1 : i & 1, t = PAIR ? i & 1 : 0;
        constexpr int g = PAIR ? i : 2 * i;
        constexpr int x = g1_x_at(U, g), dm = g1_dma_at(U, g);
        constexpr bool more = U < 11 || i + 2 < N;
        if constexpr (i == N - 2 && U < 11) {
            asm volatile("s_waitcnt vmcnt(%0) lgkmcnt(0)\n\ts_barrier" :: "n"(g1_meet(U)) : "memory");
            constexpr int delta = (((U + 1) & 3) - (U & 3)) * G1_SLOT;
            if constexpr (U == 9) b0 = odd ? b1 : b0;
            b0 += delta; b1 += delta;
            asm volatile("" : "+v"(b0), "+v"(b1));
        }
        if constexpr (x >= 0) { g1_ldx(xb[0][x], cx.xs, cx.lo, x); g1_ldx(xb[1][x], cx.xs + KS * 1024, cx.lo, x); }
        if constexpr (dm >= 0) g1_piece(cx, dm >> 3, dm & 7);
        if constexpr ((U == 0 || U == 2) && (i & 3) == 0) {
            constexpr int young = g1_x_younger(ks, U, g);
            asm volatile("s_waitcnt vmcnt(%2)" : "+v"(xb[0][ks]), "+v"(xb[1][ks]) : "n"(young));
        }
        constexpr bool SWAP = GRP == 0 && sel == 1 && t == 0;
        const bf16x8 a = join(fr.lo[i % 3], fr.hi[i % 3]);
        if constexpr (ks == 0) { if constexpr (SWAP) AMF_NEW(c[sel][t][0], xb[0][ks], a); else AMF_NEW(c[sel][t][0], a, xb[0][ks]); }
        else { if constexpr (SWAP) AMF_ACC(c[sel][t][0], xb[0][ks], a); else AMF_ACC(c[sel][t][0], a, xb[0][ks]); }
        SBAR();
        if constexpr (more) fr.lo[(i + 2) % 3] = trread(addr(i + 2));
        SBAR();
        if constexpr (ks == 0) { if constexpr (SWAP) AMF_NEW(c[sel][t][1], xb[1][ks], a); else AMF_NEW(c[sel][t][1], a, xb[1][ks]); }
        else { if constexpr (SWAP) AMF_ACC(c[sel][t][1], xb[1][ks], a); else AMF_ACC(c[sel][t][1], a, xb[1][ks]); }
        SBAR();
        if constexpr (more) fr.hi[(i + 2) % 3] = trread(addr(i + 2) + 1024);
        SBAR();
        if constexpr (U == 3 && (i & 3) == 2 && i / 4 < 7) qkv_slice(i / 4, cprev, qe, cx.lo);
        if constexpr (EPI && PAIR) {
            constexpr int sl = 36 * HK + i - 4;
            if constexpr (sl >= 0 && sl < 64) g1_epi_slice(sl, cprev, ep, hprev, cx.lo);
        } else if constexpr (EPI) {
            constexpr int sl = 2 * (18 * HK + i - 2);
            if constexpr (sl >= 0 && sl < 64) { g1_epi_slice(sl, cprev, ep, hprev, cx.lo); g1_epi_slice(sl + 1, cprev, ep, hprev, cx.lo); }
        }
        SBAR();
    });
}

__global__ __launch_bounds__(512, 2) void k_gemm1(const char* wg, const char* wu, char* ws) {
    extern __shared__ __attribute__((aligned(16))) char lds[];
    diag_stamp(ws, 1, 0);

    const int tid = threadIdx.x, lane = tid & 63;
    const int w = __builtin_amdgcn_readfirstlane(tid >> 6);
    const int xcd = blockIdx.x & 7, idx = blockIdx.x >> 3;
    const int cg = 8 * (xcd >> 1) + (idx & 7), rg = 4 * (xcd & 1) + (idx >> 3), tb = 8 * rg + w;

    const int odd = cg & 1;
    G1Ctx cx;
    cx.w = w; cx.lds0 = (unsigned)(uintptr_t)lds; cx.lo = lane * 16;
    cx.fbase = 144 * cg + 16 * odd; cx.fsingle = odd ? 144 * cg - 16 : 144 * cg + 128;
    cx.wq = ws + WS_WQP + (size_t)cg * KS * 4096;
    cx.xs = ws + WS_XNF + (size_t)(2 * tb) * KS * 1024;
    cx.wrow = ((w & 1) ? wu : wg) + (size_t)(32 * (w >> 2) + 16 * ((w >> 1) & 1)) * (F * 2);
    cx.dsto = (w >> 2) * 4096 + (w & 1) * 2048 + ((w >> 1) & 1) * 1024;
    cx.voff = (lane >> 2) * (F * 2) + (((lane & 3) ^ (2 * ((lane >> 4) & 1))) * 16);
    cx.hdst = ws + WS_HF + ((size_t)(2 * tb) * FS + cx.fbase / 32) * 1024;

#pragma unroll
    for (int m = 0; m < 5; ++m) g1_piece(cx, 0, m);
    bf16x8 xb[2][KS];
#pragma unroll
    for (int s = 0; s < 6; ++s) { g1_ldx(xb[0][s], cx.xs, cx.lo, s); g1_ldx(xb[1][s], cx.xs + KS * 1024, cx.lo, s); }

    const int aoff0 = img_aoff(lane);
    lds_cptr b0 = (lds_cptr)lds + aoff0, b1 = (lds_cptr)lds + (aoff0 ^ 32);
    asm volatile("" : "+v"(b0), "+v"(b1));
    f32x4 ca[2][2][2], cb[2][2][2];
    QkvEpi qe;
    qe.qg = ws + WS_Q + ((size_t)cg * NTB + tb) * QREC;
    qe.kg = ws + WS_KV + ((size_t)cg * NTB + tb) * KVREC;
    qe.stg = (lds_ptr)lds + 2 * G1_SLOT + w * 4608;

    WAITV_BAR(12);

    G1Ring fr;
#pragma unroll
    for (int i = 0; i < 2; ++i) {
        const lds_cptr a = (i & 1) ? b1 : b0;
        fr.lo[i] = trread(a); fr.hi[i] = trread(a + 1024);
    }
    g1_unit<0>(cx, b0, b1, odd, fr, xb, ca, cb, qe);
    g1_unit<1>(cx, b0, b1, odd, fr, xb, cb, ca, qe);
    g1_unit<2>(cx, b0, b1, odd, fr, xb, ca, cb, qe);
    g1_unit<3>(cx, b0, b1, odd, fr, xb, cb, ca, qe);
    g1_unit<4>(cx, b0, b1, odd, fr, xb, ca, cb, qe);
    g1_unit<5>(cx, b0, b1, odd, fr, xb, ca, cb, qe);
    g1_unit<6>(cx, b0, b1, odd, fr, xb, cb, ca, qe);
    g1_unit<7>(cx, b0, b1, odd, fr, xb, cb, ca, qe);
    g1_unit<8>(cx, b0, b1, odd, fr, xb, ca, cb, qe);
    g1_unit<9>(cx, b0, b1, odd, fr, xb, ca, cb, qe);
    g1_unit<10>(cx, b0, b1, odd, fr, xb, cb, ca, qe);
    g1_unit<11>(cx, b0, b1, odd, fr, xb, cb, ca, qe);
    asm volatile("s_nop 7\n\ts_nop 3");
    SBAR();
    {
        char* hs = ws + WS_HF + ((size_t)(2 * tb) * FS + cx.fsingle / 32) * 1024 + odd * 8;
#pragma unroll
        for (int nh = 0; nh < 2; ++nh) {
            float h[4];
#pragma unroll
            for (int j = 0; j < 4; ++j) h[j] = glu(cb[0][0][nh][j], cb[1][0][nh][j]);
            gstore8(hs + (size_t)nh * FS * 1024, cx.lo, (u32x2){cvtpk(h[0], h[1]), cvtpk(h[2], h[3])});
        }
    }
}

constexpr int AT_STAGE = 8 * KVREC;
constexpr float AT_BOUND_MAX = 50.0f;

struct KFrag { bf16x8 k1; bf16x4 k2; };
struct VFrag { bf16x8 v0, v1; };
DI KFrag at_readk(lds_cptr rec, int lane) {
    KFrag k;
    k.k1 = *(const __attribute__((address_space(3))) bf16x8*)(rec + lane * 16);
    k.k2 = *(const __attribute__((address_space(3))) bf16x4*)(rec + 1024 + (lane & 31) * 8);
    return k;
}
DI VFrag at_readv(lds_cptr recv) {
    VFrag v;
    v.v0 = *(const __attribute__((address_space(3))) bf16x8*)(recv);
    v.v1 = *(const __attribute__((address_space(3))) bf16x8*)(recv + 640);
    return v;
}
DI bf16x8 packp(const f32x16& p, int s) {
    return __builtin_bit_cast(bf16x8, (u32x4){cvtpk(p[8 * s], p[8 * s + 1]), cvtpk(p[8 * s + 2], p[8 * s + 3]), cvtpk(p[8 * s + 4], p[8 * s + 5]), cvtpk(p[8 * s + 6], p[8 * s + 7])});
}

DI void at_dma(const char* kvh, int stage, unsigned ldsb, int w, int lane) {
    const char* src = kvh + (size_t)stage * AT_STAGE + w * 5 * 1024;
    const unsigned dst = ldsb + w * 5 * 1024, lo = lane * 16;
    asm volatile("s_mov_b32 m0, %2\n\ts_nop 0\n\t"
                 "global_load_lds_dwordx4 %0, %1\n\tglobal_load_lds_dwordx4 %0, %1 offset:1024\n\t"
                 "global_load_lds_dwordx4 %0, %1 offset:2048\n\tglobal_load_lds_dwordx4 %0, %1 offset:3072"
                 :: "v"(lo), "s"(src), "s"(dst) : "memory");
    glds16(src + 4096, lo, dst + 4096);
}

#define AMFMA16_NEW(d, a, b, c) asm volatile("v_mfma_f32_32x32x16_bf16 %0, %1, %2, %3" : "=&v"(d) : "v"(a), "v"(b), "v"(c))
#define AMFMA16_ACC(d, a, b) asm volatile("v_mfma_f32_32x32x16_bf16 %0, %1, %2, %0" : "+v"(d) : "v"(a), "v"(b))
#define AMFMA8_ACC(d, a, b) asm volatile("v_mfma_f32_32x32x8_bf16 %0, %1, %2, %0" : "+v"(d) : "v"(a), "v"(b))
#define AT_EXP4(E, SX, a) do { _Pragma("unroll") for (int i_ = 0; i_ < 4; ++i_) asm volatile("v_exp_f32 %0, %1" : "=v"(E[i_]) : "v"(SX[a + i_])); } while (0)
#define AT_PK2(DST, k, E) do { asm volatile("v_cvt_pk_bf16_f32 %0, %1, %2" : "=v"(DST[k]) : "v"(E[0]), "v"(E[1]));   \
                               asm volatile("v_cvt_pk_bf16_f32 %0, %1, %2" : "=v"(DST[k + 1]) : "v"(E[2]), "v"(E[3])); } while (0)
#define AT_STEP(SX, PX0, PX1, SY, CY, QY1, QY2, KF, OY, PY0, PY1, VF, LOADS)                                   \
    do {                                                                                                       \
        float e0_[4], e1_[4], e2_[4];                                                                          \
        AMFMA16_NEW(SY, KF.k1, QY1, CY);                                                                       \
        LOADS;                                                                                                 \
        SBAR();                                                                                                \
        AT_EXP4(e0_, SX, 0);                                                                                   \
        AT_PK2(PY1, 2, etail);                                                                                 \
        AMFMA8_ACC(SY, KF.k2, QY2);                                                                            \
        AT_EXP4(e1_, SX, 4);                                                                                   \
        AT_PK2(PX0, 0, e0_);                                                                                   \
        AMFMA16_ACC(OY, VF.v0, PY0);                                                                           \
        AT_EXP4(e2_, SX, 8);                                                                                   \
        AT_PK2(PX0, 2, e1_);                                                                                   \
        AMFMA16_ACC(OY, VF.v1, PY1);                                                                           \
        AT_EXP4(etail, SX, 12);                                                                                \
        AT_PK2(PX1, 0, e2_);                                                                                   \
        SBAR();                                                                                                \
    } while (0)

DI void at_online(const KFrag& k, const VFrag& v, bf16x8 q1, bf16x4 q2, float& m, f32x16& o) {
    f32x16 z = {};
    f32x16 s = MFMA16(k.k1, q1, z);
    s = MFMA8(k.k2, q2, s);
    float t = s[0];
#pragma unroll
    for (int i = 1; i < 16; ++i) t = fmaxf(t, s[i]);
    t = fmaxf(t, swap32(t));
    const float mn = fmaxf(m, t);
    const float alpha = __builtin_amdgcn_exp2f(m - mn);
    m = mn;
#pragma unroll
    for (int i = 0; i < 16; ++i) { s[i] = __builtin_amdgcn_exp2f(s[i] - mn); o[i] *= alpha; }
    o = MFMA16(v.v0, packp(s, 0), o);
    o = MFMA16(v.v1, packp(s, 1), o);
}

DI void at_store(const f32x16& o, char* ws, int h, int tb, int lane) {
    const int r = lane & 31, hi = lane >> 5;
    const float other = swap32(o[10]);
    const float inv = 1.0f / (hi ? other : o[10]);
    char* of = ws + WS_OF + (size_t)(2 * tb + (r >> 4)) * KS * 1024;
    gstore16p(of + (h >> 1) * 1024 + ((r & 15) + 16 * (2 * (h & 1) + hi)) * 16,
              (u32x4){cvtpk(o[0] * inv, o[1] * inv), cvtpk(o[2] * inv, o[3] * inv), cvtpk(o[4] * inv, o[5] * inv), cvtpk(o[6] * inv, o[7] * inv)});
    if (hi == 0) *(unsigned*)(of + (16 + (h >> 4)) * 1024 + ((r & 15) + 16 * ((h >> 2) & 3)) * 16 + (h & 3) * 4) = cvtpk(o[8] * inv, o[9] * inv);
}

DI float at_qnorm2(bf16x8 q1, bf16x4 q2) {
    const u32x4 a = __builtin_bit_cast(u32x4, q1); const u32x2 b = __builtin_bit_cast(u32x2, q2);
    float n = bf_lo(b[0]) * bf_lo(b[0]) + bf_hi(b[0]) * bf_hi(b[0]);
#pragma unroll
    for (int j = 0; j < 4; ++j) n += bf_lo(a[j]) * bf_lo(a[j]) + bf_hi(a[j]) * bf_hi(a[j]);
    return sum32x(n);
}

DI void attn_block(char* ws, char* lds, int bid) {
    const int tid = threadIdx.x, lane = tid & 63, hi = lane >> 5;
    const int w = __builtin_amdgcn_readfirstlane(tid >> 6);
    const int xcd = bid & 7, idx = bid >> 3;
    const int h = 4 * xcd + (idx & 3), qg = idx >> 2;
    const int tbA = 8 * qg + 2 * w, tbB = tbA + 1;
    const unsigned lds0 = (unsigned)(uintptr_t)lds;
    const char* kvh = ws + WS_KV + (size_t)h * NTB * KVREC;

    bf16x8 qA1, qB1; unsigned qa2, qb2; float km;
    {
        const char* q1 = ws + WS_Q + ((size_t)h * NTB + tbA) * QREC;
        const char* kx = ws + WS_Q + (size_t)h * NTB * QREC + 1152;
        const unsigned l16 = lane * 16, l4 = (lane & 31) * 4 + 1024, lk = lane * QREC;
        asm volatile("global_load_dword %0, %1, %2" : "=v"(km) : "v"(lk), "s"(kx));
        asm volatile("global_load_dwordx4 %0, %1, %2" : "=v"(qA1) : "v"(l16), "s"(q1));
        asm volatile("global_load_dwordx4 %0, %1, %2 offset:%3" : "=v"(qB1) : "v"(l16), "s"(q1), "n"(QREC));
        asm volatile("global_load_dword %0, %1, %2" : "=v"(qa2) : "v"(l4), "s"(q1));
        asm volatile("global_load_dword %0, %1, %2 offset:%3" : "=v"(qb2) : "v"(l4), "s"(q1), "n"(QREC));
    }
    at_dma(kvh, 0, lds0, w, lane);
    at_dma(kvh, 1, lds0 + AT_STAGE, w, lane);
    asm volatile("s_waitcnt vmcnt(10)" : "+v"(km), "+v"(qA1), "+v"(qB1), "+v"(qa2), "+v"(qb2));
    const bf16x4 qA2 = __builtin_bit_cast(bf16x4, (u32x2){hi ? 0u : qa2, 0u});
    const bf16x4 qB2 = __builtin_bit_cast(bf16x4, (u32x2){hi ? 0u : qb2, 0u});
    km = rowmax16(km);
    km = max32x(max16x(km));
    const float mA = __builtin_amdgcn_sqrtf(at_qnorm2(qA1, qA2) * km) * 1.01f, mB = __builtin_amdgcn_sqrtf(at_qnorm2(qB1, qB2) * km) * 1.01f;
    const bool fast = __all(mA <= AT_BOUND_MAX && mB <= AT_BOUND_MAX);

    const int voffs = 1280 + hi * 320 + min(lane & 31, 19) * 16;
    const lds_cptr L = (lds_cptr)lds;
    f32x16 oA = {}, oB = {};

    if (fast) {
        f32x16 cA, cB;
#pragma unroll
        for (int i = 0; i < 16; ++i) { cA[i] = -mA; cB[i] = -mB; }
        asm volatile("" : "+v"(cA), "+v"(cB));
        f32x16 sA, sB;
        u32x4 pA0 = {}, pA1 = {}, pB0 = {}, pB1 = {};
        float etail[4] = {0.f, 0.f, 0.f, 0.f};
        KFrag k0, k1; VFrag v0, v1;
        WAITV_BAR(5);
        k0 = at_readk(L, lane);
        v1 = at_readv(L + voffs);
        sA = MFMA16(k0.k1, qA1, cA); sA = MFMA8(k0.k2, qA2, sA);
        asm volatile("s_nop 15" : "+v"(sA));
        int slot = 0;
        for (int st = 0; st < 8; ++st) {
            const lds_cptr cur = L + slot * AT_STAGE;
            const int nslot = slot == 2 ? 0 : slot + 1;
#pragma unroll
            for (int t = 0; t < 8; t += 2) {
                AT_STEP(sA, pA0, pA1, sB, cB, qB1, qB2, k0, oB, pB0, pB1, v1,
                        (k1 = at_readk(cur + (t + 1) * KVREC, lane), v0 = at_readv(cur + t * KVREC + voffs)));
                AT_STEP(sB, pB0, pB1, sA, cA, qA1, qA2, k1, oA, pA0, pA1, v0, (void)0);
                lds_cptr nk = cur + (t + 2) * KVREC;
                if (t == 6) {
                    if (st < 7) {
                        if (st < 6) { WAITV_BAR(0); at_dma(kvh, st + 2, lds0 + (nslot == 2 ? 0 : nslot + 1) * AT_STAGE, w, lane); }
                        else WAITV_BAR(0);
                        nk = L + nslot * AT_STAGE;
                    } else nk = cur;
                }
                AT_STEP(sA, pA0, pA1, sB, cB, qB1, qB2, k1, oB, pB0, pB1, v0,
                        (k0 = at_readk(nk, lane), v1 = at_readv(cur + (t + 1) * KVREC + voffs)));
                AT_STEP(sB, pB0, pB1, sA, cA, qA1, qA2, k0, oA, pA0, pA1, v1, (void)0);
            }
            slot = nslot;
        }
        asm volatile("s_nop 1");
        AT_PK2(pB1, 2, etail);
        asm volatile("s_nop 1" : "+v"(pB1));
        AMFMA16_ACC(oB, v1.v0, pB0);
        AMFMA16_ACC(oB, v1.v1, pB1);
        asm volatile("s_nop 15\n\ts_nop 7" : "+v"(oA), "+v"(oB));
        SBAR();
    } else {
        float mxA = -INFINITY, mxB = -INFINITY;
        WAITV_BAR(5);
        int slot = 0;
        for (int st = 0; st < 8; ++st) {
            const lds_cptr cur = L + slot * AT_STAGE;
            const int nslot = slot == 2 ? 0 : slot + 1;
            for (int t = 0; t < 8; ++t) {
                const KFrag k = at_readk(cur + t * KVREC, lane);
                const VFrag v = at_readv(cur + t * KVREC + voffs);
                at_online(k, v, qA1, qA2, mxA, oA);
                at_online(k, v, qB1, qB2, mxB, oB);
            }
            if (st < 7) {
                WAITV_BAR(0);
                if (st < 6) at_dma(kvh, st + 2, lds0 + (nslot == 2 ? 0 : nslot + 1) * AT_STAGE, w, lane);
            }
            slot = nslot;
        }
    }
    at_store(oA, ws, h, tbA, lane);
    at_store(oB, ws, h, tbB, lane);
}

constexpr int DN_STEP = 32 * 288, DN_SLOT = 2 * DN_STEP, DN_LDS = 4 * DN_SLOT, DN_NST = 18;
constexpr int dn_piece(int st, int i) {
    if ((i == 3 || i == 7 || i == 11) && st + 3 < DN_NST) return (st + 3) * 8 + 2 + (i - 3) / 4;
    if ((i == 15 || i == 17) && st + 4 < DN_NST) return (st + 4) * 8 + (i - 15) / 2;
    return -1;
}
constexpr int dn_young(int st) {
    int n = 0; bool seen = st + 1 == 1;
    const int need = st + 1;
    for (int s2 = 0; s2 <= st; ++s2)
        for (int i = 0; i < 18; ++i) {
            if (s2 == st && i >= 15) break;
            if (i == 0 && s2 + 2 < DN_NST) { if (seen) n += 4; if (s2 + 2 == need) { seen = true; n = 0; } }
            const int pc = dn_piece(s2, i);
            if (pc >= 0) { if (seen) n += 1; if ((pc >> 3) == need) { seen = true; n = 0; } }
        }
    return n;
}
DI void dn_ldb(bf16x8 (&b)[2][2], const char* hs, unsigned lo, int st) {
#pragma unroll
    for (int k = 0; k < 2; ++k) {
        asm volatile("global_load_dwordx4 %0, %1, %2 offset:%3" : "=v"(b[0][k]) : "v"(lo), "s"(hs + (size_t)st * 2048), "n"(k * 1024));
        asm volatile("global_load_dwordx4 %0, %1, %2 offset:%3" : "=v"(b[1][k]) : "v"(lo), "s"(hs + (size_t)FS * 1024 + (size_t)st * 2048), "n"(k * 1024));
    }
}
template <int N> DI void dn_wait(bf16x8 (&b)[2][2]) {
    asm volatile("s_waitcnt vmcnt(%4) lgkmcnt(0)\n\ts_barrier" : "+v"(b[0][0]), "+v"(b[0][1]), "+v"(b[1][0]), "+v"(b[1][1]) : "n"(N) : "memory");
}

DI void down_block(const char* wd, char* ws, char* lds, int bid) {
    const int tid = threadIdx.x, lane = tid & 63;
    const int w = __builtin_amdgcn_readfirstlane(tid >> 6);
    const int xcd = bid & 7, idx = bid >> 3;
    const int kq = xcd >> 1, tg = (xcd & 1) * 8 + (idx >> 2), dq = idx & 3;
    const int tb16 = 8 * tg + 2 * w, fs0 = 36 * kq;
    const unsigned lds0 = (unsigned)(uintptr_t)lds;
    unsigned voff[5], doff[5];
#pragma unroll
    for (int m = 0; m < 5; ++m) { const int i = min(5 * (w & 1) + m, 8), n = 64 * i + lane; voff[m] = (n / 18) * (D * 2) + (n % 18) * 16; doff[m] = (w >> 1) * DN_STEP + i * 1024; }
    const char* wsrc = wd + (size_t)(fs0 + (w >> 1)) * 32 * (D * 2) + dq * 288;
    const lds_cptr A = (lds_cptr)lds + (4 * (lane >> 4) + ((lane & 15) >> 2)) * 288 + (lane & 3) * 8;
    const char* hs = ws + WS_HF + ((size_t)tb16 * FS + fs0) * 1024;
    const unsigned lo = lane * 16;

    auto dma = [&](int st, int m) {
        const char* base = wsrc;
        asm volatile("" : "+s"(base));
        glds16(base + (size_t)st * 2 * 32 * (D * 2), voff[m], lds0 + (st & 3) * DN_SLOT + doff[m]);
    };
#pragma unroll
    for (int st = 0; st < 3; ++st)
#pragma unroll
        for (int m = 0; m < 5; ++m) dma(st, m);
    dma(3, 0); dma(3, 1);
    f32x4 acc[9][2];
    bf16x8 b[3][2][2];
    dn_ldb(b[0], hs, lo, 0);
    dn_ldb(b[1], hs, lo, 1);
    lds_cptr Alo = A, Ahi = A + 2 * DN_SLOT;
    asm volatile("" : "+v"(Alo), "+v"(Ahi));
    auto addr = [&](int st, int i) { return ((st & 2) ? Ahi : Alo) + (st & 1) * DN_SLOT + (i / 9) * DN_STEP + (i % 9) * 32; };
    dn_wait<4>(b[0]);
    bf16x4 flo[4], fhi[4];
#pragma unroll
    for (int i = 0; i < 3; ++i) { flo[i] = trread(addr(0, i)); fhi[i] = trread(addr(0, i) + 16 * 288); }
    static_for<0, DN_NST * 18>([&](auto nc) {
        constexpr int n = decltype(nc)::value, st = n / 18, i = n % 18, set = st % 3;
        if constexpr (i == 0 && st + 2 < DN_NST) dn_ldb(b[(st + 2) % 3], hs, lo, st + 2);
        if constexpr (i == 15 && st + 1 < DN_NST) dn_wait<dn_young(st)>(b[(st + 1) % 3]);
        if constexpr (dn_piece(st, i) >= 0) dma(dn_piece(st, i) >> 3, dn_piece(st, i) & 7);
        constexpr bool more = n + 3 < DN_NST * 18;
        constexpr int nst = (n + 3) / 18, ni = (n + 3) % 18;
        const bf16x8 fr = join(flo[n & 3], fhi[n & 3]);
        if constexpr (n < 9) AMF_NEW(acc[i % 9][0], fr, b[set][0][i / 9]); else AMF_ACC(acc[i % 9][0], fr, b[set][0][i / 9]);
        SBAR();
        if constexpr (more) flo[(n + 3) & 3] = trread(addr(nst, ni));
        SBAR();
        if constexpr (n < 9) AMF_NEW(acc[i % 9][1], fr, b[set][1][i / 9]); else AMF_ACC(acc[i % 9][1], fr, b[set][1][i / 9]);
        SBAR();
        if constexpr (more) fhi[(n + 3) & 3] = trread(addr(nst, ni) + 16 * 288);
        SBAR();
    });

    asm volatile("s_nop 7\n\ts_nop 3");
    SBAR();
    char* dst = ws + WS_PART + (((size_t)kq * NT16 + tb16) * 36 + 9 * dq) * PTILE;
#pragma unroll
    for (int nh = 0; nh < 2; ++nh)
#pragma unroll
        for (int i = 0; i < 9; ++i) gstore12(dst + (size_t)nh * 36 * PTILE + i * PTILE, lane * 12, pack24(acc[i][nh]));
}

constexpr int MIX_LDS = DN_LDS;
static_assert(MIX_LDS >= 3 * AT_STAGE && 2 * MIX_LDS <= 160 * 1024, "two workgroups per CU");
__global__ __launch_bounds__(256, 2) void k_mix(const char* wd, char* ws) {
    extern __shared__ __attribute__((aligned(16))) char lds[];

    const int b = blockIdx.x;
    diag_stamp(ws, 2, 0);
    if (b < 256) attn_block(ws, lds, b); else down_block(wd, ws, lds, b - 256);
    diag_stamp(ws, 3, 0); diag_stamp(ws, 4, 256);
}

constexpr int FN_A = 3 * KS * 1024, FN_LDS = FN_A + 8 * 3 * 1024;
DI void ldx1(bf16x8& d, const char* base, unsigned lo, int off) {
    asm volatile("global_load_dwordx4 %0, %1, %2 offset:%3" : "=v"(d) : "v"(lo), "s"(base), "n"(off));
}
template <int N> DI void waitx(bf16x8& a, bf16x8& b) { asm volatile("s_waitcnt vmcnt(%2)" : "+v"(a), "+v"(b) : "n"(N)); }
DI void fn_ldpx(u32x3 (&pq)[3][4], u32x2 (&xv)[3], const char* pp, unsigned l12, const char* xp, unsigned xo) {
#pragma unroll
    for (int t = 0; t < 3; ++t) {
#pragma unroll
        for (int k = 0; k < 4; ++k) asm volatile("global_load_dwordx3 %0, %1, %2 offset:%3" : "=v"(pq[t][k]) : "v"(l12), "s"(pp + (size_t)k * NT16 * 36 * PTILE), "n"(t * PTILE));
        asm volatile("global_load_dwordx2 %0, %1, %2 offset:%3" : "=v"(xv[t]) : "v"(xo), "s"(xp), "n"(t * 32));
    }
}
__global__ __launch_bounds__(512, 1) void k_final(const unsigned short* x, char* ws, unsigned short* out) {
    extern __shared__ __attribute__((aligned(16))) char lds[];
    const unsigned long long t5 = __builtin_amdgcn_s_memrealtime();

    const int tid = threadIdx.x, lane = tid & 63;
    const int w = __builtin_amdgcn_readfirstlane(tid >> 6);
    const int xcd = blockIdx.x & 7, idx = blockIdx.x >> 3;
    const int tg = 2 * xcd + idx / 12, dg = idx % 12, kh = w >> 2, tb16 = 8 * tg + 2 * (w & 3);
    const unsigned lds0 = (unsigned)(uintptr_t)lds, lo = lane * 16;

    {
        const char* src = ws + WS_WOP + (size_t)dg * FN_A;
#pragma unroll
        for (int i = 0; i < 7; ++i) { const int n = min(w * 7 + i, 3 * KS - 1); glds16(src + n * 1024, lo, lds0 + n * 1024); }
    }
    const char* os = ws + WS_OF + ((size_t)tb16 * KS + 9 * kh) * 1024;
    bf16x8 b[2][9];
    auto ldo = [&](int s) { ldx1(b[0][s], os + (s >> 2) * 4096, lo, (s & 3) * 1024); ldx1(b[1][s], os + KS * 1024 + (s >> 2) * 4096, lo, (s & 3) * 1024); };
#pragma unroll
    for (int s = 0; s < 4; ++s) ldo(s);

    asm volatile("s_waitcnt vmcnt(8)\n\ts_barrier" ::: "memory");

    const int tok = (tb16 + kh) * 16 + (lane & 15);
    const char* xp = (const char*)(x + (size_t)(tb16 + kh) * 16 * D + dg * 48);
    const unsigned xo2 = ((lane & 15) * D + 4 * (lane >> 4)) * 2;
    u32x2 xv[3];
    u32x3 pq[3][4];
    const char* pp = ws + WS_PART + ((size_t)(tb16 + kh) * 36 + 3 * dg) * PTILE;
    const lds_cptr A = (lds_cptr)lds + lo + 9 * kh * 1024;
    f32x4 acc[2][3] = {};
    static_for<0, 9>([&](auto sc) {
        constexpr int s = decltype(sc)::value;
        bf16x8 a[3];
#pragma unroll
        for (int t = 0; t < 3; ++t) a[t] = *(const __attribute__((address_space(3))) bf16x8*)(A + (t * KS + s) * 1024);
        if constexpr (s + 4 < 9) ldo(s + 4);
        if constexpr (s == 4) fn_ldpx(pq, xv, pp, lane * 12, xp, xo2);
        waitx<2 * ((s + 4 < 9 ? s + 4 : 8) - s) + (s >= 4 ? 15 : 0)>(b[0][s], b[1][s]);
#pragma unroll
        for (int t = 0; t < 3; ++t) {
            acc[0][t] = MFMA32K(a[t], b[0][s], acc[0][t]);
            acc[1][t] = MFMA32K(a[t], b[1][s], acc[1][t]);
        }
        SBAR();
    });

    typedef __attribute__((address_space(3))) f32x4* l128;
    const lds_ptr xo = (lds_ptr)lds + FN_A + lo;
#pragma unroll
    for (int t = 0; t < 3; ++t) *(l128)(xo + (w * 3 + t) * 1024) = kh ? acc[0][t] : acc[1][t];
    asm volatile("s_waitcnt lgkmcnt(0)\n\ts_barrier" ::: "memory");
    f32x4 sum[3];
#pragma unroll
    for (int t = 0; t < 3; ++t) {
        const f32x4 other = *(l128)(xo + ((w ^ 4) * 3 + t) * 1024);
        sum[t] = kh ? other + acc[1][t] : acc[0][t] + other;
    }
    asm volatile("s_waitcnt vmcnt(0)" : "+v"(xv[0]), "+v"(xv[1]), "+v"(xv[2]));
#pragma unroll
    for (int t = 0; t < 3; ++t) {
        asm volatile("" : "+v"(pq[t][0]), "+v"(pq[t][1]), "+v"(pq[t][2]), "+v"(pq[t][3]));
        const f32x4 ffn = (unpack24(pq[t][0]) + unpack24(pq[t][1])) + (unpack24(pq[t][2]) + unpack24(pq[t][3]));
        float o[4];
#pragma unroll
        for (int c = 0; c < 4; ++c) {
            const float f = rbf(ffn[c]);
            const float g = rbf(f * __builtin_amdgcn_rcpf(1.0f + __builtin_amdgcn_exp2f(f * -LOG2E)));
            const float xx = (c & 1) ? bf_hi(xv[t][c >> 1]) : bf_lo(xv[t][c >> 1]);
            o[c] = xx + rbf(rbf(sum[t][c]) * g);
        }
        *(u32x2*)(out + (size_t)tok * D + dg * 48 + 16 * t + 4 * (lane >> 4)) = (u32x2){cvtpk(o[0], o[1]), cvtpk(o[2], o[3])};
    }
    if (blockIdx.x == 0 && threadIdx.x < 64 && DIAG > 0) {
        const unsigned long long* st = (const unsigned long long*)(ws + WS_DIAG);
        const unsigned long long t6 = __builtin_amdgcn_s_memrealtime();
        unsigned long long dl = DIAG == 1 ? st[2] - st[1] : DIAG == 2 ? st[3] - st[2] : DIAG == 3 ? st[4] - st[2] : DIAG == 4 ? t5 - st[2] : t6 - t5;
        if (dl > 5000) dl = 5000;
        while (__builtin_amdgcn_s_memrealtime() - t6 < 10 * dl) __builtin_amdgcn_s_sleep(8);
    }
}

extern "C" void kernel_launch(void* const* d_in, const int* in_sizes, int n_in, void* d_out, int out_size, void* d_ws, size_t ws_size, hipStream_t stream) {
    const unsigned short* x = (const unsigned short*)d_in[0];
    const unsigned short* wqkv = (const unsigned short*)d_in[1];
    const unsigned short* wo = (const unsigned short*)d_in[2];
    const char* wg = (const char*)d_in[4];
    const char* wu = (const char*)d_in[5];
    const char* wd = (const char*)d_in[6];
    char* ws = (char*)d_ws;
    (void)hipFuncSetAttribute((const void*)k_gemm1, hipFuncAttributeMaxDynamicSharedMemorySize, 4 * G1_SLOT);
    (void)hipFuncSetAttribute((const void*)k_mix, hipFuncAttributeMaxDynamicSharedMemorySize, MIX_LDS);
    (void)hipFuncSetAttribute((const void*)k_final, hipFuncAttributeMaxDynamicSharedMemorySize, FN_LDS);
    k_prep<<<256, 1024, 0, stream>>>(x, wqkv, wo, ws);
    k_gemm1<<<256, 512, 4 * G1_SLOT, stream>>>(wg, wu, ws);
    k_mix<<<512, 256, MIX_LDS, stream>>>(wd, ws);
    k_final<<<192, 512, FN_LDS, stream>>>(x, ws, (unsigned short*)d_out);
}
```

```cpp
#include <hip/hip_runtime.h>
#include <hip/hip_bf16.h>
#include <stdint.h>
#include <type_traits>

typedef __attribute__((ext_vector_type(8))) short bf16x8;
typedef __attribute__((ext_vector_type(4))) short bf16x4;
typedef __attribute__((ext_vector_type(2))) __bf16 bf16p;
typedef __attribute__((ext_vector_type(2))) float f32x2;
typedef __attribute__((ext_vector_type(4))) float f32x4;
typedef __attribute__((ext_vector_type(16))) float f32x16;
typedef __attribute__((ext_vector_type(2))) unsigned u32x2;
typedef __attribute__((ext_vector_type(3))) unsigned u32x3;
typedef __attribute__((ext_vector_type(4))) unsigned u32x4;
typedef __attribute__((address_space(3))) const char* lds_cptr;
typedef __attribute__((address_space(3))) char* lds_ptr;
typedef short v4i16_t __attribute__((ext_vector_type(4)));

#define DI __device__ __forceinline__
#define MFMA16(a, b, c) __builtin_amdgcn_mfma_f32_32x32x16_bf16((a), (b), (c), 0, 0, 0)
#define MFMA32K(a, b, c) __builtin_amdgcn_mfma_f32_16x16x32_bf16((a), (b), (c), 0, 0, 0)
#define AMF_ACC(c, a, b) asm volatile("v_mfma_f32_16x16x32_bf16 %0, %1, %2, %0" : "+v"(c) : "v"(a), "v"(b))
#define AMF_NEW(c, a, b) asm volatile("v_mfma_f32_16x16x32_bf16 %0, %1, %2, 0" : "=&v"(c) : "v"(a), "v"(b))
#define MFMA8(a, b, c) __builtin_amdgcn_mfma_f32_32x32x8bf16_1k((a), (b), (c), 0, 0, 0)
#define SBAR() __builtin_amdgcn_sched_barrier(0)

template <int I, int N, class Fn> DI void static_for(Fn&& fn) {
    if constexpr (I < N) { fn(std::integral_constant<int, I>{}); static_for<I + 1, N>(fn); }
}

constexpr int S = 2048, D = 576, NH = 32, DH = 18, F = 4608;
constexpr int NTB = S / 32;
constexpr int NT16 = S / 16;
constexpr int KS = D / 32;
constexpr int FS = F / 32;

constexpr int KVREC = 2560;
constexpr size_t WS_XNF = 0;
constexpr size_t WS_WQP = WS_XNF + (size_t)NT16 * KS * 1024;
constexpr int QREC = 1280;
constexpr size_t WS_WOP = WS_WQP + (size_t)NH * KS * 4096;
constexpr size_t WS_Q = WS_WOP + (size_t)36 * KS * 1024;
constexpr size_t WS_KV = WS_Q + (size_t)NH * NTB * QREC;
constexpr size_t WS_OF = WS_KV + (size_t)NH * NTB * KVREC;
constexpr size_t WS_HF = WS_OF + (size_t)NT16 * KS * 1024;
constexpr int PTILE = 768;
constexpr size_t WS_PART = WS_HF + (size_t)NT16 * FS * 1024;
constexpr size_t WS_END = WS_PART + (size_t)4 * NT16 * 36 * PTILE;
static_assert(WS_END <= (size_t)256 << 20, "the workspace holds at least 256 MiB");

constexpr float LOG2E = 1.4426950408889634f;
constexpr float QSCALE = 0.23570226039551584f * LOG2E;

DI unsigned cvtpk(float lo, float hi) { return __builtin_bit_cast(unsigned, __builtin_convertvector((f32x2){lo, hi}, bf16p)); }
DI float bf_lo(unsigned u) { return __uint_as_float(u << 16); }
DI float bf_hi(unsigned u) { return __uint_as_float(u & 0xffff0000u); }
DI float rbf(float x) { return bf_lo(cvtpk(x, x)); }
DI bf16x4 trread(lds_cptr p) { return __builtin_bit_cast(bf16x4, __builtin_amdgcn_ds_read_tr16_b64_v4i16((__attribute__((address_space(3))) v4i16_t*)p)); }
DI bf16x8 join(bf16x4 a, bf16x4 b) { return (bf16x8){a[0], a[1], a[2], a[3], b[0], b[1], b[2], b[3]}; }
DI float sum16x(float x) { auto r = __builtin_amdgcn_permlane16_swap(__float_as_uint(x), __float_as_uint(x), false, false); return __uint_as_float(r[0]) + __uint_as_float(r[1]); }
DI float sum32x(float x) { auto r = __builtin_amdgcn_permlane32_swap(__float_as_uint(x), __float_as_uint(x), false, false); return __uint_as_float(r[0]) + __uint_as_float(r[1]); }
DI float max16x(float x) { auto r = __builtin_amdgcn_permlane16_swap(__float_as_uint(x), __float_as_uint(x), false, false); return fmaxf(__uint_as_float(r[0]), __uint_as_float(r[1])); }
DI float max32x(float x) { auto r = __builtin_amdgcn_permlane32_swap(__float_as_uint(x), __float_as_uint(x), false, false); return fmaxf(__uint_as_float(r[0]), __uint_as_float(r[1])); }
template <int CTRL> DI float dppf(float x) { return __uint_as_float(__builtin_amdgcn_update_dpp(0u, __float_as_uint(x), CTRL, 0xf, 0xf, true)); }
DI float rowmax16(float x) {
    x = fmaxf(x, dppf<0xB1>(x));
    x = fmaxf(x, dppf<0x4E>(x));
    x = fmaxf(x, dppf<0x141>(x));
    return fmaxf(x, dppf<0x140>(x));
}
DI float swap32(float x) { auto r = __builtin_amdgcn_permlane32_swap(__float_as_uint(x), __float_as_uint(x), false, false); return __uint_as_float((threadIdx.x & 32) ? r[0] : r[1]); }

DI void glds16(const void* sbase, unsigned voff, unsigned lds_base) {
    asm volatile("s_mov_b32 m0, %2\n\ts_nop 0\n\tglobal_load_lds_dwordx4 %0, %1" :: "v"(voff), "s"(sbase), "s"(lds_base) : "memory");
}
#define STPOL "sc1"
DI void gstore16(void* sbase, unsigned voff, u32x4 v) { asm volatile("global_store_dwordx4 %0, %1, %2 " STPOL "\n\ts_nop 1" :: "v"(voff), "v"(v), "s"(sbase) : "memory"); }
DI void gstore8(void* sbase, unsigned voff, u32x2 v) { asm volatile("global_store_dwordx2 %0, %1, %2" :: "v"(voff), "v"(v), "s"(sbase) : "memory"); }
DI u32x3 pack24(f32x4 v) {
    const unsigned a = __float_as_uint(v[0]), b = __float_as_uint(v[1]), c = __float_as_uint(v[2]), d = __float_as_uint(v[3]);
    return (u32x3){__builtin_amdgcn_perm(a, d, 0x07060501u), __builtin_amdgcn_perm(b, d, 0x07060502u), __builtin_amdgcn_perm(c, d, 0x07060503u)};
}
DI f32x4 unpack24(u32x3 p) {
    const unsigned t = __builtin_amdgcn_perm(p[2], p[1], 0x04000c0cu);
    const unsigned d = __builtin_amdgcn_perm(t, p[0], 0x0706000cu);
    return (f32x4){__uint_as_float(p[0] & 0xffffff00u), __uint_as_float(p[1] & 0xffffff00u), __uint_as_float(p[2] & 0xffffff00u), __uint_as_float(d)};
}
DI void gstore12(void* sbase, unsigned voff, u32x3 v) { asm volatile("global_store_dwordx3 %0, %1, %2 " STPOL "\n\ts_nop 1" :: "v"(voff), "v"(v), "s"(sbase) : "memory"); }
DI void gstore16p(void* p, u32x4 v) { asm volatile("global_store_dwordx4 %0, %1, off " STPOL "\n\ts_nop 1" :: "v"(p), "v"(v) : "memory"); }
#define WAITV_BAR(n) asm volatile("s_waitcnt vmcnt(" #n ") lgkmcnt(0)\n\ts_barrier" ::: "memory")

DI int wo_row(int ks, int kg, int j) {
    return ks < 16 ? DH * (2 * ks + (kg >> 1)) + 8 * (j >> 2) + 4 * (kg & 1) + (j & 3) : DH * (16 * (ks - 16) + 4 * kg + (j >> 1)) + 16 + (j & 1);
}
DI float sum32(float x) {
    x += dppf<0xB1>(x);
    x += dppf<0x4E>(x);
    x += dppf<0x141>(x);
    x += dppf<0x140>(x);
    return sum16x(x);
}

__global__ __launch_bounds__(1024) void k_prep(const unsigned short* __restrict__ x, const unsigned short* __restrict__ wqkv, const unsigned short* __restrict__ wo, char* __restrict__ ws) {
    __shared__ __attribute__((aligned(16))) char stage[KS * 512];
    const int tid = threadIdx.x;
    const int wv = __builtin_amdgcn_readfirstlane(tid >> 6);
    if (wv < 4) {
        const int row = blockIdx.x * 8 + (tid >> 5), t = tid & 31;
        const u32x4* xr = (const u32x4*)(x + (size_t)row * D);
        u32x4 v[3];
        v[0] = xr[t]; v[1] = xr[t + 32]; v[2] = (t < 8) ? xr[t + 64] : (u32x4){0u, 0u, 0u, 0u};
        float sum = 0.f;
#pragma unroll
        for (int i = 0; i < 3; ++i)
#pragma unroll
            for (int j = 0; j < 4; ++j) sum += bf_lo(v[i][j]) + bf_hi(v[i][j]);
        const float mean = sum32(sum) * (1.0f / D);
        float sq = 0.f;
#pragma unroll
        for (int i = 0; i < 3; ++i)
            if (i < 2 || t < 8)
#pragma unroll
                for (int j = 0; j < 4; ++j) { const float a = bf_lo(v[i][j]) - mean, b = bf_hi(v[i][j]) - mean; sq += a * a + b * b; }
        const float mb = rbf(mean);
        const float den = rbf(__fsqrt_rn(rbf(rbf(sum32(sq) * (1.0f / D)) + 1e-5f)));
        const float rc = __builtin_amdgcn_rcpf(den);
        auto quot = [&](float a) { const float q = a * rc; return __builtin_fmaf(__builtin_fmaf(-q, den, a), rc, q); };
#pragma unroll
        for (int i = 0; i < 3; ++i) {
            if (i < 2 || t < 8) {
                const int p = t + 32 * i;
                unsigned o[4];
#pragma unroll
                for (int j = 0; j < 4; ++j) o[j] = cvtpk(quot(rbf(bf_lo(v[i][j]) - mb)), quot(rbf(bf_hi(v[i][j]) - mb)));
                char* d = stage + (p >> 2) * 512 + (p & 1) * 256 + (tid >> 5) * 16 + ((p >> 1) & 1) * 8;
                *(u32x2*)d = (u32x2){o[0], o[1]};
                *(u32x2*)(d + 128) = (u32x2){o[2], o[3]};
            }
        }
        asm volatile("s_waitcnt lgkmcnt(0)\n\ts_barrier" ::: "memory");
        char* dst = ws + WS_XNF + (size_t)(blockIdx.x >> 1) * KS * 1024 + (blockIdx.x & 1) * 128;
#pragma unroll
        for (int i = 0; i < 3; ++i) {
            const int n = tid + 256 * i;
            if (n < KS * 32) gstore16p(dst + (n >> 3) * 256 + (n & 7) * 16, *(const u32x4*)(stage + n * 16));
        }
    } else if (wv < 13) {
        const int n = blockIdx.x * 576 + (tid - 256);
        const int pc = n & 3, r = (n >> 2) & 31, sel = (n >> 7) & 1, hs = n >> 8, s = hs % KS, h = hs / KS;
        const int c = pc ^ (2 * ((r >> 2) & 1)), t = c >> 1, g = c & 1;
        const unsigned* w = (const unsigned*)(wqkv + (size_t)(32 * s + r) * (3 * D) + DH * h);
        u32x4 o = {0u, 0u, 0u, 0u};
        if (sel == 0 || t == 0) {
            const int col = (sel == 0 ? t * D : 2 * D) + 8 * g;
            o = (u32x4){w[col / 2], w[col / 2 + 1], w[col / 2 + 2], w[col / 2 + 3]};
        } else if (g == 0) {
            o = (u32x4){w[8], w[D / 2 + 8], w[D + 8], 0u};
        }
        gstore16p(ws + WS_WQP + (size_t)n * 16, o);
    } else if (tid < 256 + 576 + 162) {
        const int n = blockIdx.x * 162 + (tid - (256 + 576));
        const int lane = n & 63, fs = n >> 6, ks = fs % KS, dt = fs / KS;
        unsigned short v[8];
#pragma unroll
        for (int j = 0; j < 8; ++j) v[j] = wo[(size_t)wo_row(ks, lane >> 4, j) * D + 16 * dt + (lane & 15)];
        gstore16p(ws + WS_WOP + (size_t)n * 16, (u32x4){v[0] | ((unsigned)v[1] << 16), v[2] | ((unsigned)v[3] << 16), v[4] | ((unsigned)v[5] << 16), v[6] | ((unsigned)v[7] << 16)});
    }
}

constexpr int G1_SLOT = 9 * 4096;

DI int img_aoff(int lane) {
    const int kg = lane >> 4, q = (lane & 15) >> 2, p = lane & 3;
    return (4 * kg + q) * 64 + (((p >> 1) ^ (2 * (kg & 1))) * 16) + 8 * (p & 1);
}
DI void g1_ldx(bf16x8& d, const char* base, unsigned lo, int ks) {
    asm volatile("global_load_dwordx4 %0, %1, %2 offset:%3" : "=v"(d) : "v"(lo), "s"(base + (ks >> 2) * 4096), "n"((ks & 3) * 1024));
}
DI float glu(float g, float u) { return u * __builtin_amdgcn_rcpf(1.0f + __builtin_amdgcn_exp2f(g * -LOG2E)); }

constexpr int g1_group(int u) { return u < 4 ? (u & 1) : u / 2; }
constexpr int g1_half(int u) { return u < 4 ? (u >> 1) : (u & 1); }
constexpr int g1_dma_at(int u, int g) {
    if (u == 0) return (g >= 12 && g <= 20 && !(g & 1)) ? 1 * 8 + (g - 12) / 2 : (g >= 22 && g <= 30 && !(g & 1)) ? 2 * 8 + (g - 22) / 2 : -1;
    if (u == 1) return -1;
    if (u == 2) return (g <= 16 && !(g & 3)) ? 3 * 8 + g / 4 : (g >= 20 && g <= 28 && !(g & 1)) ? 4 * 8 + (g - 20) / 2 : -1;
    if (u <= 9) { const int sp = 5; return (g % sp == 0 && g / sp < 5) ? (u + 2) * 8 + g / sp : -1; }
    return -1;
}
constexpr int g1_x_at(int u, int g) {
    if (g & 3) return -1;
    if (u == 0) return g / 4 < 3 ? 6 + g / 4 : -1;
    if (u == 1) return 9 + g / 4;
    return -1;
}
constexpr int g1_x_younger(int ks, int u, int g) {
    int n = 0; bool seen = ks < 6;
    if (ks < 6) n += 2 * (5 - ks);
    for (int uu = 0; uu <= u; ++uu)
        for (int gg = 0; gg < 36 && (uu < u || gg <= g); ++gg) {
            const int x = g1_x_at(uu, gg);
            if (x >= 0) { if (seen) n += 2; if (x == ks) seen = true; }
            if (g1_dma_at(uu, gg) >= 0 && seen) n += 1;
        }
    return n;
}

struct G1Ctx {
    const char *wrow, *wq, *xs;
    char* hdst;
    unsigned lds0, dsto, voff, lo;
    int fbase, fsingle, w;
};
DI void g1_piece(const G1Ctx& c, int unit, int m) {
    const int grp = g1_group(unit), hk = g1_half(unit);
    const unsigned slot = c.lds0 + (unit & 3) * G1_SLOT;
    const int mm = (m == 4 && c.w >= 4) ? 3 : m;
    if (grp == 0) {
        const int pi = c.w + 8 * mm;
        glds16(c.wq + hk * G1_SLOT + pi * 1024, c.lo, slot + pi * 1024);
    } else {
        const int f0 = grp == 5 ? c.fsingle : c.fbase + 32 * (grp - 1);
        glds16(c.wrow + (size_t)(288 * hk + 64 * mm) * (F * 2) + f0 * 2, c.voff, slot + c.dsto + mm * 8192);
    }
}

struct G1Epi { float e[2], h[2]; u32x4 pk; };
DI void g1_epi_slice(int s, const f32x4 (&c)[2][2][2], G1Epi& t, char* hdst, unsigned lo) {
    const int v = s >> 2, nh = v >> 3, j = v & 7, ph = s & 3;
    if (ph == 0) { t.e[v & 1] = __builtin_amdgcn_exp2f(c[0][j >> 2][nh][j & 3] * -LOG2E); asm volatile("" : "+v"(t.e[v & 1])); }
    if (ph == 1) { t.e[v & 1] = __builtin_amdgcn_rcpf(1.0f + t.e[v & 1]); asm volatile("" : "+v"(t.e[v & 1])); }
    if (ph == 2) { t.h[v & 1] = c[1][j >> 2][nh][j & 3] * t.e[v & 1]; asm volatile("" : "+v"(t.h[v & 1])); }
    if (ph == 3 && (v & 1)) {
        t.pk[j >> 1] = cvtpk(t.h[0], t.h[1]);
        asm volatile("" : "+v"(t.pk[j >> 1]));
        if (j == 7) gstore16(hdst + (size_t)nh * FS * 1024, lo, t.pk);
    }
}

struct QkvEpi { char *qg, *kg; lds_ptr stg; float kmax; };
DI void qkv_slice(int k, const f32x4 (&c)[2][2][2], QkvEpi& e, unsigned lo) {
    typedef __attribute__((address_space(3))) u32x2* l64;
    typedef __attribute__((address_space(3))) unsigned* l32;
    typedef __attribute__((address_space(3))) unsigned short* l16;
    typedef __attribute__((address_space(3))) const u32x4* l128;
    asm volatile("" : "+v"(lo));
    const int lane = lo >> 4;
    const int t = lane & 15, g4 = lane >> 4;
    const lds_ptr rec = e.stg + QREC;
    if (k < 4) {
        const int nh = k >> 1, r = 16 * nh + t;
        const f32x4 ex = c[1][1][nh];
        if ((k & 1) == 0) {
            const unsigned fo = (r + 32 * (g4 >> 1)) * 16 + (g4 & 1) * 8;
            const f32x4 q = c[0][0][nh], kk = c[0][1][nh];
            *(l64)(e.stg + fo) = (u32x2){cvtpk(q[0] * QSCALE, q[1] * QSCALE), cvtpk(q[2] * QSCALE, q[3] * QSCALE)};
            *(l64)(rec + fo) = (u32x2){cvtpk(kk[0], kk[1]), cvtpk(kk[2], kk[3])};
            float n2 = g4 == 0 ? ex[2] * ex[2] + ex[3] * ex[3] : 0.f;
            n2 += (kk[0] * kk[0] + kk[1] * kk[1]) + (kk[2] * kk[2] + kk[3] * kk[3]);
            n2 = sum32x(sum16x(n2));
            e.kmax = nh ? fmaxf(e.kmax, n2) : n2;
            asm volatile("" : "+v"(e.kmax));
        } else {
            const f32x4 v = c[1][0][nh];
            *(l64)(rec + 1280 + nh * 640 + (g4 & 1) * 320 + t * 16 + (g4 >> 1) * 8) = (u32x2){cvtpk(v[0], v[1]), cvtpk(v[2], v[3])};
            const unsigned e01 = cvtpk(ex[0], ex[1]), e23 = cvtpk(ex[2], ex[3]);
            const bool g0 = g4 == 0, g1 = g4 == 1;
            const lds_ptr spare = e.stg + 1168;
            const lds_ptr vt = rec + 1280 + nh * 640 + ((t >> 2) & 1) * 320 + (4 * (t >> 3) + (t & 3)) * 2;
            *(l64)(g0 ? rec + 1024 + r * 8 : spare + 8) = (u32x2){e23, 0u};
            *(l32)(g0 ? e.stg + 1024 + r * 4 : spare + 4) = cvtpk(ex[0] * QSCALE, ex[1] * QSCALE);
            *(l16)(g0 ? spare : vt + (g1 ? 16 : 16 + g4) * 16) = (unsigned short)(g1 ? e01 : (g4 == 2 ? 0x3f80u : 0u));
            *(l16)(g1 ? vt + 17 * 16 : spare + 2) = (unsigned short)(e01 >> 16);
        }
    } else if (k == 4) {
        const float m = rowmax16(e.kmax);
        if (lane == 0) *(l32)(e.stg + 1152) = __float_as_uint(m);
    } else if (k == 5) {
        const unsigned o0 = lane * 16, oq = min(lane, 8) * 16 + 1024;
        const u32x4 d0 = *(l128)(e.stg + o0), d1 = *(l128)(e.stg + oq);
        gstore16(e.qg, o0, d0);
        gstore16(e.qg, oq, d1);
    } else {
        const unsigned o0 = lane * 16, ok = min(lane, 31) * 16 + 2048;
        const u32x4 d2 = *(l128)(rec + o0), d3 = *(l128)(rec + 1024 + o0), d4 = *(l128)(rec + ok);
        gstore16(e.kg, o0, d2);
        gstore16(e.kg, o0 + 1024, d3);
        gstore16(e.kg, ok, d4);
    }
}

struct G1Ring { bf16x4 lo[3], hi[3]; };
constexpr int g1_meet(int u) { return u == 1 ? 18 : u == 3 ? 10 : u == 10 ? 0 : 5; }
template <int U>
DI void g1_unit(const G1Ctx& cx, lds_cptr& b0, lds_cptr& b1, bool odd, G1Ring& fr, bf16x8 (&xb)[2][KS], f32x4 (&c)[2][2][2],
                const f32x4 (&cprev)[2][2][2], QkvEpi& qe) {
    constexpr int GRP = g1_group(U), HK = g1_half(U);
    constexpr bool PAIR = GRP != 5, NPAIR = g1_group(U + 1) != 5;
    constexpr int PER = PAIR ? 4 : 2, N = 9 * PER;
    constexpr bool EPI = U >= 4;
    auto addr = [&](int i) {
        const bool nx = i >= N, pr = nx ? NPAIR : PAIR;
        const int j = nx ? i - N : i, ks = pr ? j >> 2 : j >> 1, sel = pr ? (j >> 1) & 1 : j & 1, t = pr ? j & 1 : 0;
        return (t ? b1 : b0) + ks * 4096 + sel * 2048;
    };
    G1Epi ep;
    char* hprev = cx.hdst + (U / 2 - 2) * 1024;
    static_for<0, N>([&](auto ic) {
        constexpr int i = decltype(ic)::value;
        constexpr int kl = i / PER, ks = 9 * HK + kl, sel = PAIR ? (i >> 1) & 1 : i & 1, t = PAIR ? i & 1 : 0;
        constexpr int g = PAIR ? i : 2 * i;
        constexpr int x = g1_x_at(U, g), dm = g1_dma_at(U, g);
        constexpr bool more = U < 11 || i + 2 < N;
        if constexpr (i == N - 2 && U < 11) {
            asm volatile("s_waitcnt vmcnt(%0) lgkmcnt(0)\n\ts_barrier" :: "n"(g1_meet(U)) : "memory");
            constexpr int delta = (((U + 1) & 3) - (U & 3)) * G1_SLOT;
            if constexpr (U == 9) b0 = odd ? b1 : b0;
            b0 += delta; b1 += delta;
            asm volatile("" : "+v"(b0), "+v"(b1));
        }
        if constexpr (x >= 0) { g1_ldx(xb[0][x], cx.xs, cx.lo, x); g1_ldx(xb[1][x], cx.xs + KS * 1024, cx.lo, x); }
        if constexpr (dm >= 0) g1_piece(cx, dm >> 3, dm & 7);
        if constexpr ((U == 0 || U == 2) && (i & 3) == 0) {
            constexpr int young = g1_x_younger(ks, U, g);
            asm volatile("s_waitcnt vmcnt(%2)" : "+v"(xb[0][ks]), "+v"(xb[1][ks]) : "n"(young));
        }
        constexpr bool SWAP = GRP == 0 && sel == 1 && t == 0;
        const bf16x8 a = join(fr.lo[i % 3], fr.hi[i % 3]);
        if constexpr (ks == 0) { if constexpr (SWAP) AMF_NEW(c[sel][t][0], xb[0][ks], a); else AMF_NEW(c[sel][t][0], a, xb[0][ks]); }
        else { if constexpr (SWAP) AMF_ACC(c[sel][t][0], xb[0][ks], a); else AMF_ACC(c[sel][t][0], a, xb[0][ks]); }
        SBAR();
        if constexpr (more) fr.lo[(i + 2) % 3] = trread(addr(i + 2));
        SBAR();
        if constexpr (ks == 0) { if constexpr (SWAP) AMF_NEW(c[sel][t][1], xb[1][ks], a); else AMF_NEW(c[sel][t][1], a, xb[1][ks]); }
        else { if constexpr (SWAP) AMF_ACC(c[sel][t][1], xb[1][ks], a); else AMF_ACC(c[sel][t][1], a, xb[1][ks]); }
        SBAR();
        if constexpr (more) fr.hi[(i + 2) % 3] = trread(addr(i + 2) + 1024);
        SBAR();
        if constexpr (U == 3 && (i & 3) == 2 && i / 4 < 7) qkv_slice(i / 4, cprev, qe, cx.lo);
        if constexpr (EPI && PAIR) {
            constexpr int sl = 36 * HK + i - 4;
            if constexpr (sl >= 0 && sl < 64) g1_epi_slice(sl, cprev, ep, hprev, cx.lo);
        } else if constexpr (EPI) {
            constexpr int sl = 2 * (18 * HK + i - 2);
            if constexpr (sl >= 0 && sl < 64) { g1_epi_slice(sl, cprev, ep, hprev, cx.lo); g1_epi_slice(sl + 1, cprev, ep, hprev, cx.lo); }
        }
        SBAR();
    });
}

__global__ __launch_bounds__(512, 2) void k_gemm1(const char* wg, const char* wu, char* ws) {
    extern __shared__ __attribute__((aligned(16))) char lds[];

    const int tid = threadIdx.x, lane = tid & 63;
    const int w = __builtin_amdgcn_readfirstlane(tid >> 6);
    const int xcd = blockIdx.x & 7, idx = blockIdx.x >> 3;
    const int cg = 8 * (xcd >> 1) + (idx & 7), rg = 4 * (xcd & 1) + (idx >> 3), tb = 8 * rg + w;

    const int odd = cg & 1;
    G1Ctx cx;
    cx.w = w; cx.lds0 = (unsigned)(uintptr_t)lds; cx.lo = lane * 16;
    cx.fbase = 144 * cg + 16 * odd; cx.fsingle = odd ? 144 * cg - 16 : 144 * cg + 128;
    cx.wq = ws + WS_WQP + (size_t)cg * KS * 4096;
    cx.xs = ws + WS_XNF + (size_t)(2 * tb) * KS * 1024;
    cx.wrow = ((w & 1) ? wu : wg) + (size_t)(32 * (w >> 2) + 16 * ((w >> 1) & 1)) * (F * 2);
    cx.dsto = (w >> 2) * 4096 + (w & 1) * 2048 + ((w >> 1) & 1) * 1024;
    cx.voff = (lane >> 2) * (F * 2) + (((lane & 3) ^ (2 * ((lane >> 4) & 1))) * 16);
    cx.hdst = ws + WS_HF + ((size_t)(2 * tb) * FS + cx.fbase / 32) * 1024;

#pragma unroll
    for (int m = 0; m < 5; ++m) g1_piece(cx, 0, m);
    bf16x8 xb[2][KS];
#pragma unroll
    for (int s = 0; s < 6; ++s) { g1_ldx(xb[0][s], cx.xs, cx.lo, s); g1_ldx(xb[1][s], cx.xs + KS * 1024, cx.lo, s); }

    const int aoff0 = img_aoff(lane);
    lds_cptr b0 = (lds_cptr)lds + aoff0, b1 = (lds_cptr)lds + (aoff0 ^ 32);
    asm volatile("" : "+v"(b0), "+v"(b1));
    f32x4 ca[2][2][2], cb[2][2][2];
    QkvEpi qe;
    qe.qg = ws + WS_Q + ((size_t)cg * NTB + tb) * QREC;
    qe.kg = ws + WS_KV + ((size_t)cg * NTB + tb) * KVREC;
    qe.stg = (lds_ptr)lds + 2 * G1_SLOT + w * 4608;

    WAITV_BAR(12);

    G1Ring fr;
#pragma unroll
    for (int i = 0; i < 2; ++i) {
        const lds_cptr a = (i & 1) ? b1 : b0;
        fr.lo[i] = trread(a); fr.hi[i] = trread(a + 1024);
    }
    g1_unit<0>(cx, b0, b1, odd, fr, xb, ca, cb, qe);
    g1_unit<1>(cx, b0, b1, odd, fr, xb, cb, ca, qe);
    g1_unit<2>(cx, b0, b1, odd, fr, xb, ca, cb, qe);
    g1_unit<3>(cx, b0, b1, odd, fr, xb, cb, ca, qe);
    g1_unit<4>(cx, b0, b1, odd, fr, xb, ca, cb, qe);
    g1_unit<5>(cx, b0, b1, odd, fr, xb, ca, cb, qe);
    g1_unit<6>(cx, b0, b1, odd, fr, xb, cb, ca, qe);
    g1_unit<7>(cx, b0, b1, odd, fr, xb, cb, ca, qe);
    g1_unit<8>(cx, b0, b1, odd, fr, xb, ca, cb, qe);
    g1_unit<9>(cx, b0, b1, odd, fr, xb, ca, cb, qe);
    g1_unit<10>(cx, b0, b1, odd, fr, xb, cb, ca, qe);
    g1_unit<11>(cx, b0, b1, odd, fr, xb, cb, ca, qe);
    asm volatile("s_nop 7\n\ts_nop 3");
    SBAR();
    {
        char* hs = ws + WS_HF + ((size_t)(2 * tb) * FS + cx.fsingle / 32) * 1024 + odd * 8;
#pragma unroll
        for (int nh = 0; nh < 2; ++nh) {
            float h[4];
#pragma unroll
            for (int j = 0; j < 4; ++j) h[j] = glu(cb[0][0][nh][j], cb[1][0][nh][j]);
            gstore8(hs + (size_t)nh * FS * 1024, cx.lo, (u32x2){cvtpk(h[0], h[1]), cvtpk(h[2], h[3])});
        }
    }
}

constexpr int AT_STAGE = 8 * KVREC;
constexpr float AT_BOUND_MAX = 50.0f;

struct KFrag { bf16x8 k1; bf16x4 k2; };
struct VFrag { bf16x8 v0, v1; };
DI KFrag at_readk(lds_cptr rec, int lane) {
    KFrag k;
    k.k1 = *(const __attribute__((address_space(3))) bf16x8*)(rec + lane * 16);
    k.k2 = *(const __attribute__((address_space(3))) bf16x4*)(rec + 1024 + (lane & 31) * 8);
    return k;
}
DI VFrag at_readv(lds_cptr recv) {
    VFrag v;
    v.v0 = *(const __attribute__((address_space(3))) bf16x8*)(recv);
    v.v1 = *(const __attribute__((address_space(3))) bf16x8*)(recv + 640);
    return v;
}
DI bf16x8 packp(const f32x16& p, int s) {
    return __builtin_bit_cast(bf16x8, (u32x4){cvtpk(p[8 * s], p[8 * s + 1]), cvtpk(p[8 * s + 2], p[8 * s + 3]), cvtpk(p[8 * s + 4], p[8 * s + 5]), cvtpk(p[8 * s + 6], p[8 * s + 7])});
}

DI void at_dma(const char* kvh, int stage, unsigned ldsb, int w, int lane) {
    const char* src = kvh + (size_t)stage * AT_STAGE + w * 5 * 1024;
    const unsigned dst = ldsb + w * 5 * 1024, lo = lane * 16;
    asm volatile("s_mov_b32 m0, %2\n\ts_nop 0\n\t"
                 "global_load_lds_dwordx4 %0, %1\n\tglobal_load_lds_dwordx4 %0, %1 offset:1024\n\t"
                 "global_load_lds_dwordx4 %0, %1 offset:2048\n\tglobal_load_lds_dwordx4 %0, %1 offset:3072"
                 :: "v"(lo), "s"(src), "s"(dst) : "memory");
    glds16(src + 4096, lo, dst + 4096);
}

#define AMFMA16_NEW(d, a, b, c) asm volatile("v_mfma_f32_32x32x16_bf16 %0, %1, %2, %3" : "=&v"(d) : "v"(a), "v"(b), "v"(c))
#define AMFMA16_ACC(d, a, b) asm volatile("v_mfma_f32_32x32x16_bf16 %0, %1, %2, %0" : "+v"(d) : "v"(a), "v"(b))
#define AMFMA8_ACC(d, a, b) asm volatile("v_mfma_f32_32x32x8_bf16 %0, %1, %2, %0" : "+v"(d) : "v"(a), "v"(b))
#define AT_EXP4(E, SX, a) do { _Pragma("unroll") for (int i_ = 0; i_ < 4; ++i_) asm volatile("v_exp_f32 %0, %1" : "=v"(E[i_]) : "v"(SX[a + i_])); } while (0)
#define AT_PK2(DST, k, E) do { asm volatile("v_cvt_pk_bf16_f32 %0, %1, %2" : "=v"(DST[k]) : "v"(E[0]), "v"(E[1]));   \
                               asm volatile("v_cvt_pk_bf16_f32 %0, %1, %2" : "=v"(DST[k + 1]) : "v"(E[2]), "v"(E[3])); } while (0)
#define AT_STEP(SX, PX0, PX1, SY, CY, QY1, QY2, KF, OY, PY0, PY1, VF, LOADS)                                   \
    do {                                                                                                       \
        float e0_[4], e1_[4], e2_[4];                                                                          \
        AMFMA16_NEW(SY, KF.k1, QY1, CY);                                                                       \
        LOADS;                                                                                                 \
        SBAR();                                                                                                \
        AT_EXP4(e0_, SX, 0);                                                                                   \
        AT_PK2(PY1, 2, etail);                                                                                 \
        AMFMA8_ACC(SY, KF.k2, QY2);                                                                            \
        AT_EXP4(e1_, SX, 4);                                                                                   \
        AT_PK2(PX0, 0, e0_);                                                                                   \
        AMFMA16_ACC(OY, VF.v0, PY0);                                                                           \
        AT_EXP4(e2_, SX, 8);                                                                                   \
        AT_PK2(PX0, 2, e1_);                                                                                   \
        AMFMA16_ACC(OY, VF.v1, PY1);                                                                           \
        AT_EXP4(etail, SX, 12);                                                                                \
        AT_PK2(PX1, 0, e2_);                                                                                   \
        SBAR();                                                                                                \
    } while (0)

DI void at_online(const KFrag& k, const VFrag& v, bf16x8 q1, bf16x4 q2, float& m, f32x16& o) {
    f32x16 z = {};
    f32x16 s = MFMA16(k.k1, q1, z);
    s = MFMA8(k.k2, q2, s);
    float t = s[0];
#pragma unroll
    for (int i = 1; i < 16; ++i) t = fmaxf(t, s[i]);
    t = fmaxf(t, swap32(t));
    const float mn = fmaxf(m, t);
    const float alpha = __builtin_amdgcn_exp2f(m - mn);
    m = mn;
#pragma unroll
    for (int i = 0; i < 16; ++i) { s[i] = __builtin_amdgcn_exp2f(s[i] - mn); o[i] *= alpha; }
    o = MFMA16(v.v0, packp(s, 0), o);
    o = MFMA16(v.v1, packp(s, 1), o);
}

DI void at_store(const f32x16& o, char* ws, int h, int tb, int lane) {
    const int r = lane & 31, hi = lane >> 5;
    const float other = swap32(o[10]);
    const float inv = 1.0f / (hi ? other : o[10]);
    char* of = ws + WS_OF + (size_t)(2 * tb + (r >> 4)) * KS * 1024;
    gstore16p(of + (h >> 1) * 1024 + ((r & 15) + 16 * (2 * (h & 1) + hi)) * 16,
              (u32x4){cvtpk(o[0] * inv, o[1] * inv), cvtpk(o[2] * inv, o[3] * inv), cvtpk(o[4] * inv, o[5] * inv), cvtpk(o[6] * inv, o[7] * inv)});
    if (hi == 0) *(unsigned*)(of + (16 + (h >> 4)) * 1024 + ((r & 15) + 16 * ((h >> 2) & 3)) * 16 + (h & 3) * 4) = cvtpk(o[8] * inv, o[9] * inv);
}

DI float at_qnorm2(bf16x8 q1, bf16x4 q2) {
    const u32x4 a = __builtin_bit_cast(u32x4, q1); const u32x2 b = __builtin_bit_cast(u32x2, q2);
    float n = bf_lo(b[0]) * bf_lo(b[0]) + bf_hi(b[0]) * bf_hi(b[0]);
#pragma unroll
    for (int j = 0; j < 4; ++j) n += bf_lo(a[j]) * bf_lo(a[j]) + bf_hi(a[j]) * bf_hi(a[j]);
    return sum32x(n);
}

DI void attn_block(char* ws, char* lds, int bid) {
    const int tid = threadIdx.x, lane = tid & 63, hi = lane >> 5;
    const int w = __builtin_amdgcn_readfirstlane(tid >> 6);
    const int xcd = bid & 7, idx = bid >> 3;
    const int h = 4 * xcd + (idx & 3), qg = idx >> 2;
    const int tbA = 8 * qg + 2 * w, tbB = tbA + 1;
    const unsigned lds0 = (unsigned)(uintptr_t)lds;
    const char* kvh = ws + WS_KV + (size_t)h * NTB * KVREC;

    bf16x8 qA1, qB1; unsigned qa2, qb2; float km;
    {
        const char* q1 = ws + WS_Q + ((size_t)h * NTB + tbA) * QREC;
        const char* kx = ws + WS_Q + (size_t)h * NTB * QREC + 1152;
        const unsigned l16 = lane * 16, l4 = (lane & 31) * 4 + 1024, lk = lane * QREC;
        asm volatile("global_load_dword %0, %1, %2" : "=v"(km) : "v"(lk), "s"(kx));
        asm volatile("global_load_dwordx4 %0, %1, %2" : "=v"(qA1) : "v"(l16), "s"(q1));
        asm volatile("global_load_dwordx4 %0, %1, %2 offset:%3" : "=v"(qB1) : "v"(l16), "s"(q1), "n"(QREC));
        asm volatile("global_load_dword %0, %1, %2" : "=v"(qa2) : "v"(l4), "s"(q1));
        asm volatile("global_load_dword %0, %1, %2 offset:%3" : "=v"(qb2) : "v"(l4), "s"(q1), "n"(QREC));
    }
    at_dma(kvh, 0, lds0, w, lane);
    at_dma(kvh, 1, lds0 + AT_STAGE, w, lane);
    asm volatile("s_waitcnt vmcnt(10)" : "+v"(km), "+v"(qA1), "+v"(qB1), "+v"(qa2), "+v"(qb2));
    const bf16x4 qA2 = __builtin_bit_cast(bf16x4, (u32x2){hi ? 0u : qa2, 0u});
    const bf16x4 qB2 = __builtin_bit_cast(bf16x4, (u32x2){hi ? 0u : qb2, 0u});
    km = rowmax16(km);
    km = max32x(max16x(km));
    const float mA = __builtin_amdgcn_sqrtf(at_qnorm2(qA1, qA2) * km) * 1.01f, mB = __builtin_amdgcn_sqrtf(at_qnorm2(qB1, qB2) * km) * 1.01f;
    const bool fast = __all(mA <= AT_BOUND_MAX && mB <= AT_BOUND_MAX);

    const int voffs = 1280 + hi * 320 + min(lane & 31, 19) * 16;
    const lds_cptr L = (lds_cptr)lds;
    f32x16 oA = {}, oB = {};

    if (fast) {
        f32x16 cA, cB;
#pragma unroll
        for (int i = 0; i < 16; ++i) { cA[i] = -mA; cB[i] = -mB; }
        asm volatile("" : "+v"(cA), "+v"(cB));
        f32x16 sA, sB;
        u32x4 pA0 = {}, pA1 = {}, pB0 = {}, pB1 = {};
        float etail[4] = {0.f, 0.f, 0.f, 0.f};
        KFrag k0, k1; VFrag v0, v1;
        WAITV_BAR(5);
        k0 = at_readk(L, lane);
        v1 = at_readv(L + voffs);
        sA = MFMA16(k0.k1, qA1, cA); sA = MFMA8(k0.k2, qA2, sA);
        asm volatile("s_nop 15" : "+v"(sA));
        int slot = 0;
        for (int st = 0; st < 8; ++st) {
            const lds_cptr cur = L + slot * AT_STAGE;
            const int nslot = slot == 2 ? 0 : slot + 1;
#pragma unroll
            for (int t = 0; t < 8; t += 2) {
                AT_STEP(sA, pA0, pA1, sB, cB, qB1, qB2, k0, oB, pB0, pB1, v1,
                        (k1 = at_readk(cur + (t + 1) * KVREC, lane), v0 = at_readv(cur + t * KVREC + voffs)));
                AT_STEP(sB, pB0, pB1, sA, cA, qA1, qA2, k1, oA, pA0, pA1, v0, (void)0);
                lds_cptr nk = cur + (t + 2) * KVREC;
                if (t == 6) {
                    if (st < 7) {
                        if (st < 6) { WAITV_BAR(0); at_dma(kvh, st + 2, lds0 + (nslot == 2 ? 0 : nslot + 1) * AT_STAGE, w, lane); }
                        else WAITV_BAR(0);
                        nk = L + nslot * AT_STAGE;
                    } else nk = cur;
                }
                AT_STEP(sA, pA0, pA1, sB, cB, qB1, qB2, k1, oB, pB0, pB1, v0,
                        (k0 = at_readk(nk, lane), v1 = at_readv(cur + (t + 1) * KVREC + voffs)));
                AT_STEP(sB, pB0, pB1, sA, cA, qA1, qA2, k0, oA, pA0, pA1, v1, (void)0);
            }
            slot = nslot;
        }
        asm volatile("s_nop 1");
        AT_PK2(pB1, 2, etail);
        asm volatile("s_nop 1" : "+v"(pB1));
        AMFMA16_ACC(oB, v1.v0, pB0);
        AMFMA16_ACC(oB, v1.v1, pB1);
        asm volatile("s_nop 15\n\ts_nop 7" : "+v"(oA), "+v"(oB));
        SBAR();
    } else {
        float mxA = -INFINITY, mxB = -INFINITY;
        WAITV_BAR(5);
        int slot = 0;
        for (int st = 0; st < 8; ++st) {
            const lds_cptr cur = L + slot * AT_STAGE;
            const int nslot = slot == 2 ? 0 : slot + 1;
            for (int t = 0; t < 8; ++t) {
                const KFrag k = at_readk(cur + t * KVREC, lane);
                const VFrag v = at_readv(cur + t * KVREC + voffs);
                at_online(k, v, qA1, qA2, mxA, oA);
                at_online(k, v, qB1, qB2, mxB, oB);
            }
            if (st < 7) {
                WAITV_BAR(0);
                if (st < 6) at_dma(kvh, st + 2, lds0 + (nslot == 2 ? 0 : nslot + 1) * AT_STAGE, w, lane);
            }
            slot = nslot;
        }
    }
    at_store(oA, ws, h, tbA, lane);
    at_store(oB, ws, h, tbB, lane);
}

constexpr int DN_STEP = 32 * 288, DN_SLOT = 2 * DN_STEP, DN_LDS = 4 * DN_SLOT, DN_NST = 18;
constexpr int dn_piece(int st, int i) {
    if ((i == 3 || i == 7 || i == 11) && st + 3 < DN_NST) return (st + 3) * 8 + 2 + (i - 3) / 4;
    if ((i == 15 || i == 17) && st + 4 < DN_NST) return (st + 4) * 8 + (i - 15) / 2;
    return -1;
}
constexpr int dn_young(int st) {
    int n = 0; bool seen = st + 1 == 1;
    const int need = st + 1;
    for (int s2 = 0; s2 <= st; ++s2)
        for (int i = 0; i < 18; ++i) {
            if (s2 == st && i >= 15) break;
            if (i == 0 && s2 + 2 < DN_NST) { if (seen) n += 4; if (s2 + 2 == need) { seen = true; n = 0; } }
            const int pc = dn_piece(s2, i);
            if (pc >= 0) { if (seen) n += 1; if ((pc >> 3) == need) { seen = true; n = 0; } }
        }
    return n;
}
DI void dn_ldb(bf16x8 (&b)[2][2], const char* hs, unsigned lo, int st) {
#pragma unroll
    for (int k = 0; k < 2; ++k) {
        asm volatile("global_load_dwordx4 %0, %1, %2 offset:%3" : "=v"(b[0][k]) : "v"(lo), "s"(hs + (size_t)st * 2048), "n"(k * 1024));
        asm volatile("global_load_dwordx4 %0, %1, %2 offset:%3" : "=v"(b[1][k]) : "v"(lo), "s"(hs + (size_t)FS * 1024 + (size_t)st * 2048), "n"(k * 1024));
    }
}
template <int N> DI void dn_wait(bf16x8 (&b)[2][2]) {
    asm volatile("s_waitcnt vmcnt(%4) lgkmcnt(0)\n\ts_barrier" : "+v"(b[0][0]), "+v"(b[0][1]), "+v"(b[1][0]), "+v"(b[1][1]) : "n"(N) : "memory");
}

DI void down_block(const char* wd, char* ws, char* lds, int bid) {
    const int tid = threadIdx.x, lane = tid & 63;
    const int w = __builtin_amdgcn_readfirstlane(tid >> 6);
    const int xcd = bid & 7, idx = bid >> 3;
    const int kq = xcd >> 1, tg = (xcd & 1) * 8 + (idx >> 2), dq = idx & 3;
    const int tb16 = 8 * tg + 2 * w, fs0 = 36 * kq;
    const unsigned lds0 = (unsigned)(uintptr_t)lds;
    unsigned voff[5], doff[5];
#pragma unroll
    for (int m = 0; m < 5; ++m) { const int i = min(5 * (w & 1) + m, 8), n = 64 * i + lane; voff[m] = (n / 18) * (D * 2) + (n % 18) * 16; doff[m] = (w >> 1) * DN_STEP + i * 1024; }
    const char* wsrc = wd + (size_t)(fs0 + (w >> 1)) * 32 * (D * 2) + dq * 288;
    const lds_cptr A = (lds_cptr)lds + (4 * (lane >> 4) + ((lane & 15) >> 2)) * 288 + (lane & 3) * 8;
    const char* hs = ws + WS_HF + ((size_t)tb16 * FS + fs0) * 1024;
    const unsigned lo = lane * 16;

    auto dma = [&](int st, int m) {
        const char* base = wsrc;
        asm volatile("" : "+s"(base));
        glds16(base + (size_t)st * 2 * 32 * (D * 2), voff[m], lds0 + (st & 3) * DN_SLOT + doff[m]);
    };
#pragma unroll
    for (int st = 0; st < 3; ++st)
#pragma unroll
        for (int m = 0; m < 5; ++m) dma(st, m);
    dma(3, 0); dma(3, 1);
    f32x4 acc[9][2];
    bf16x8 b[3][2][2];
    dn_ldb(b[0], hs, lo, 0);
    dn_ldb(b[1], hs, lo, 1);
    lds_cptr Alo = A, Ahi = A + 2 * DN_SLOT;
    asm volatile("" : "+v"(Alo), "+v"(Ahi));
    auto addr = [&](int st, int i) { return ((st & 2) ? Ahi : Alo) + (st & 1) * DN_SLOT + (i / 9) * DN_STEP + (i % 9) * 32; };
    dn_wait<4>(b[0]);
    bf16x4 flo[4], fhi[4];
#pragma unroll
    for (int i = 0; i < 3; ++i) { flo[i] = trread(addr(0, i)); fhi[i] = trread(addr(0, i) + 16 * 288); }
    static_for<0, DN_NST * 18>([&](auto nc) {
        constexpr int n = decltype(nc)::value, st = n / 18, i = n % 18, set = st % 3;
        if constexpr (i == 0 && st + 2 < DN_NST) dn_ldb(b[(st + 2) % 3], hs, lo, st + 2);
        if constexpr (i == 15 && st + 1 < DN_NST) dn_wait<dn_young(st)>(b[(st + 1) % 3]);
        if constexpr (dn_piece(st, i) >= 0) dma(dn_piece(st, i) >> 3, dn_piece(st, i) & 7);
        constexpr bool more = n + 3 < DN_NST * 18;
        constexpr int nst = (n + 3) / 18, ni = (n + 3) % 18;
        const bf16x8 fr = join(flo[n & 3], fhi[n & 3]);
        if constexpr (n < 9) AMF_NEW(acc[i % 9][0], fr, b[set][0][i / 9]); else AMF_ACC(acc[i % 9][0], fr, b[set][0][i / 9]);
        SBAR();
        if constexpr (more) flo[(n + 3) & 3] = trread(addr(nst, ni));
        SBAR();
        if constexpr (n < 9) AMF_NEW(acc[i % 9][1], fr, b[set][1][i / 9]); else AMF_ACC(acc[i % 9][1], fr, b[set][1][i / 9]);
        SBAR();
        if constexpr (more) fhi[(n + 3) & 3] = trread(addr(nst, ni) + 16 * 288);
        SBAR();
    });

    asm volatile("s_nop 7\n\ts_nop 3");
    SBAR();
    char* dst = ws + WS_PART + (((size_t)kq * NT16 + tb16) * 36 + 9 * dq) * PTILE;
#pragma unroll
    for (int nh = 0; nh < 2; ++nh)
#pragma unroll
        for (int i = 0; i < 9; ++i) gstore12(dst + (size_t)nh * 36 * PTILE + i * PTILE, lane * 12, pack24(acc[i][nh]));
}

constexpr int MIX_LDS = DN_LDS;
static_assert(MIX_LDS >= 3 * AT_STAGE && 2 * MIX_LDS <= 160 * 1024, "two workgroups per CU");
__global__ __launch_bounds__(256, 2) void k_mix(const char* wd, char* ws) {
    extern __shared__ __attribute__((aligned(16))) char lds[];

    const int b = blockIdx.x;
    if (b < 256) attn_block(ws, lds, b); else down_block(wd, ws, lds, b - 256);
}

constexpr int FN_A = 3 * KS * 1024, FN_LDS = FN_A + 8 * 3 * 1024;
DI void ldx1(bf16x8& d, const char* base, unsigned lo, int off) {
    asm volatile("global_load_dwordx4 %0, %1, %2 offset:%3" : "=v"(d) : "v"(lo), "s"(base), "n"(off));
}
template <int N> DI void waitx(bf16x8& a, bf16x8& b) { asm volatile("s_waitcnt vmcnt(%2)" : "+v"(a), "+v"(b) : "n"(N)); }
DI void fn_ldpx(u32x3 (&pq)[3][4], u32x2 (&xv)[3], const char* pp, unsigned l12, const char* xp, unsigned xo) {
#pragma unroll
    for (int t = 0; t < 3; ++t) {
#pragma unroll
        for (int k = 0; k < 4; ++k) asm volatile("global_load_dwordx3 %0, %1, %2 offset:%3" : "=v"(pq[t][k]) : "v"(l12), "s"(pp + (size_t)k * NT16 * 36 * PTILE), "n"(t * PTILE));
        asm volatile("global_load_dwordx2 %0, %1, %2 offset:%3" : "=v"(xv[t]) : "v"(xo), "s"(xp), "n"(t * 32));
    }
}
__global__ __launch_bounds__(512, 1) void k_final(const unsigned short* x, char* ws, unsigned short* out) {
    extern __shared__ __attribute__((aligned(16))) char lds[];

    const int tid = threadIdx.x, lane = tid & 63;
    const int w = __builtin_amdgcn_readfirstlane(tid >> 6);
    const int xcd = blockIdx.x & 7, idx = blockIdx.x >> 3;
    const int tg = 2 * xcd + idx / 12, dg = idx % 12, kh = w >> 2, tb16 = 8 * tg + 2 * (w & 3);
    const unsigned lds0 = (unsigned)(uintptr_t)lds, lo = lane * 16;

    {
        const char* src = ws + WS_WOP + (size_t)dg * FN_A;
#pragma unroll
        for (int i = 0; i < 7; ++i) { const int n = min(w * 7 + i, 3 * KS - 1); glds16(src + n * 1024, lo, lds0 + n * 1024); }
    }
    const char* os = ws + WS_OF + ((size_t)tb16 * KS + 9 * kh) * 1024;
    bf16x8 b[2][9];
    auto ldo = [&](int s) { ldx1(b[0][s], os + (s >> 2) * 4096, lo, (s & 3) * 1024); ldx1(b[1][s], os + KS * 1024 + (s >> 2) * 4096, lo, (s & 3) * 1024); };
#pragma unroll
    for (int s = 0; s < 4; ++s) ldo(s);

    asm volatile("s_waitcnt vmcnt(8)\n\ts_barrier" ::: "memory");

    const int tok = (tb16 + kh) * 16 + (lane & 15);
    const char* xp = (const char*)(x + (size_t)(tb16 + kh) * 16 * D + dg * 48);
    const unsigned xo2 = ((lane & 15) * D + 4 * (lane >> 4)) * 2;
    u32x2 xv[3];
    u32x3 pq[3][4];
    const char* pp = ws + WS_PART + ((size_t)(tb16 + kh) * 36 + 3 * dg) * PTILE;
    const lds_cptr A = (lds_cptr)lds + lo + 9 * kh * 1024;
    f32x4 acc[2][3] = {};
    static_for<0, 9>([&](auto sc) {
        constexpr int s = decltype(sc)::value;
        bf16x8 a[3];
#pragma unroll
        for (int t = 0; t < 3; ++t) a[t] = *(const __attribute__((address_space(3))) bf16x8*)(A + (t * KS + s) * 1024);
        if constexpr (s + 4 < 9) ldo(s + 4);
        if constexpr (s == 4) fn_ldpx(pq, xv, pp, lane * 12, xp, xo2);
        waitx<2 * ((s + 4 < 9 ? s + 4 : 8) - s) + (s >= 4 ? 15 : 0)>(b[0][s], b[1][s]);
#pragma unroll
        for (int t = 0; t < 3; ++t) {
            acc[0][t] = MFMA32K(a[t], b[0][s], acc[0][t]);
            acc[1][t] = MFMA32K(a[t], b[1][s], acc[1][t]);
        }
        SBAR();
    });

    typedef __attribute__((address_space(3))) f32x4* l128;
    const lds_ptr xo = (lds_ptr)lds + FN_A + lo;
#pragma unroll
    for (int t = 0; t < 3; ++t) *(l128)(xo + (w * 3 + t) * 1024) = kh ? acc[0][t] : acc[1][t];
    asm volatile("s_waitcnt lgkmcnt(0)\n\ts_barrier" ::: "memory");
    f32x4 sum[3];
#pragma unroll
    for (int t = 0; t < 3; ++t) {
        const f32x4 other = *(l128)(xo + ((w ^ 4) * 3 + t) * 1024);
        sum[t] = kh ? other + acc[1][t] : acc[0][t] + other;
    }
    asm volatile("s_waitcnt vmcnt(0)" : "+v"(xv[0]), "+v"(xv[1]), "+v"(xv[2]));
#pragma unroll
    for (int t = 0; t < 3; ++t) {
        asm volatile("" : "+v"(pq[t][0]), "+v"(pq[t][1]), "+v"(pq[t][2]), "+v"(pq[t][3]));
        const f32x4 ffn = (unpack24(pq[t][0]) + unpack24(pq[t][1])) + (unpack24(pq[t][2]) + unpack24(pq[t][3]));
        float o[4];
#pragma unroll
        for (int c = 0; c < 4; ++c) {
            const float f = rbf(ffn[c]);
            const float g = rbf(f * __builtin_amdgcn_rcpf(1.0f + __builtin_amdgcn_exp2f(f * -LOG2E)));
            const float xx = (c & 1) ? bf_hi(xv[t][c >> 1]) : bf_lo(xv[t][c >> 1]);
            o[c] = xx + rbf(rbf(sum[t][c]) * g);
        }
        *(u32x2*)(out + (size_t)tok * D + dg * 48 + 16 * t + 4 * (lane >> 4)) = (u32x2){cvtpk(o[0], o[1]), cvtpk(o[2], o[3])};
    }
}

extern "C" void kernel_launch(void* const* d_in, const int* in_sizes, int n_in, void* d_out, int out_size, void* d_ws, size_t ws_size, hipStream_t stream) {
    const unsigned short* x = (const unsigned short*)d_in[0];
    const unsigned short* wqkv = (const unsigned short*)d_in[1];
    const unsigned short* wo = (const unsigned short*)d_in[2];
    const char* wg = (const char*)d_in[4];
    const char* wu = (const char*)d_in[5];
    const char* wd = (const char*)d_in[6];
    char* ws = (char*)d_ws;
    (void)hipFuncSetAttribute((const void*)k_gemm1, hipFuncAttributeMaxDynamicSharedMemorySize, 4 * G1_SLOT);
    (void)hipFuncSetAttribute((const void*)k_mix, hipFuncAttributeMaxDynamicSharedMemorySize, MIX_LDS);
    (void)hipFuncSetAttribute((const void*)k_final, hipFuncAttributeMaxDynamicSharedMemorySize, FN_LDS);
    k_prep<<<256, 1024, 0, stream>>>(x, wqkv, wo, ws);
    k_gemm1<<<256, 512, 4 * G1_SLOT, stream>>>(wg, wu, ws);
    k_mix<<<512, 256, MIX_LDS, stream>>>(wd, ws);
    k_final<<<192, 512, FN_LDS, stream>>>(x, ws, (unsigned short*)d_out);
}
```
